```python
import torch

def _softmax(x, axis=-1):
    return torch.softmax(x, dim=axis)
def _gelu(x):
    return torch.nn.functional.gelu(x, approximate="tanh")
def _silu(x):
    return torch.nn.functional.silu(x)
def _sigmoid(x):
    return torch.sigmoid(x)
def _layernorm(x, axis=-1, eps=1e-5):
    m = x.mean(dim=axis, keepdim=True)
    v = x.var(dim=axis, keepdim=True, unbiased=False)
    return (x - m) / torch.sqrt(v + eps)
def _scatter_add(tgt, idx, src, axis=0):
    return tgt.clone().index_add_(axis, idx, src)
def _segment_sum(src, seg, n):
    out = torch.zeros((n,) + src.shape[1:], dtype=src.dtype)
    return out.index_add_(0, seg, src)
def _pad(x, pw):
    flat = tuple(v for pair in reversed(pw) for v in pair)
    return torch.nn.functional.pad(x, flat)

kernel_name = 'standalone__gqa_block_cached__near_ridge'

def setup_inputs():

    _w = (lambda a: (lambda t: t.to(torch.bfloat16) if t.is_floating_point() else t)(torch.as_tensor(a)))
    B, Sq, D = 16, 48, 512
    Hq, Hkv, Sk, Dh = 32, 8, 512, 128
    assert Hq % Hkv == 0, (Hq, Hkv)
    return {
        "x": _w((lambda *s: torch.randn(*s, dtype=torch.bfloat16) * 0.8)(B, Sq, D)),
        "k_cache": _w((lambda *s: torch.randn(*s, dtype=torch.bfloat16) * 0.8)(B, Hkv, Sk, Dh)),
        "v_cache": _w((lambda *s: torch.randn(*s, dtype=torch.bfloat16) * 0.05)(B, Hkv, Sk, Dh)),
        "wq": _w((lambda *s: torch.randn(*s, dtype=torch.bfloat16) * 0.8)(D, Hq * Dh) * (D ** -0.5)),
        "wo": _w((lambda *s: torch.randn(*s, dtype=torch.bfloat16) * 0.05)(Hq * Dh, D) * ((Hq * Dh) ** -0.5)),
    }

def reference(x, k_cache, v_cache, wq, wo):
    B, Sq, D = x.shape
    Hq, Dh = 32, 128
    Hkv = k_cache.shape[1]
    rep = Hq // Hkv
    q = (lambda x, *d: x.permute(*d))((x @ wq).reshape(B, Sq, Hq, Dh), 0, 2, 1, 3)
    k = (lambda x, n, axis: torch.repeat_interleave(x, n, dim=axis))(k_cache, rep, axis=1)
    v = (lambda x, n, axis: torch.repeat_interleave(x, n, dim=axis))(v_cache, rep, axis=1)
    scores = torch.einsum("bhqd,bhkd->bhqk", q, k) * (Dh ** -0.5)
    p = _softmax(scores, axis=-1)
    o = torch.einsum("bhqk,bhkd->bhqd", p, v)
    return (lambda x, *d: x.permute(*d))(o, 0, 2, 1, 3).reshape(B, Sq, Hq * Dh) @ wo
```

```cpp
#include <hip/hip_runtime.h>
#include <hip/hip_bf16.h>
#include <stdint.h>

typedef __bf16 bf16x8 __attribute__((ext_vector_type(8)));
typedef __bf16 bf16x4 __attribute__((ext_vector_type(4)));
typedef __bf16 bf16x2 __attribute__((ext_vector_type(2)));
typedef float f32x4 __attribute__((ext_vector_type(4)));
typedef int i32x4 __attribute__((ext_vector_type(4)));
typedef unsigned u32x4 __attribute__((ext_vector_type(4)));
typedef unsigned u32x2 __attribute__((ext_vector_type(2)));
#define LDS_AS __attribute__((address_space(3)))
typedef LDS_AS char* ldsp;

constexpr int B_ = 16, SQ = 48, DM = 512, HQ = 32, HKV = 8, SK = 512, DH = 128;
constexpr float SCL2E = 0.08838834764831845f * 1.4426950408889634f;

constexpr int L_SLOT = 32768;
constexpr int L_RING = 32768;
constexpr int L_X = 114688;
constexpr int L_Q = 131072;
constexpr int L_STAT = 155648;
constexpr int L_P = 65536;
constexpr int L_LSUM = 0;
constexpr int L_O = 8192;
constexpr int L_WO = 32768;
constexpr int LDS_BYTES = 163840;
constexpr int NRING = 5;

#define BAR() asm volatile("s_waitcnt lgkmcnt(0)\n\ts_barrier" ::: "memory")
template <int N> __device__ __forceinline__ void waitv() { asm volatile("s_waitcnt vmcnt(%0)" ::"i"(N) : "memory"); }

__device__ __forceinline__ void dma16(unsigned ldsaddr, unsigned voff, i32x4 srd, unsigned soff) {
  asm volatile("s_mov_b32 m0, %0\n\ts_nop 0\n\tbuffer_load_dwordx4 %1, %2, %3 offen lds" ::"s"(ldsaddr), "v"(voff), "s"(srd), "s"(soff) : "memory");
}
__device__ __forceinline__ i32x4 make_srd(const void* p, unsigned bytes) {
  unsigned long long a = (unsigned long long)p;
  i32x4 s;
  s[0] = (int)(unsigned)a; s[1] = (int)((unsigned)(a >> 32) & 0xffffu); s[2] = (int)bytes; s[3] = 0x00020000;
  return s;
}
__device__ __forceinline__ bf16x8 lds_rd128(ldsp p) { return *(LDS_AS bf16x8*)p; }
__device__ __forceinline__ bf16x8 lds_tr2(ldsp p0, ldsp p1) {
  bf16x4 a = __builtin_amdgcn_ds_read_tr16_b64_v4bf16((LDS_AS bf16x4*)p0);
  bf16x4 b = __builtin_amdgcn_ds_read_tr16_b64_v4bf16((LDS_AS bf16x4*)p1);
  return __builtin_shufflevector(a, b, 0, 1, 2, 3, 4, 5, 6, 7);
}
__device__ __forceinline__ f32x4 mfma(bf16x8 a, bf16x8 b, f32x4 c) { return __builtin_amdgcn_mfma_f32_16x16x32_bf16(a, b, c, 0, 0, 0); }
__device__ __forceinline__ bf16x4 cvt4(f32x4 v) {
  bf16x4 r; r[0] = (__bf16)v[0]; r[1] = (__bf16)v[1]; r[2] = (__bf16)v[2]; r[3] = (__bf16)v[3]; return r;
}
__device__ __forceinline__ bf16x8 cvt8(f32x4 a, f32x4 b) { return __builtin_shufflevector(cvt4(a), cvt4(b), 0, 1, 2, 3, 4, 5, 6, 7); }
__device__ __forceinline__ float xmax_g(float v) {
  auto r = __builtin_amdgcn_permlane16_swap(__float_as_uint(v), __float_as_uint(v), false, false);
  v = fmaxf(__uint_as_float(r[0]), __uint_as_float(r[1]));
  auto r2 = __builtin_amdgcn_permlane32_swap(__float_as_uint(v), __float_as_uint(v), false, false);
  return fmaxf(__uint_as_float(r2[0]), __uint_as_float(r2[1]));
}
__device__ __forceinline__ float xsum_g(float v) {
  auto r = __builtin_amdgcn_permlane16_swap(__float_as_uint(v), __float_as_uint(v), false, false);
  v = __uint_as_float(r[0]) + __uint_as_float(r[1]);
  auto r2 = __builtin_amdgcn_permlane32_swap(__float_as_uint(v), __float_as_uint(v), false, false);
  return __uint_as_float(r2[0]) + __uint_as_float(r2[1]);
}

__global__ __launch_bounds__(512, 2) void gqa_fused(const __bf16* __restrict__ x, const __bf16* __restrict__ kc, const __bf16* __restrict__ vc,
                                                    const __bf16* __restrict__ wq, const __bf16* __restrict__ wo, float* __restrict__ part) {
  extern __shared__ __attribute__((aligned(16))) char smem_raw[];
  ldsp sm = (ldsp)smem_raw;
  const unsigned smbase = (unsigned)(uintptr_t)sm;
  const int tid = threadIdx.x, l = tid & 63, c = l & 15, g = l >> 4, qq = c >> 2, pp = l & 3;
  const int w = __builtin_amdgcn_readfirstlane(tid >> 6);
  const int bid = blockIdx.x, kvh = bid & 7, jj = bid >> 3, b = jj >> 1, hp = jj & 1;
  const int n0 = kvh * 512 + hp * 256;

  u32x4 kreg[4][8], vreg[4][8];
  f32x4 qacc[4][3];

  if (w < 4) {
    const i32x4 srd_x = make_srd(x + (size_t)b * SQ * DM, SQ * DM * 2);
    const i32x4 srd_w = make_srd(wq, DM * HQ * DH * 2);
#pragma unroll
    for (int i = 0; i < 12; ++i) {
      const int r = w * 12 + i;
      dma16(smbase + L_X + r * 1024, (unsigned)((l ^ (r & 15)) * 16), srd_x, (unsigned)(r * 1024));
    }
    const unsigned ring = smbase + L_RING + w * (NRING * 4096);
    const unsigned voff_e = (unsigned)((l >> 3) * 8192 + (((l & 7) ^ (((l >> 4) & 1) << 1)) * 16));
    const unsigned voff_o = voff_e ^ 64u;
    const unsigned scol = (unsigned)((n0 + 64 * w) * 2);
#define WQ_ISSUE(s)                                                                                                        \
  do {                                                                                                                     \
    _Pragma("unroll") for (int i_ = 0; i_ < 4; ++i_)                                                                       \
        dma16(ring + ((s) % NRING) * 4096 + i_ * 1024, (i_ & 1) ? voff_o : voff_e, srd_w, scol + (unsigned)((32 * (s) + 8 * i_) * 8192)); \
  } while (0)
    WQ_ISSUE(0); WQ_ISSUE(1); WQ_ISSUE(2); WQ_ISSUE(3);
    waitv<16>();
    BAR();
    const int fA = ((qq >> 1) & 1) | ((g & 1) << 1);
    ldsp wqa = sm + L_RING + w * (NRING * 4096) + (8 * g + qq) * 128 + 8 * pp;
    ldsp xa = sm + L_X + c * 1024;
#pragma unroll
    for (int t = 0; t < 4; ++t)
#pragma unroll
      for (int qt = 0; qt < 3; ++qt) qacc[t][qt] = f32x4{0.f, 0.f, 0.f, 0.f};
    bf16x8 fw[2][4], fx[2][3];
#define A_READ(s, buf)                                                                                          \
  do {                                                                                                          \
    _Pragma("unroll") for (int t_ = 0; t_ < 4; ++t_) {                                                          \
      ldsp p_ = wqa + ((s) % NRING) * 4096 + ((t_ ^ fA) << 5);                                                  \
      fw[buf][t_] = lds_tr2(p_, p_ + 512);                                                                      \
    }                                                                                                           \
    _Pragma("unroll") for (int q_ = 0; q_ < 3; ++q_)                                                            \
        fx[buf][q_] = lds_rd128(xa + q_ * 16384 + ((((4 * (s)) | g) ^ c) << 4));                                \
  } while (0)
    waitv<12>();
    A_READ(0, 0);
#pragma unroll
    for (int s = 0; s < 16; ++s) {
      if (s + 4 < 16) WQ_ISSUE(s + 4);
      if (s + 1 < 16) {
        constexpr int dummy = 0; (void)dummy;
        const int rem = ((s + 4 < 16) ? (s + 4) : 15) - (s + 1);
        if (rem == 3) waitv<12>(); else if (rem == 2) waitv<8>(); else if (rem == 1) waitv<4>(); else waitv<0>();
        A_READ(s + 1, (s + 1) & 1);
      }
#pragma unroll
      for (int t = 0; t < 4; ++t)
#pragma unroll
        for (int qt = 0; qt < 3; ++qt) qacc[t][qt] = mfma(fw[s & 1][t], fx[s & 1][qt], qacc[t][qt]);
    }
    BAR();
    {
      const int h = w >> 1;
#pragma unroll
      for (int t = 0; t < 4; ++t)
#pragma unroll
        for (int qt = 0; qt < 3; ++qt) {
          const int c16 = 8 * (w & 1) + 2 * t + (g >> 1);
          *(LDS_AS bf16x4*)(sm + L_Q + (48 * h + 16 * qt + c) * 256 + ((c16 ^ c) << 4) + 8 * (g & 1)) = cvt4(qacc[t][qt]);
        }
    }
  } else {
    const int v = w - 4;
    const u32x4* kp = (const u32x4*)(kc + ((size_t)(b * HKV + kvh) * SK + 128 * v) * DH) + l;
    const u32x4* vp = (const u32x4*)(vc + ((size_t)(b * HKV + kvh) * SK + 128 * v) * DH) + l;
#pragma unroll
    for (int j = 0; j < 4; ++j)
#pragma unroll
      for (int i = 0; i < 8; ++i) kreg[j][i] = __builtin_nontemporal_load(kp + (j * 8 + i) * 64);
#pragma unroll
    for (int j = 0; j < 3; ++j)
#pragma unroll
      for (int i = 0; i < 8; ++i) vreg[j][i] = __builtin_nontemporal_load(vp + (j * 8 + i) * 64);
    BAR();
#pragma unroll
    for (int i = 0; i < 8; ++i)
      *(LDS_AS u32x4*)(sm + (32 * v + 4 * i + g) * 256 + (((c ^ g) ^ (4 * (i & 3))) << 4)) = kreg[0][i];
    BAR();
#pragma unroll
    for (int i = 0; i < 8; ++i)
      *(LDS_AS u32x4*)(sm + L_SLOT + (32 * v + 4 * i + g) * 256 + (((c ^ g) ^ (4 * (i & 3))) << 4)) = kreg[1][i];
#pragma unroll
    for (int i = 0; i < 8; ++i)
      *(LDS_AS u32x4*)(sm + 2 * L_SLOT + (32 * v + 4 * i + g) * 256 + (((c ^ g) ^ (4 * (i & 3))) << 4)) = kreg[2][i];
#pragma unroll
    for (int i = 0; i < 8; ++i) vreg[3][i] = __builtin_nontemporal_load(vp + (3 * 8 + i) * 64);
  }
  BAR();

#define K_DUMP(j)                                                                                                            \
  do {                                                                                                                       \
    if (w >= 4) {                                                                                                            \
      _Pragma("unroll") for (int i_ = 0; i_ < 8; ++i_)                                                                       \
          *(LDS_AS u32x4*)(sm + (j) * L_SLOT + (32 * (w - 4) + 4 * i_ + g) * 256 + (((c ^ g) ^ (4 * (i_ & 3))) << 4)) = kreg[j][i_]; \
    }                                                                                                                        \
  } while (0)
#define V_DUMP(j)                                                                                                            \
  do {                                                                                                                       \
    if (w >= 4) {                                                                                                            \
      _Pragma("unroll") for (int i_ = 0; i_ < 8; ++i_)                                                                       \
          *(LDS_AS u32x4*)(sm + ((j) & 1) * L_SLOT + (32 * (w - 4) + 4 * i_ + g) * 256 + (((c ^ (2 * g)) ^ (8 * (i_ & 1))) << 4)) = vreg[j][i_]; \
    }                                                                                                                        \
  } while (0)

  const int h = w >> 2, kq = w & 3;
  f32x4 sacc[4][2][3];
  {
    bf16x8 qf[4][3];
#pragma unroll
    for (int s = 0; s < 4; ++s)
#pragma unroll
      for (int qt = 0; qt < 3; ++qt) qf[s][qt] = lds_rd128(sm + L_Q + (48 * h + 16 * qt + c) * 256 + ((((4 * s) | g) ^ c) << 4));
    ldsp ka = sm + (32 * kq + c) * 256;
#pragma unroll
    for (int j = 0; j < 4; ++j) {
      if (j == 0) K_DUMP(3);
      if (j == 1) V_DUMP(0);
      if (j == 2) V_DUMP(1);
#pragma unroll
      for (int kt = 0; kt < 2; ++kt) {
#pragma unroll
        for (int qt = 0; qt < 3; ++qt) sacc[j][kt][qt] = f32x4{0.f, 0.f, 0.f, 0.f};
#pragma unroll
        for (int s = 0; s < 4; ++s) {
          bf16x8 kf = lds_rd128(ka + j * L_SLOT + kt * 4096 + ((((4 * s) | g) ^ c) << 4));
#pragma unroll
          for (int qt = 0; qt < 3; ++qt) sacc[j][kt][qt] = mfma(kf, qf[s][qt], sacc[j][kt][qt]);
        }
      }
      if (j < 3) BAR();
    }
  }
  float mx[3];
#pragma unroll
  for (int qt = 0; qt < 3; ++qt) {
    float m = sacc[0][0][qt][0];
#pragma unroll
    for (int j = 0; j < 4; ++j)
#pragma unroll
      for (int kt = 0; kt < 2; ++kt)
#pragma unroll
        for (int r = 0; r < 4; ++r) m = fmaxf(m, sacc[j][kt][qt][r]);
    m = xmax_g(m);
    if (g == 0) *(LDS_AS float*)(sm + L_STAT + ((h * 4 + kq) * 48 + 16 * qt + c) * 4) = m;
  }
  BAR();
#pragma unroll
  for (int qt = 0; qt < 3; ++qt) {
    float m = *(LDS_AS float*)(sm + L_STAT + ((h * 4 + 0) * 48 + 16 * qt + c) * 4);
#pragma unroll
    for (int k = 1; k < 4; ++k) m = fmaxf(m, *(LDS_AS float*)(sm + L_STAT + ((h * 4 + k) * 48 + 16 * qt + c) * 4));
    mx[qt] = m * SCL2E;
  }
  BAR();
  float lsum[3];
#pragma unroll
  for (int qt = 0; qt < 3; ++qt) {
    float ls = 0.f;
#pragma unroll
    for (int j = 0; j < 4; ++j) {
#pragma unroll
      for (int kt = 0; kt < 2; ++kt)
#pragma unroll
        for (int r = 0; r < 4; ++r) {
          float p = __builtin_amdgcn_exp2f(fmaf(sacc[j][kt][qt][r], SCL2E, -mx[qt]));
          sacc[j][kt][qt][r] = p;
          ls += p;
        }
      *(LDS_AS bf16x8*)(sm + L_P + ((w * 4 + j) * 3 + qt) * 1024 + l * 16) = cvt8(sacc[j][0][qt], sacc[j][1][qt]);
    }
    lsum[qt] = xsum_g(ls);
  }
  u32x4 wreg[8][4];
  const u32x4* wop = (const u32x4*)(wo + (size_t)(n0 + (l >> 3)) * DM + 64 * w) + (l & 7);
#define WO_LOAD(u)                                                                                   \
  do {                                                                                               \
    _Pragma("unroll") for (int i_ = 0; i_ < 4; ++i_) wreg[u][i_] = wop[(32 * (u) + 8 * i_) * (DM / 8)]; \
  } while (0)
  WO_LOAD(0); WO_LOAD(1); WO_LOAD(2); WO_LOAD(3);
  BAR();

  const int dq = kq;
  f32x4 oacc[2][3];
#pragma unroll
  for (int dd = 0; dd < 2; ++dd)
#pragma unroll
    for (int qt = 0; qt < 3; ++qt) oacc[dd][qt] = f32x4{0.f, 0.f, 0.f, 0.f};
  {
    const int fV = 4 * (g & 1) + qq;
    ldsp va = sm + (4 * g + qq) * 256 + 8 * pp;
#pragma unroll
    for (int j = 0; j < 4; ++j) {
      if (j == 1) { V_DUMP(2); WO_LOAD(4); WO_LOAD(5); }
      if (j == 2) { V_DUMP(3); WO_LOAD(6); WO_LOAD(7); }
      if (j == 3) {
        if (g == 0) {
#pragma unroll
          for (int qt = 0; qt < 3; ++qt) *(LDS_AS float*)(sm + L_LSUM + (w * 48 + 16 * qt + c) * 4) = lsum[qt];
        }
      }
#pragma unroll
      for (int k4 = 0; k4 < 4; ++k4) {
        bf16x8 pf[3], vf[2];
#pragma unroll
        for (int qt = 0; qt < 3; ++qt) pf[qt] = lds_rd128(sm + L_P + (((h * 4 + k4) * 4 + j) * 3 + qt) * 1024 + l * 16);
#pragma unroll
        for (int dd = 0; dd < 2; ++dd) {
          ldsp p = va + (j & 1) * L_SLOT + k4 * 8192 + ((((2 * dq) ^ fV) ^ dd) << 5);
          vf[dd] = lds_tr2(p, p + 4096);
        }
#pragma unroll
        for (int dd = 0; dd < 2; ++dd)
#pragma unroll
          for (int qt = 0; qt < 3; ++qt) oacc[dd][qt] = mfma(vf[dd], pf[qt], oacc[dd][qt]);
      }
      BAR();
    }
  }
#pragma unroll
  for (int qt = 0; qt < 3; ++qt) {
    float ls = 0.f;
#pragma unroll
    for (int k = 0; k < 4; ++k) ls += *(LDS_AS float*)(sm + L_LSUM + ((h * 4 + k) * 48 + 16 * qt + c) * 4);
    const float inv = 1.0f / ls;
    *(LDS_AS bf16x8*)(sm + L_O + (w * 3 + qt) * 1024 + l * 16) = cvt8(oacc[0][qt] * inv, oacc[1][qt] * inv);
  }
  BAR();

  f32x4 cacc[4][3];
#pragma unroll
  for (int t = 0; t < 4; ++t)
#pragma unroll
    for (int qt = 0; qt < 3; ++qt) cacc[t][qt] = f32x4{0.f, 0.f, 0.f, 0.f};
  {
    ldsp wst = sm + L_WO + w * 16384 + (l >> 3) * 128 + (((l & 7) ^ (g << 1)) << 4);
    const int fC = 2 * (g & 1) + (qq >> 1);
    ldsp wra = sm + L_WO + w * 16384 + (4 * g + qq) * 128 + 8 * pp;
#pragma unroll
    for (int u = 0; u < 8; ++u) {
#pragma unroll
      for (int i = 0; i < 4; ++i) *(LDS_AS u32x4*)(wst + (u & 3) * 4096 + i * 1024) = wreg[u][i];
      bf16x8 of[3], wf[4];
#pragma unroll
      for (int qt = 0; qt < 3; ++qt) of[qt] = lds_rd128(sm + L_O + (u * 3 + qt) * 1024 + l * 16);
#pragma unroll
      for (int t = 0; t < 4; ++t) {
        ldsp p = wra + (u & 3) * 4096 + ((t ^ fC) << 5);
        wf[t] = lds_tr2(p, p + 2048);
      }
#pragma unroll
      for (int t = 0; t < 4; ++t)
#pragma unroll
        for (int qt = 0; qt < 3; ++qt) cacc[t][qt] = mfma(wf[t], of[qt], cacc[t][qt]);
    }
  }
  f32x4* pp4 = (f32x4*)part + ((size_t)(b * 16 + kvh * 2 + hp) * 8 + w) * 12 * 64 + l;
#pragma unroll
  for (int t = 0; t < 4; ++t)
#pragma unroll
    for (int qt = 0; qt < 3; ++qt) pp4[(t * 3 + qt) * 64] = cacc[t][qt];
}

__global__ __launch_bounds__(384) void gqa_reduce(const float* __restrict__ part, __bf16* __restrict__ out) {
  const int idx = blockIdx.x * 384 + threadIdx.x;
  const int b = idx / 6144, rem = idx - b * 6144;
  const f32x4* p = (const f32x4*)part + (size_t)b * 16 * 6144 + rem;
  f32x4 v[16];
#pragma unroll
  for (int s = 0; s < 16; ++s) v[s] = p[(size_t)s * 6144];
  f32x4 a = v[0];
#pragma unroll
  for (int s = 1; s < 16; ++s) a += v[s];
  const int lane = rem & 63, tq = rem >> 6, qt = tq % 3, wt = tq / 3;
  const int q = 16 * qt + (lane & 15), n = 16 * wt + 4 * (lane >> 4);
  *(bf16x4*)(out + ((size_t)b * SQ + q) * DM + n) = cvt4(a);
}

extern "C" void kernel_launch(void* const* d_in, const int* in_sizes, int n_in, void* d_out, int out_size, void* d_ws, size_t ws_size,
                              hipStream_t stream) {
  const __bf16* x = (const __bf16*)d_in[0];
  const __bf16* kc = (const __bf16*)d_in[1];
  const __bf16* vc = (const __bf16*)d_in[2];
  const __bf16* wq = (const __bf16*)d_in[3];
  const __bf16* wo = (const __bf16*)d_in[4];
  float* part = (float*)d_ws;
  (void)hipFuncSetAttribute((const void*)gqa_fused, hipFuncAttributeMaxDynamicSharedMemorySize, LDS_BYTES);
  hipLaunchKernelGGL(gqa_fused, dim3(256), dim3(512), LDS_BYTES, stream, x, kc, vc, wq, wo, part);
  hipLaunchKernelGGL(gqa_reduce, dim3(256), dim3(384), 0, stream, (const float*)part, (__bf16*)d_out);
}
```

```cpp
#include <hip/hip_runtime.h>
#include <hip/hip_bf16.h>
#include <stdint.h>

typedef __bf16 bf16x8 __attribute__((ext_vector_type(8)));
typedef __bf16 bf16x4 __attribute__((ext_vector_type(4)));
typedef __bf16 bf16x2 __attribute__((ext_vector_type(2)));
typedef float f32x4 __attribute__((ext_vector_type(4)));
typedef int i32x4 __attribute__((ext_vector_type(4)));
typedef unsigned u32x4 __attribute__((ext_vector_type(4)));
typedef unsigned u32x2 __attribute__((ext_vector_type(2)));
#define LDS_AS __attribute__((address_space(3)))
typedef LDS_AS char* ldsp;

constexpr int B_ = 16, SQ = 48, DM = 512, HQ = 32, HKV = 8, SK = 512, DH = 128;
constexpr float SCL2E = 0.08838834764831845f * 1.4426950408889634f;

constexpr int L_SLOT = 32768;
constexpr int L_RING = 32768;
constexpr int L_X = 114688;
constexpr int L_Q = 131072;
constexpr int L_STAT = 155648;
constexpr int L_P = 65536;
constexpr int L_LSUM = 0;
constexpr int L_O = 8192;
constexpr int L_WO = 32768;
constexpr int LDS_BYTES = 163840;
constexpr int NRING = 5;
#define KVD 8

#define BAR() asm volatile("s_waitcnt lgkmcnt(0)\n\ts_barrier" ::: "memory")
#define STAMP(i)
template <int N> __device__ __forceinline__ void waitv() { asm volatile("s_waitcnt vmcnt(%0)" ::"i"(N) : "memory"); }

__device__ __forceinline__ void dma16(unsigned ldsaddr, unsigned voff, i32x4 srd, unsigned soff) {
  asm volatile("s_mov_b32 m0, %0\n\ts_nop 0\n\tbuffer_load_dwordx4 %1, %2, %3 offen lds" ::"s"(ldsaddr), "v"(voff), "s"(srd), "s"(soff) : "memory");
}
__device__ __forceinline__ i32x4 make_srd(const void* p, unsigned bytes) {
  unsigned long long a = (unsigned long long)p;
  i32x4 s;
  s[0] = (int)(unsigned)a; s[1] = (int)((unsigned)(a >> 32) & 0xffffu); s[2] = (int)bytes; s[3] = 0x00020000;
  return s;
}
__device__ __forceinline__ bf16x8 lds_rd128(ldsp p) { return *(LDS_AS bf16x8*)p; }
__device__ __forceinline__ bf16x8 lds_tr2(ldsp p0, ldsp p1) {
  bf16x4 a = __builtin_amdgcn_ds_read_tr16_b64_v4bf16((LDS_AS bf16x4*)p0);
  bf16x4 b = __builtin_amdgcn_ds_read_tr16_b64_v4bf16((LDS_AS bf16x4*)p1);
  return __builtin_shufflevector(a, b, 0, 1, 2, 3, 4, 5, 6, 7);
}
__device__ __forceinline__ f32x4 mfma(bf16x8 a, bf16x8 b, f32x4 c) { return __builtin_amdgcn_mfma_f32_16x16x32_bf16(a, b, c, 0, 0, 0); }
__device__ __forceinline__ bf16x4 cvt4(f32x4 v) {
  bf16x4 r; r[0] = (__bf16)v[0]; r[1] = (__bf16)v[1]; r[2] = (__bf16)v[2]; r[3] = (__bf16)v[3]; return r;
}
__device__ __forceinline__ bf16x8 cvt8(f32x4 a, f32x4 b) { return __builtin_shufflevector(cvt4(a), cvt4(b), 0, 1, 2, 3, 4, 5, 6, 7); }
__device__ __forceinline__ float xmax_g(float v) {
  auto r = __builtin_amdgcn_permlane16_swap(__float_as_uint(v), __float_as_uint(v), false, false);
  v = fmaxf(__uint_as_float(r[0]), __uint_as_float(r[1]));
  auto r2 = __builtin_amdgcn_permlane32_swap(__float_as_uint(v), __float_as_uint(v), false, false);
  return fmaxf(__uint_as_float(r2[0]), __uint_as_float(r2[1]));
}
__device__ __forceinline__ float xsum_g(float v) {
  auto r = __builtin_amdgcn_permlane16_swap(__float_as_uint(v), __float_as_uint(v), false, false);
  v = __uint_as_float(r[0]) + __uint_as_float(r[1]);
  auto r2 = __builtin_amdgcn_permlane32_swap(__float_as_uint(v), __float_as_uint(v), false, false);
  return __uint_as_float(r2[0]) + __uint_as_float(r2[1]);
}

__global__ __launch_bounds__(512, 2) void gqa_fused(const __bf16* __restrict__ x, const __bf16* __restrict__ kc, const __bf16* __restrict__ vc,
                                                    const __bf16* __restrict__ wq, const __bf16* __restrict__ wo, float* __restrict__ part) {
  extern __shared__ __attribute__((aligned(16))) char smem_raw[];
  ldsp sm = (ldsp)smem_raw;
  const unsigned smbase = (unsigned)(uintptr_t)sm;
  const int tid = threadIdx.x, l = tid & 63, c = l & 15, g = l >> 4, qq = c >> 2, pp = l & 3;
  const int w = __builtin_amdgcn_readfirstlane(tid >> 6);
  const int bid = blockIdx.x, kvh = bid & 7, jj = bid >> 3, b = jj >> 1, hp = jj & 1;
  STAMP(0);
  const int n0 = kvh * 512 + hp * 256;

  u32x4 kreg[4][8], vreg[4][8];
  f32x4 qacc[4][3];

  if (w < 4) {
    const i32x4 srd_x = make_srd(x + (size_t)b * SQ * DM, SQ * DM * 2);
    const i32x4 srd_w = make_srd(wq, DM * HQ * DH * 2);
#pragma unroll
    for (int i = 0; i < 12; ++i) {
      const int r = w * 12 + i;
      dma16(smbase + L_X + r * 1024, (unsigned)((l ^ (r & 15)) * 16), srd_x, (unsigned)(r * 1024));
    }
    const unsigned ring = smbase + L_RING + w * (NRING * 4096);
    const unsigned voff_e = (unsigned)((l >> 3) * 8192 + (((l & 7) ^ (((l >> 4) & 1) << 1)) * 16));
    const unsigned voff_o = voff_e ^ 64u;
    const unsigned scol = (unsigned)((n0 + 64 * w) * 2);
#define WQ_ISSUE(s)                                                                                                        \
  do {                                                                                                                     \
    _Pragma("unroll") for (int i_ = 0; i_ < 4; ++i_)                                                                       \
        dma16(ring + ((s) % NRING) * 4096 + i_ * 1024, (i_ & 1) ? voff_o : voff_e, srd_w, scol + (unsigned)((32 * (((s) + b) & 15) + 8 * i_) * 8192)); \
  } while (0)
    WQ_ISSUE(0); WQ_ISSUE(1); WQ_ISSUE(2); WQ_ISSUE(3);
    waitv<16>();
    STAMP(1); BAR(); STAMP(2);
    const int fA = ((qq >> 1) & 1) | ((g & 1) << 1);
    ldsp wqa = sm + L_RING + w * (NRING * 4096) + (8 * g + qq) * 128 + 8 * pp;
    ldsp xa = sm + L_X + c * 1024;
#pragma unroll
    for (int t = 0; t < 4; ++t)
#pragma unroll
      for (int qt = 0; qt < 3; ++qt) qacc[t][qt] = f32x4{0.f, 0.f, 0.f, 0.f};
    bf16x8 fw[2][4], fx[2][3];
#define A_READ(s, buf)                                                                                          \
  do {                                                                                                          \
    _Pragma("unroll") for (int t_ = 0; t_ < 4; ++t_) {                                                          \
      ldsp p_ = wqa + ((s) % NRING) * 4096 + ((t_ ^ fA) << 5);                                                  \
      fw[buf][t_] = lds_tr2(p_, p_ + 512);                                                                      \
    }                                                                                                           \
    _Pragma("unroll") for (int q_ = 0; q_ < 3; ++q_)                                                            \
        fx[buf][q_] = lds_rd128(xa + q_ * 16384 + ((((4 * (((s) + b) & 15)) | g) ^ c) << 4));                   \
  } while (0)
    waitv<12>();
    A_READ(0, 0);
#pragma unroll
    for (int s = 0; s < 16; ++s) {
      if (s + 4 < 16) WQ_ISSUE(s + 4);
      if (s + 1 < 16) {
        constexpr int dummy = 0; (void)dummy;
        const int rem = ((s + 4 < 16) ? (s + 4) : 15) - (s + 1);
        if (rem == 3) waitv<12>(); else if (rem == 2) waitv<8>(); else if (rem == 1) waitv<4>(); else waitv<0>();
        A_READ(s + 1, (s + 1) & 1);
      }
#pragma unroll
      for (int t = 0; t < 4; ++t)
#pragma unroll
        for (int qt = 0; qt < 3; ++qt) qacc[t][qt] = mfma(fw[s & 1][t], fx[s & 1][qt], qacc[t][qt]);
    }
    STAMP(3); BAR(); STAMP(4);
    {
      const int h = w >> 1;
#pragma unroll
      for (int t = 0; t < 4; ++t)
#pragma unroll
        for (int qt = 0; qt < 3; ++qt) {
          const int c16 = 8 * (w & 1) + 2 * t + (g >> 1);
          *(LDS_AS bf16x4*)(sm + L_Q + (48 * h + 16 * qt + c) * 256 + ((c16 ^ c) << 4) + 8 * (g & 1)) = cvt4(qacc[t][qt]);
        }
    }
  } else {
    const int v = w - 4;
    const u32x4* kp = (const u32x4*)(kc + ((size_t)(b * HKV + kvh) * SK + 128 * v) * DH) + l;
    const u32x4* vp = (const u32x4*)(vc + ((size_t)(b * HKV + kvh) * SK + 128 * v) * DH) + l;
#pragma unroll
    for (int j = 0; j < 2; ++j)
#pragma unroll
      for (int i = 0; i < 8; ++i) kreg[j][i] = *(kp + (j * 8 + i) * 64);
    STAMP(1); BAR(); STAMP(2);
#pragma unroll
    for (int j = 2; j < 4; ++j)
#pragma unroll
      for (int i = 0; i < 8; ++i) { if ((i & 3) == 0) waitv<KVD>(); kreg[j][i] = *(kp + (j * 8 + i) * 64); }
#pragma unroll
    for (int j = 0; j < 3; ++j)
#pragma unroll
      for (int i = 0; i < 8; ++i) { if ((i & 3) == 0) waitv<KVD>(); vreg[j][i] = *(vp + (j * 8 + i) * 64); }
#pragma unroll
    for (int i = 0; i < 8; ++i)
      *(LDS_AS u32x4*)(sm + (32 * v + 4 * i + g) * 256 + (((c ^ g) ^ (4 * (i & 3))) << 4)) = kreg[0][i];
    STAMP(3); BAR(); STAMP(4);
#pragma unroll
    for (int i = 0; i < 8; ++i)
      *(LDS_AS u32x4*)(sm + L_SLOT + (32 * v + 4 * i + g) * 256 + (((c ^ g) ^ (4 * (i & 3))) << 4)) = kreg[1][i];
#pragma unroll
    for (int i = 0; i < 8; ++i)
      *(LDS_AS u32x4*)(sm + 2 * L_SLOT + (32 * v + 4 * i + g) * 256 + (((c ^ g) ^ (4 * (i & 3))) << 4)) = kreg[2][i];
#pragma unroll
    for (int i = 0; i < 8; ++i) vreg[3][i] = *(vp + (3 * 8 + i) * 64);
  }
  STAMP(5); BAR(); STAMP(6);

#define K_DUMP(j)                                                                                                            \
  do {                                                                                                                       \
    if (w >= 4) {                                                                                                            \
      _Pragma("unroll") for (int i_ = 0; i_ < 8; ++i_)                                                                       \
          *(LDS_AS u32x4*)(sm + (j) * L_SLOT + (32 * (w - 4) + 4 * i_ + g) * 256 + (((c ^ g) ^ (4 * (i_ & 3))) << 4)) = kreg[j][i_]; \
    }                                                                                                                        \
  } while (0)
#define V_DUMP(j)                                                                                                            \
  do {                                                                                                                       \
    if (w >= 4) {                                                                                                            \
      _Pragma("unroll") for (int i_ = 0; i_ < 8; ++i_)                                                                       \
          *(LDS_AS u32x4*)(sm + ((j) & 1) * L_SLOT + (32 * (w - 4) + 4 * i_ + g) * 256 + (((c ^ (2 * g)) ^ (8 * (i_ & 1))) << 4)) = vreg[j][i_]; \
    }                                                                                                                        \
  } while (0)

  const int h = w >> 2, kq = w & 3;
  f32x4 sacc[4][2][3];
  {
    bf16x8 qf[4][3];
#pragma unroll
    for (int s = 0; s < 4; ++s)
#pragma unroll
      for (int qt = 0; qt < 3; ++qt) qf[s][qt] = lds_rd128(sm + L_Q + (48 * h + 16 * qt + c) * 256 + ((((4 * s) | g) ^ c) << 4));
    ldsp ka = sm + (32 * kq + c) * 256;
#pragma unroll
    for (int j = 0; j < 4; ++j) {
      if (j == 0) K_DUMP(3);
      if (j == 1) V_DUMP(0);
      if (j == 2) V_DUMP(1);
#pragma unroll
      for (int kt = 0; kt < 2; ++kt) {
#pragma unroll
        for (int qt = 0; qt < 3; ++qt) sacc[j][kt][qt] = f32x4{0.f, 0.f, 0.f, 0.f};
#pragma unroll
        for (int s = 0; s < 4; ++s) {
          bf16x8 kf = lds_rd128(ka + j * L_SLOT + kt * 4096 + ((((4 * s) | g) ^ c) << 4));
#pragma unroll
          for (int qt = 0; qt < 3; ++qt) sacc[j][kt][qt] = mfma(kf, qf[s][qt], sacc[j][kt][qt]);
        }
      }
      if (j < 3) { BAR(); STAMP(7 + j); }
    }
  }
  float mx[3];
#pragma unroll
  for (int qt = 0; qt < 3; ++qt) {
    float m = sacc[0][0][qt][0];
#pragma unroll
    for (int j = 0; j < 4; ++j)
#pragma unroll
      for (int kt = 0; kt < 2; ++kt)
#pragma unroll
        for (int r = 0; r < 4; ++r) m = fmaxf(m, sacc[j][kt][qt][r]);
    m = xmax_g(m);
    if (g == 0) *(LDS_AS float*)(sm + L_STAT + ((h * 4 + kq) * 48 + 16 * qt + c) * 4) = m;
  }
  STAMP(10); BAR(); STAMP(11);
#pragma unroll
  for (int qt = 0; qt < 3; ++qt) {
    float m = *(LDS_AS float*)(sm + L_STAT + ((h * 4 + 0) * 48 + 16 * qt + c) * 4);
#pragma unroll
    for (int k = 1; k < 4; ++k) m = fmaxf(m, *(LDS_AS float*)(sm + L_STAT + ((h * 4 + k) * 48 + 16 * qt + c) * 4));
    mx[qt] = m * SCL2E;
  }
  BAR();
  float lsum[3];
#pragma unroll
  for (int qt = 0; qt < 3; ++qt) {
    float ls = 0.f;
#pragma unroll
    for (int j = 0; j < 4; ++j) {
#pragma unroll
      for (int kt = 0; kt < 2; ++kt)
#pragma unroll
        for (int r = 0; r < 4; ++r) {
          float p = __builtin_amdgcn_exp2f(fmaf(sacc[j][kt][qt][r], SCL2E, -mx[qt]));
          sacc[j][kt][qt][r] = p;
          ls += p;
        }
      *(LDS_AS bf16x8*)(sm + L_P + ((w * 4 + j) * 3 + qt) * 1024 + l * 16) = cvt8(sacc[j][0][qt], sacc[j][1][qt]);
    }
    lsum[qt] = xsum_g(ls);
  }
  u32x4 wreg[8][4];
  const u32x4* wop = (const u32x4*)(wo + (size_t)(n0 + (l >> 3)) * DM + 64 * w) + (l & 7);
#define WO_LOAD(u)                                                                                   \
  do {                                                                                               \
    _Pragma("unroll") for (int i_ = 0; i_ < 4; ++i_) wreg[u][i_] = wop[(32 * (((u) + b) & 7) + 8 * i_) * (DM / 8)]; \
  } while (0)
  WO_LOAD(0); WO_LOAD(1); WO_LOAD(2); WO_LOAD(3);
  STAMP(12); BAR(); STAMP(13);

  const int dq = kq;
  f32x4 oacc[2][3];
#pragma unroll
  for (int dd = 0; dd < 2; ++dd)
#pragma unroll
    for (int qt = 0; qt < 3; ++qt) oacc[dd][qt] = f32x4{0.f, 0.f, 0.f, 0.f};
  {
    const int fV = 4 * (g & 1) + qq;
    ldsp va = sm + (4 * g + qq) * 256 + 8 * pp;
#pragma unroll
    for (int j = 0; j < 4; ++j) {
      if (j == 1) { V_DUMP(2); WO_LOAD(4); WO_LOAD(5); }
      if (j == 2) { V_DUMP(3); WO_LOAD(6); WO_LOAD(7); }
      if (j == 3) {
        if (g == 0) {
#pragma unroll
          for (int qt = 0; qt < 3; ++qt) *(LDS_AS float*)(sm + L_LSUM + (w * 48 + 16 * qt + c) * 4) = lsum[qt];
        }
      }
#pragma unroll
      for (int k4 = 0; k4 < 4; ++k4) {
        bf16x8 pf[3], vf[2];
#pragma unroll
        for (int qt = 0; qt < 3; ++qt) pf[qt] = lds_rd128(sm + L_P + (((h * 4 + k4) * 4 + j) * 3 + qt) * 1024 + l * 16);
#pragma unroll
        for (int dd = 0; dd < 2; ++dd) {
          ldsp p = va + (j & 1) * L_SLOT + k4 * 8192 + ((((2 * dq) ^ fV) ^ dd) << 5);
          vf[dd] = lds_tr2(p, p + 4096);
        }
#pragma unroll
        for (int dd = 0; dd < 2; ++dd)
#pragma unroll
          for (int qt = 0; qt < 3; ++qt) oacc[dd][qt] = mfma(vf[dd], pf[qt], oacc[dd][qt]);
      }
      BAR(); STAMP(14 + j);
    }
  }
#pragma unroll
  for (int qt = 0; qt < 3; ++qt) {
    float ls = 0.f;
#pragma unroll
    for (int k = 0; k < 4; ++k) ls += *(LDS_AS float*)(sm + L_LSUM + ((h * 4 + k) * 48 + 16 * qt + c) * 4);
    const float inv = 1.0f / ls;
    *(LDS_AS bf16x8*)(sm + L_O + (w * 3 + qt) * 1024 + l * 16) = cvt8(oacc[0][qt] * inv, oacc[1][qt] * inv);
  }
  BAR(); STAMP(18);

  f32x4 cacc[4][3];
#pragma unroll
  for (int t = 0; t < 4; ++t)
#pragma unroll
    for (int qt = 0; qt < 3; ++qt) cacc[t][qt] = f32x4{0.f, 0.f, 0.f, 0.f};
  {
    ldsp wst = sm + L_WO + w * 16384 + (l >> 3) * 128 + (((l & 7) ^ (g << 1)) << 4);
    const int fC = 2 * (g & 1) + (qq >> 1);
    ldsp wra = sm + L_WO + w * 16384 + (4 * g + qq) * 128 + 8 * pp;
#pragma unroll
    for (int u = 0; u < 8; ++u) {
#pragma unroll
      for (int i = 0; i < 4; ++i) *(LDS_AS u32x4*)(wst + (u & 3) * 4096 + i * 1024) = wreg[u][i];
      bf16x8 of[3], wf[4];
#pragma unroll
      for (int qt = 0; qt < 3; ++qt) of[qt] = lds_rd128(sm + L_O + (((u + b) & 7) * 3 + qt) * 1024 + l * 16);
#pragma unroll
      for (int t = 0; t < 4; ++t) {
        ldsp p = wra + (u & 3) * 4096 + ((t ^ fC) << 5);
        wf[t] = lds_tr2(p, p + 2048);
      }
#pragma unroll
      for (int t = 0; t < 4; ++t)
#pragma unroll
        for (int qt = 0; qt < 3; ++qt) cacc[t][qt] = mfma(wf[t], of[qt], cacc[t][qt]);
    }
  }
  STAMP(19);
  f32x4* pp4 = (f32x4*)part + ((size_t)(b * 16 + kvh * 2 + hp) * 8 + w) * 12 * 64 + l;
#pragma unroll
  for (int t = 0; t < 4; ++t)
#pragma unroll
    for (int qt = 0; qt < 3; ++qt) pp4[(t * 3 + qt) * 64] = cacc[t][qt];
}

__global__ __launch_bounds__(384) void gqa_reduce(const float* __restrict__ part, __bf16* __restrict__ out) {
  const int idx = blockIdx.x * 384 + threadIdx.x;
  const int b = idx / 6144, rem = idx - b * 6144;
  const f32x4* p = (const f32x4*)part + (size_t)b * 16 * 6144 + rem;
  f32x4 v[16];
#pragma unroll
  for (int s = 0; s < 16; ++s) v[s] = p[(size_t)s * 6144];
  f32x4 a = v[0];
#pragma unroll
  for (int s = 1; s < 16; ++s) a += v[s];
  const int lane = rem & 63, tq = rem >> 6, qt = tq % 3, wt = tq / 3;
  const int q = 16 * qt + (lane & 15), n = 16 * wt + 4 * (lane >> 4);
  *(bf16x4*)(out + ((size_t)b * SQ + q) * DM + n) = cvt4(a);
}

extern "C" void kernel_launch(void* const* d_in, const int* in_sizes, int n_in, void* d_out, int out_size, void* d_ws, size_t ws_size,
                              hipStream_t stream) {
  const __bf16* x = (const __bf16*)d_in[0];
  const __bf16* kc = (const __bf16*)d_in[1];
  const __bf16* vc = (const __bf16*)d_in[2];
  const __bf16* wq = (const __bf16*)d_in[3];
  const __bf16* wo = (const __bf16*)d_in[4];
  float* part = (float*)d_ws;
  (void)hipFuncSetAttribute((const void*)gqa_fused, hipFuncAttributeMaxDynamicSharedMemorySize, LDS_BYTES);
  hipLaunchKernelGGL(gqa_fused, dim3(256), dim3(512), LDS_BYTES, stream, x, kc, vc, wq, wo, part);
  hipLaunchKernelGGL(gqa_reduce, dim3(256), dim3(384), 0, stream, (const float*)part, (__bf16*)d_out);
}
```

```cpp
#include <hip/hip_runtime.h>
#include <hip/hip_bf16.h>
#include <stdint.h>

typedef __bf16 bf16x8 __attribute__((ext_vector_type(8)));
typedef __bf16 bf16x4 __attribute__((ext_vector_type(4)));
typedef float f32x4 __attribute__((ext_vector_type(4)));
typedef int i32x4 __attribute__((ext_vector_type(4)));
typedef unsigned u32x4 __attribute__((ext_vector_type(4)));
#define LDS_AS __attribute__((address_space(3)))
typedef LDS_AS char* ldsp;

constexpr int B_ = 16, SQ = 48, DM = 512, HQ = 32, HKV = 8, SK = 512, DH = 128;
constexpr float SCL2E = 0.08838834764831845f * 1.4426950408889634f;

constexpr int L_SLOT = 32768;
constexpr int L_RING = 32768;
constexpr int L_X = 114688;
constexpr int L_Q = 131072;
constexpr int L_STAT = 155648;
constexpr int L_P = 65536;
constexpr int L_LSUM = 0;
constexpr int L_O = 8192;
constexpr int L_WO = 32768;
constexpr int LDS_BYTES = 163840;
constexpr int NRING = 5;

#define BAR() asm volatile("s_waitcnt lgkmcnt(0)\n\ts_barrier" ::: "memory")
#define SCHED() __builtin_amdgcn_sched_barrier(0)
#define STAMP(i)
template <int N> __device__ __forceinline__ void waitv() { asm volatile("s_waitcnt vmcnt(%0)" ::"i"(N) : "memory"); }

__device__ __forceinline__ void dma16(unsigned ldsaddr, unsigned voff, i32x4 srd, unsigned soff) {
  asm volatile("s_mov_b32 m0, %0\n\ts_nop 0\n\tbuffer_load_dwordx4 %1, %2, %3 offen lds" ::"s"(ldsaddr), "v"(voff), "s"(srd), "s"(soff) : "memory");
}
__device__ __forceinline__ i32x4 make_srd(const void* p, unsigned bytes) {
  unsigned long long a = (unsigned long long)p;
  i32x4 s;
  s[0] = (int)(unsigned)a; s[1] = (int)((unsigned)(a >> 32) & 0xffffu); s[2] = (int)bytes; s[3] = 0x00020000;
  return s;
}
__device__ __forceinline__ bf16x8 lds_rd128(ldsp p) { return *(LDS_AS bf16x8*)p; }
__device__ __forceinline__ bf16x8 lds_tr2(ldsp p0, ldsp p1) {
  bf16x4 a = __builtin_amdgcn_ds_read_tr16_b64_v4bf16((LDS_AS bf16x4*)p0);
  bf16x4 b = __builtin_amdgcn_ds_read_tr16_b64_v4bf16((LDS_AS bf16x4*)p1);
  return __builtin_shufflevector(a, b, 0, 1, 2, 3, 4, 5, 6, 7);
}
__device__ __forceinline__ f32x4 mfma(bf16x8 a, bf16x8 b, f32x4 c) { return __builtin_amdgcn_mfma_f32_16x16x32_bf16(a, b, c, 0, 0, 0); }
__device__ __forceinline__ bf16x4 cvt4(f32x4 v) {
  bf16x4 r; r[0] = (__bf16)v[0]; r[1] = (__bf16)v[1]; r[2] = (__bf16)v[2]; r[3] = (__bf16)v[3]; return r;
}
__device__ __forceinline__ bf16x8 cvt8(f32x4 a, f32x4 b) { return __builtin_shufflevector(cvt4(a), cvt4(b), 0, 1, 2, 3, 4, 5, 6, 7); }
__device__ __forceinline__ float xmax_g(float v) {
  auto r = __builtin_amdgcn_permlane16_swap(__float_as_uint(v), __float_as_uint(v), false, false);
  v = fmaxf(__uint_as_float(r[0]), __uint_as_float(r[1]));
  auto r2 = __builtin_amdgcn_permlane32_swap(__float_as_uint(v), __float_as_uint(v), false, false);
  return fmaxf(__uint_as_float(r2[0]), __uint_as_float(r2[1]));
}
__device__ __forceinline__ float xsum_g(float v) {
  auto r = __builtin_amdgcn_permlane16_swap(__float_as_uint(v), __float_as_uint(v), false, false);
  v = __uint_as_float(r[0]) + __uint_as_float(r[1]);
  auto r2 = __builtin_amdgcn_permlane32_swap(__float_as_uint(v), __float_as_uint(v), false, false);
  return __uint_as_float(r2[0]) + __uint_as_float(r2[1]);
}

__global__ __launch_bounds__(512, 2) void gqa_fused(const __bf16* __restrict__ x, const __bf16* __restrict__ kc, const __bf16* __restrict__ vc,
                                                    const __bf16* __restrict__ wq, const __bf16* __restrict__ wo, __bf16* __restrict__ part) {
  extern __shared__ __attribute__((aligned(16))) char smem_raw[];
  ldsp sm = (ldsp)smem_raw;
  const unsigned smbase = (unsigned)(uintptr_t)sm;
  const int tid = threadIdx.x, l = tid & 63, c = l & 15, g = l >> 4, qq = c >> 2, pp = l & 3;
  const int w = __builtin_amdgcn_readfirstlane(tid >> 6);
  const int bid = blockIdx.x, kvh = bid & 7, jj = bid >> 3, b = jj >> 1, hp = jj & 1;
  STAMP(0);
  const int n0 = kvh * 512 + hp * 256;
  const i32x4 srd_k = make_srd(kc + (size_t)(b * HKV + kvh) * SK * DH, SK * DH * 2);
  const i32x4 srd_v = make_srd(vc + (size_t)(b * HKV + kvh) * SK * DH, SK * DH * 2);
  const unsigned kvo = (unsigned)(g * 256 + ((c ^ g) << 4));
  const unsigned vvo = (unsigned)(g * 256 + ((c ^ (2 * g)) << 4));

  if (w < 4) {
    f32x4 qacc[4][3];
    const i32x4 srd_x = make_srd(x + (size_t)b * SQ * DM, SQ * DM * 2);
    const i32x4 srd_w = make_srd(wq, DM * HQ * DH * 2);
#pragma unroll
    for (int i = 0; i < 12; ++i) {
      const int r = w * 12 + i;
      dma16(smbase + L_X + r * 1024, (unsigned)((l ^ (r & 15)) * 16), srd_x, (unsigned)(r * 1024));
    }
    const unsigned ring = smbase + L_RING + w * (NRING * 4096);
    const unsigned voff_e = (unsigned)((l >> 3) * 8192 + (((l & 7) ^ (((l >> 4) & 1) << 1)) * 16));
    const unsigned voff_o = voff_e ^ 64u;
    const unsigned scol = (unsigned)((n0 + 64 * w) * 2);
#define WQ_ISSUE(s)                                                                                                        \
  do {                                                                                                                     \
    _Pragma("unroll") for (int i_ = 0; i_ < 4; ++i_)                                                                       \
        dma16(ring + ((s) % NRING) * 4096 + i_ * 1024, (i_ & 1) ? voff_o : voff_e, srd_w, scol + (unsigned)((32 * (((s) + b) & 15) + 8 * i_) * 8192)); \
  } while (0)
    WQ_ISSUE(0); WQ_ISSUE(1); WQ_ISSUE(2); WQ_ISSUE(3);
    waitv<16>();
    STAMP(1); BAR(); STAMP(2);
    const int fA = ((qq >> 1) & 1) | ((g & 1) << 1);
    ldsp wqa = sm + L_RING + w * (NRING * 4096) + (8 * g + qq) * 128 + 8 * pp;
    ldsp xa = sm + L_X + c * 1024;
#pragma unroll
    for (int t = 0; t < 4; ++t)
#pragma unroll
      for (int qt = 0; qt < 3; ++qt) qacc[t][qt] = f32x4{0.f, 0.f, 0.f, 0.f};
    bf16x8 fw[2][4], fx[2][3];
#define A_READ(s, buf)                                                                                          \
  do {                                                                                                          \
    _Pragma("unroll") for (int t_ = 0; t_ < 4; ++t_) {                                                          \
      ldsp p_ = wqa + ((s) % NRING) * 4096 + ((t_ ^ fA) << 5);                                                  \
      fw[buf][t_] = lds_tr2(p_, p_ + 512);                                                                      \
    }                                                                                                           \
    _Pragma("unroll") for (int q_ = 0; q_ < 3; ++q_)                                                            \
        fx[buf][q_] = lds_rd128(xa + q_ * 16384 + ((((4 * (((s) + b) & 15)) | g) ^ c) << 4));                   \
  } while (0)
    waitv<12>();
    A_READ(0, 0);
#pragma unroll
    for (int s = 0; s < 16; ++s) {
      if (s + 4 < 16) WQ_ISSUE(s + 4);
      if (s == 12) {
#pragma unroll
        for (int i = 0; i < 8; ++i)
          dma16(smbase + (32 * w + 4 * i) * 256, (kvo ^ (unsigned)(64 * (i & 3))), srd_k, (unsigned)((128 * w + 4 * i) * 256));
      }
      if (s + 1 < 16) {
        const int rem = ((s + 4 < 16) ? (s + 4) : 15) - (s + 1);
        if (s < 12) waitv<12>(); else if (rem == 2) waitv<16>(); else if (rem == 1) waitv<12>(); else waitv<8>();
        A_READ(s + 1, (s + 1) & 1);
      }
      SCHED();
#pragma unroll
      for (int t = 0; t < 4; ++t)
#pragma unroll
        for (int qt = 0; qt < 3; ++qt) qacc[t][qt] = mfma(fw[s & 1][t], fx[s & 1][qt], qacc[t][qt]);
      SCHED();
    }
    STAMP(3); BAR(); STAMP(4);
    {
      const int h = w >> 1;
#pragma unroll
      for (int t = 0; t < 4; ++t)
#pragma unroll
        for (int qt = 0; qt < 3; ++qt) {
          const int c16 = 8 * (w & 1) + 2 * t + (g >> 1);
          *(LDS_AS bf16x4*)(sm + L_Q + (48 * h + 16 * qt + c) * 256 + ((c16 ^ c) << 4) + 8 * (g & 1)) = cvt4(qacc[t][qt]);
        }
    }
  } else {
    STAMP(1); BAR(); STAMP(2);
    STAMP(3); BAR(); STAMP(4);
  }
  const unsigned kvrow = (unsigned)((128 * (w >> 1) + 16 * (w & 1)) * 256);
#define K_ISSUE(j)                                                                                                      \
  do {                                                                                                                  \
    _Pragma("unroll") for (int i_ = 0; i_ < 4; ++i_)                                                                    \
        dma16(smbase + (j) * L_SLOT + (16 * w + 4 * i_) * 256, kvo ^ (unsigned)(64 * i_), srd_k, kvrow + (unsigned)((32 * (j) + 4 * i_) * 256)); \
  } while (0)
#define V_ISSUE(j)                                                                                                      \
  do {                                                                                                                  \
    _Pragma("unroll") for (int i_ = 0; i_ < 4; ++i_)                                                                    \
        dma16(smbase + ((j) & 1) * L_SLOT + (16 * w + 4 * i_) * 256, vvo ^ (unsigned)(128 * (i_ & 1)), srd_v, kvrow + (unsigned)((32 * (j) + 4 * i_) * 256)); \
  } while (0)
  K_ISSUE(1); K_ISSUE(2); K_ISSUE(3);
  waitv<12>();
  STAMP(5); BAR(); STAMP(6);

  const int h = w >> 2, kq = w & 3;
  f32x4 sacc[4][2][3];
  u32x4 vreg[2][4];
  {
    bf16x8 qf[4][3];
#pragma unroll
    for (int s = 0; s < 4; ++s)
#pragma unroll
      for (int qt = 0; qt < 3; ++qt) qf[s][qt] = lds_rd128(sm + L_Q + (48 * h + 16 * qt + c) * 256 + ((((4 * s) | g) ^ c) << 4));
    ldsp ka = sm + (32 * kq + c) * 256;
#pragma unroll
    for (int j = 0; j < 4; ++j) {
      bf16x8 kf[2][4];
#pragma unroll
      for (int kt = 0; kt < 2; ++kt)
#pragma unroll
        for (int s = 0; s < 4; ++s) kf[kt][s] = lds_rd128(ka + j * L_SLOT + kt * 4096 + ((((4 * s) | g) ^ c) << 4));
      if (j == 1) V_ISSUE(0);
      if (j == 2) {
        V_ISSUE(1);
        const u32x4* vp = (const u32x4*)(vc + ((size_t)(b * HKV + kvh) * SK + 128 * (w >> 1) + 16 * (w & 1)) * DH) + l;
#pragma unroll
        for (int jv = 0; jv < 2; ++jv)
#pragma unroll
          for (int i = 0; i < 4; ++i) vreg[jv][i] = vp[(32 * (jv + 2) + 4 * i) * 16];
      }
      SCHED();
#pragma unroll
      for (int kt = 0; kt < 2; ++kt)
#pragma unroll
        for (int s = 0; s < 4; ++s)
#pragma unroll
          for (int qt = 0; qt < 3; ++qt)
            sacc[j][kt][qt] = mfma(kf[kt][s], qf[s][qt], s == 0 ? f32x4{0.f, 0.f, 0.f, 0.f} : sacc[j][kt][qt]);
      SCHED();
      if (j == 0) waitv<8>();
      if (j == 1) waitv<8>();
      if (j == 2) waitv<16>();
      if (j < 3) { BAR(); STAMP(7 + j); }
    }
  }
  float mx[3];
#pragma unroll
  for (int qt = 0; qt < 3; ++qt) {
    float m = sacc[0][0][qt][0];
#pragma unroll
    for (int j = 0; j < 4; ++j)
#pragma unroll
      for (int kt = 0; kt < 2; ++kt)
#pragma unroll
        for (int r = 0; r < 4; ++r) m = fmaxf(m, sacc[j][kt][qt][r]);
    m = xmax_g(m);
    if (g == 0) *(LDS_AS float*)(sm + L_STAT + ((h * 4 + kq) * 48 + 16 * qt + c) * 4) = m;
  }
  u32x4 wreg[8][4];
  const u32x4* wop = (const u32x4*)(wo + (size_t)(n0 + (l >> 3)) * DM + 64 * w) + (l & 7);
#define WO_LOAD(u)                                                                                   \
  do {                                                                                               \
    _Pragma("unroll") for (int i_ = 0; i_ < 4; ++i_) wreg[u][i_] = wop[(32 * (((u) + b) & 7) + 8 * i_) * (DM / 8)]; \
  } while (0)
  WO_LOAD(0); WO_LOAD(1);
  STAMP(10); BAR(); STAMP(11);
#pragma unroll
  for (int qt = 0; qt < 3; ++qt) {
    float m = *(LDS_AS float*)(sm + L_STAT + ((h * 4 + 0) * 48 + 16 * qt + c) * 4);
#pragma unroll
    for (int k = 1; k < 4; ++k) m = fmaxf(m, *(LDS_AS float*)(sm + L_STAT + ((h * 4 + k) * 48 + 16 * qt + c) * 4));
    mx[qt] = m * SCL2E;
  }
  BAR();
  float lsum[3];
#pragma unroll
  for (int qt = 0; qt < 3; ++qt) {
    float ls = 0.f;
#pragma unroll
    for (int j = 0; j < 4; ++j) {
#pragma unroll
      for (int kt = 0; kt < 2; ++kt)
#pragma unroll
        for (int r = 0; r < 4; ++r) {
          float p = __builtin_amdgcn_exp2f(fmaf(sacc[j][kt][qt][r], SCL2E, -mx[qt]));
          sacc[j][kt][qt][r] = p;
          ls += p;
        }
      *(LDS_AS bf16x8*)(sm + L_P + ((w * 4 + j) * 3 + qt) * 1024 + l * 16) = cvt8(sacc[j][0][qt], sacc[j][1][qt]);
    }
    lsum[qt] = xsum_g(ls);
    SCHED();
    if (qt == 0) { WO_LOAD(2); }
    if (qt == 1) { WO_LOAD(3); WO_LOAD(4); }
    if (qt == 2) { WO_LOAD(5); }
    SCHED();
  }
  waitv<32>();
  STAMP(12); BAR(); STAMP(13);

  const int dq = kq;
  f32x4 oacc[2][3];
#pragma unroll
  for (int dd = 0; dd < 2; ++dd)
#pragma unroll
    for (int qt = 0; qt < 3; ++qt) oacc[dd][qt] = f32x4{0.f, 0.f, 0.f, 0.f};
  {
    const int fV = 4 * (g & 1) + qq;
    ldsp va = sm + (4 * g + qq) * 256 + 8 * pp;
    ldsp pa = sm + L_P + h * 49152 + l * 16;
#pragma unroll
    for (int j = 0; j < 4; ++j) {
      if (j == 1 || j == 2) {
#pragma unroll
        for (int i = 0; i < 4; ++i)
          *(LDS_AS u32x4*)(sm + ((j + 1) & 1) * L_SLOT + (16 * w + 4 * i + g) * 256 + (((c ^ (2 * g)) ^ (8 * (i & 1))) << 4)) = vreg[j - 1][i];
        WO_LOAD(5 + j);
      }
      if (j == 3) {
        if (g == 0) {
#pragma unroll
          for (int qt = 0; qt < 3; ++qt) *(LDS_AS float*)(sm + L_LSUM + (w * 48 + 16 * qt + c) * 4) = lsum[qt];
        }
      }
      bf16x8 pf[2][3], vf[2][2];
#define PV_READ(k4)                                                                                       \
  do {                                                                                                    \
    _Pragma("unroll") for (int d_ = 0; d_ < 2; ++d_) {                                                    \
      ldsp p_ = va + (j & 1) * L_SLOT + (k4) * 8192 + ((((2 * dq) ^ fV) ^ d_) << 5);                      \
      vf[(k4) & 1][d_] = lds_tr2(p_, p_ + 4096);                                                          \
    }                                                                                                     \
    _Pragma("unroll") for (int q_ = 0; q_ < 3; ++q_) pf[(k4) & 1][q_] = lds_rd128(pa + (((k4) * 4 + j) * 3 + q_) * 1024); \
  } while (0)
      PV_READ(0);
#pragma unroll
      for (int k4 = 0; k4 < 4; ++k4) {
        if (k4 + 1 < 4) PV_READ(k4 + 1);
        SCHED();
#pragma unroll
        for (int d_ = 0; d_ < 2; ++d_)
#pragma unroll
          for (int q_ = 0; q_ < 3; ++q_) oacc[d_][q_] = mfma(vf[k4 & 1][d_], pf[k4 & 1][q_], oacc[d_][q_]);
        SCHED();
      }
      BAR(); STAMP(14 + j);
    }
  }
#pragma unroll
  for (int qt = 0; qt < 3; ++qt) {
    float ls = 0.f;
#pragma unroll
    for (int k = 0; k < 4; ++k) ls += *(LDS_AS float*)(sm + L_LSUM + ((h * 4 + k) * 48 + 16 * qt + c) * 4);
    const float inv = 1.0f / ls;
    *(LDS_AS bf16x8*)(sm + L_O + (w * 3 + qt) * 1024 + l * 16) = cvt8(oacc[0][qt] * inv, oacc[1][qt] * inv);
  }
  ldsp wst = sm + L_WO + w * 16384 + (l >> 3) * 128 + (((l & 7) ^ (g << 1)) << 4);
#define WO_STAGE(u)                                                                                         \
  do {                                                                                                      \
    _Pragma("unroll") for (int i_ = 0; i_ < 4; ++i_) *(LDS_AS u32x4*)(wst + ((u) & 3) * 4096 + i_ * 1024) = wreg[u][i_]; \
  } while (0)
  WO_STAGE(0); WO_STAGE(1);
  BAR(); STAMP(18);

  f32x4 cacc[4][3];
#pragma unroll
  for (int t = 0; t < 4; ++t)
#pragma unroll
    for (int qt = 0; qt < 3; ++qt) cacc[t][qt] = f32x4{0.f, 0.f, 0.f, 0.f};
  {
    const int fC = 2 * (g & 1) + (qq >> 1);
    ldsp wra = sm + L_WO + w * 16384 + (4 * g + qq) * 128 + 8 * pp;
    bf16x8 of[2][3], wf[2][4];
#define C_READ(u)                                                                                                   \
  do {                                                                                                              \
    _Pragma("unroll") for (int t_ = 0; t_ < 4; ++t_) {                                                              \
      ldsp p_ = wra + ((u) & 3) * 4096 + ((t_ ^ fC) << 5);                                                          \
      wf[(u) & 1][t_] = lds_tr2(p_, p_ + 2048);                                                                     \
    }                                                                                                               \
    _Pragma("unroll") for (int q_ = 0; q_ < 3; ++q_)                                                                \
        of[(u) & 1][q_] = lds_rd128(sm + L_O + ((((u) + b) & 7) * 3 + q_) * 1024 + l * 16);                         \
  } while (0)
    C_READ(0);
#pragma unroll
    for (int u = 0; u < 8; ++u) {
      if (u + 2 < 8) WO_STAGE(u + 2);
      if (u + 1 < 8) C_READ(u + 1);
      SCHED();
#pragma unroll
      for (int t = 0; t < 4; ++t)
#pragma unroll
        for (int qt = 0; qt < 3; ++qt) cacc[t][qt] = mfma(wf[u & 1][t], of[u & 1][qt], cacc[t][qt]);
      SCHED();
    }
  }
  STAMP(19);
  bf16x8* pp8 = (bf16x8*)part + ((size_t)(b * 16 + kvh * 2 + hp) * 8 + w) * 6 * 64 + l;
#pragma unroll
  for (int a = 0; a < 2; ++a)
#pragma unroll
    for (int qt = 0; qt < 3; ++qt) pp8[(a * 3 + qt) * 64] = cvt8(cacc[2 * a][qt], cacc[2 * a + 1][qt]);
}

__global__ __launch_bounds__(192) void gqa_reduce(const __bf16* __restrict__ part, __bf16* __restrict__ out) {
  const int idx = blockIdx.x * 192 + threadIdx.x;
  const int b = idx / 3072, rem = idx - b * 3072;
  const bf16x8* p = (const bf16x8*)part + (size_t)b * 16 * 3072 + rem;
  bf16x8 v[16];
#pragma unroll
  for (int s = 0; s < 16; ++s) v[s] = p[(size_t)s * 3072];
  f32x4 a0 = {0.f, 0.f, 0.f, 0.f}, a1 = {0.f, 0.f, 0.f, 0.f};
#pragma unroll
  for (int s = 0; s < 16; ++s)
#pragma unroll
    for (int e = 0; e < 4; ++e) { a0[e] += (float)v[s][e]; a1[e] += (float)v[s][4 + e]; }
  const int lane = rem & 63, tq = rem >> 6, qt = tq % 3, wa = tq / 3;
  const int q = 16 * qt + (lane & 15), n = 32 * wa + 4 * (lane >> 4);
  __bf16* o = out + ((size_t)b * SQ + q) * DM + n;
  *(bf16x4*)o = cvt4(a0);
  *(bf16x4*)(o + 16) = cvt4(a1);
}

extern "C" void kernel_launch(void* const* d_in, const int* in_sizes, int n_in, void* d_out, int out_size, void* d_ws, size_t ws_size,
                              hipStream_t stream) {
  const __bf16* x = (const __bf16*)d_in[0];
  const __bf16* kc = (const __bf16*)d_in[1];
  const __bf16* vc = (const __bf16*)d_in[2];
  const __bf16* wq = (const __bf16*)d_in[3];
  const __bf16* wo = (const __bf16*)d_in[4];
  __bf16* part = (__bf16*)d_ws;
  (void)hipFuncSetAttribute((const void*)gqa_fused, hipFuncAttributeMaxDynamicSharedMemorySize, LDS_BYTES);
  hipLaunchKernelGGL(gqa_fused, dim3(256), dim3(512), LDS_BYTES, stream, x, kc, vc, wq, wo, part);
  hipLaunchKernelGGL(gqa_reduce, dim3(256), dim3(192), 0, stream, (const __bf16*)part, (__bf16*)d_out);
}
```

```cpp
#include <hip/hip_runtime.h>
#include <hip/hip_bf16.h>
#include <stdint.h>

typedef __bf16 bf16x8 __attribute__((ext_vector_type(8)));
typedef __bf16 bf16x4 __attribute__((ext_vector_type(4)));
typedef float f32x4 __attribute__((ext_vector_type(4)));
typedef int i32x4 __attribute__((ext_vector_type(4)));
typedef unsigned u32x4 __attribute__((ext_vector_type(4)));
#define LDS_AS __attribute__((address_space(3)))
typedef LDS_AS char* ldsp;

constexpr int B_ = 16, SQ = 48, DM = 512, HQ = 32, HKV = 8, SK = 512, DH = 128;
constexpr float SCL2E = 0.08838834764831845f * 1.4426950408889634f;

constexpr int L_SLOT = 32768;
constexpr int L_RING = 32768;
constexpr int L_X = 114688;
constexpr int L_Q = 131072;
constexpr int L_STAT = 155648;
constexpr int L_P = 65536;
constexpr int L_LSUM = 0;
constexpr int L_O = 8192;
constexpr int L_WO = 32768;
constexpr int L_ST2 = 4 * 32768;
constexpr int L_O2 = 2 * 32768;
constexpr int LDS_BYTES = 163840;
constexpr int NRING = 5;

#define BAR() asm volatile("s_waitcnt lgkmcnt(0)\n\ts_barrier" ::: "memory")
#define SCHED() __builtin_amdgcn_sched_barrier(0)
#define STAMP(i)
template <int N> __device__ __forceinline__ void waitv() { asm volatile("s_waitcnt vmcnt(%0)" ::"i"(N) : "memory"); }

__device__ __forceinline__ void dma16(unsigned ldsaddr, unsigned voff, i32x4 srd, unsigned soff) {
  asm volatile("s_mov_b32 m0, %0\n\ts_nop 0\n\tbuffer_load_dwordx4 %1, %2, %3 offen lds" ::"s"(ldsaddr), "v"(voff), "s"(srd), "s"(soff) : "memory");
}
__device__ __forceinline__ i32x4 make_srd(const void* p, unsigned bytes) {
  unsigned long long a = (unsigned long long)p;
  i32x4 s;
  s[0] = (int)(unsigned)a; s[1] = (int)((unsigned)(a >> 32) & 0xffffu); s[2] = (int)bytes; s[3] = 0x00020000;
  return s;
}
__device__ __forceinline__ bf16x8 lds_rd128(ldsp p) { return *(LDS_AS bf16x8*)p; }
__device__ __forceinline__ bf16x8 lds_tr2(ldsp p0, ldsp p1) {
  bf16x4 a = __builtin_amdgcn_ds_read_tr16_b64_v4bf16((LDS_AS bf16x4*)p0);
  bf16x4 b = __builtin_amdgcn_ds_read_tr16_b64_v4bf16((LDS_AS bf16x4*)p1);
  return __builtin_shufflevector(a, b, 0, 1, 2, 3, 4, 5, 6, 7);
}
__device__ __forceinline__ f32x4 mfma(bf16x8 a, bf16x8 b, f32x4 c) { return __builtin_amdgcn_mfma_f32_16x16x32_bf16(a, b, c, 0, 0, 0); }
__device__ __forceinline__ bf16x4 cvt4(f32x4 v) {
  bf16x4 r; r[0] = (__bf16)v[0]; r[1] = (__bf16)v[1]; r[2] = (__bf16)v[2]; r[3] = (__bf16)v[3]; return r;
}
__device__ __forceinline__ bf16x8 cvt8(f32x4 a, f32x4 b) { return __builtin_shufflevector(cvt4(a), cvt4(b), 0, 1, 2, 3, 4, 5, 6, 7); }
__device__ __forceinline__ float xmax_g(float v) {
  auto r = __builtin_amdgcn_permlane16_swap(__float_as_uint(v), __float_as_uint(v), false, false);
  v = fmaxf(__uint_as_float(r[0]), __uint_as_float(r[1]));
  auto r2 = __builtin_amdgcn_permlane32_swap(__float_as_uint(v), __float_as_uint(v), false, false);
  return fmaxf(__uint_as_float(r2[0]), __uint_as_float(r2[1]));
}
__device__ __forceinline__ float xsum_g(float v) {
  auto r = __builtin_amdgcn_permlane16_swap(__float_as_uint(v), __float_as_uint(v), false, false);
  v = __uint_as_float(r[0]) + __uint_as_float(r[1]);
  auto r2 = __builtin_amdgcn_permlane32_swap(__float_as_uint(v), __float_as_uint(v), false, false);
  return __uint_as_float(r2[0]) + __uint_as_float(r2[1]);
}

__device__ __forceinline__ int xoff(int wd, int r, int qt) {
  const int idx = wd * 3 + qt;
  if (r == 0) return L_O2 + idx * 1024;
  if (r == 1) return L_SLOT + idx * 1024;
  return (idx < 8 ? L_SLOT + 24576 : (idx < 16 ? 2 * L_SLOT + 24576 - 8192 : 4 * L_SLOT + 4096 - 16384)) + idx * 1024;
}

__global__ __launch_bounds__(512, 2) void gqa_fused(const __bf16* __restrict__ x, const __bf16* __restrict__ kc, const __bf16* __restrict__ vc,
                                                    const __bf16* __restrict__ wq, const __bf16* __restrict__ wo, __bf16* __restrict__ part) {
  extern __shared__ __attribute__((aligned(16))) char smem_raw[];
  ldsp sm = (ldsp)smem_raw;
  const unsigned smbase = (unsigned)(uintptr_t)sm;
  const int tid = threadIdx.x, l = tid & 63, c = l & 15, g = l >> 4, qq = c >> 2, pp = l & 3;
  const int w = __builtin_amdgcn_readfirstlane(tid >> 6);
  const int bid = blockIdx.x, kvh = bid & 7, jj = bid >> 3, b = jj >> 1, hp = jj & 1;
  STAMP(0);
  const int n0 = kvh * 512 + hp * 256;
  const i32x4 srd_k = make_srd(kc + (size_t)(b * HKV + kvh) * SK * DH, SK * DH * 2);
  const i32x4 srd_v = make_srd(vc + (size_t)(b * HKV + kvh) * SK * DH, SK * DH * 2);
  const unsigned kvo = (unsigned)(g * 256 + ((c ^ g) << 4));
  const unsigned vvo = (unsigned)(g * 256 + ((c ^ (2 * g)) << 4));

  if (w < 4) {
    f32x4 qacc[4][3];
    const i32x4 srd_x = make_srd(x + (size_t)b * SQ * DM, SQ * DM * 2);
    const i32x4 srd_w = make_srd(wq, DM * HQ * DH * 2);
#pragma unroll
    for (int i = 0; i < 12; ++i) {
      const int r = w * 12 + i;
      dma16(smbase + L_X + r * 1024, (unsigned)((l ^ (r & 15)) * 16), srd_x, (unsigned)(r * 1024));
    }
    const unsigned ring = smbase + L_RING + w * (NRING * 4096);
    const unsigned voff_e = (unsigned)((l >> 3) * 8192 + (((l & 7) ^ (((l >> 4) & 1) << 1)) * 16));
    const unsigned voff_o = voff_e ^ 64u;
    const unsigned scol = (unsigned)((n0 + 64 * w) * 2);
#define WQ_ISSUE(s)                                                                                                        \
  do {                                                                                                                     \
    _Pragma("unroll") for (int i_ = 0; i_ < 4; ++i_)                                                                       \
        dma16(ring + ((s) % NRING) * 4096 + i_ * 1024, (i_ & 1) ? voff_o : voff_e, srd_w, scol + (unsigned)((32 * (((s) + b) & 15) + 8 * i_) * 8192)); \
  } while (0)
    WQ_ISSUE(0); WQ_ISSUE(1); WQ_ISSUE(2); WQ_ISSUE(3);
    waitv<16>();
    STAMP(1); BAR(); STAMP(2);
    const int fA = ((qq >> 1) & 1) | ((g & 1) << 1);
    ldsp wqa = sm + L_RING + w * (NRING * 4096) + (8 * g + qq) * 128 + 8 * pp;
    ldsp xa = sm + L_X + c * 1024;
#pragma unroll
    for (int t = 0; t < 4; ++t)
#pragma unroll
      for (int qt = 0; qt < 3; ++qt) qacc[t][qt] = f32x4{0.f, 0.f, 0.f, 0.f};
    bf16x8 fw[2][4], fx[2][3];
#define A_READ(s, buf)                                                                                          \
  do {                                                                                                          \
    _Pragma("unroll") for (int t_ = 0; t_ < 4; ++t_) {                                                          \
      ldsp p_ = wqa + ((s) % NRING) * 4096 + ((t_ ^ fA) << 5);                                                  \
      fw[buf][t_] = lds_tr2(p_, p_ + 512);                                                                      \
    }                                                                                                           \
    _Pragma("unroll") for (int q_ = 0; q_ < 3; ++q_)                                                            \
        fx[buf][q_] = lds_rd128(xa + q_ * 16384 + ((((4 * (((s) + b) & 15)) | g) ^ c) << 4));                   \
  } while (0)
    waitv<12>();
    A_READ(0, 0);
#pragma unroll
    for (int s = 0; s < 16; ++s) {
      if (s + 4 < 16) WQ_ISSUE(s + 4);
      if (s == 12) {
#pragma unroll
        for (int i = 0; i < 8; ++i)
          dma16(smbase + (32 * w + 4 * i) * 256, (kvo ^ (unsigned)(64 * (i & 3))), srd_k, (unsigned)((128 * w + 4 * i) * 256));
      }
      if (s + 1 < 16) {
        const int rem = ((s + 4 < 16) ? (s + 4) : 15) - (s + 1);
        if (s < 12) waitv<12>(); else if (rem == 2) waitv<16>(); else if (rem == 1) waitv<12>(); else waitv<8>();
        A_READ(s + 1, (s + 1) & 1);
      }
      SCHED();
#pragma unroll
      for (int t = 0; t < 4; ++t)
#pragma unroll
        for (int qt = 0; qt < 3; ++qt) qacc[t][qt] = mfma(fw[s & 1][t], fx[s & 1][qt], qacc[t][qt]);
      SCHED();
    }
    STAMP(3); BAR(); STAMP(4);
    {
      const int h = w >> 1;
#pragma unroll
      for (int t = 0; t < 4; ++t)
#pragma unroll
        for (int qt = 0; qt < 3; ++qt) {
          const int c16 = 8 * (w & 1) + 2 * t + (g >> 1);
          *(LDS_AS bf16x4*)(sm + L_Q + (48 * h + 16 * qt + c) * 256 + ((c16 ^ c) << 4) + 8 * (g & 1)) = cvt4(qacc[t][qt]);
        }
    }
  } else {
    STAMP(1); BAR(); STAMP(2);
    STAMP(3); BAR(); STAMP(4);
  }
  const unsigned kvrow = (unsigned)((128 * (w >> 1) + 16 * (w & 1)) * 256);
#define K_ISSUE(j)                                                                                                      \
  do {                                                                                                                  \
    _Pragma("unroll") for (int i_ = 0; i_ < 4; ++i_)                                                                    \
        dma16(smbase + (j) * L_SLOT + (16 * w + 4 * i_) * 256, kvo ^ (unsigned)(64 * i_), srd_k, kvrow + (unsigned)((32 * (j) + 4 * i_) * 256)); \
  } while (0)
#define V_ISSUE(j, slot)                                                                                                \
  do {                                                                                                                  \
    _Pragma("unroll") for (int i_ = 0; i_ < 4; ++i_)                                                                    \
        dma16(smbase + (slot) * L_SLOT + (16 * w + 4 * i_) * 256, vvo ^ (unsigned)(128 * (i_ & 1)), srd_v, kvrow + (unsigned)((32 * (j) + 4 * i_) * 256)); \
  } while (0)
  const i32x4 srd_o = make_srd(wo + (size_t)n0 * DM, 256 * DM * 2);
  const unsigned wvo = (unsigned)((l >> 3) * 1024 + (((l & 7) ^ (g << 1)) << 4));
#define WO_ISSUE(u, slot)                                                                                               \
  do {                                                                                                                  \
    _Pragma("unroll") for (int i_ = 0; i_ < 4; ++i_)                                                                    \
        dma16(smbase + (slot) * L_SLOT + w * 4096 + i_ * 1024, wvo, srd_o, (unsigned)(w * 128 + (32 * (((u) + b) & 7) + 8 * i_) * 1024)); \
  } while (0)
  K_ISSUE(1); K_ISSUE(2); K_ISSUE(3);
  waitv<12>();
  STAMP(5); BAR(); STAMP(6);

  const int h = w >> 2, kq = w & 3;
  f32x4 sacc[4][2][3];
  {
    bf16x8 qf[4][3];
#pragma unroll
    for (int s = 0; s < 4; ++s)
#pragma unroll
      for (int qt = 0; qt < 3; ++qt) qf[s][qt] = lds_rd128(sm + L_Q + (48 * h + 16 * qt + c) * 256 + ((((4 * s) | g) ^ c) << 4));
    ldsp ka = sm + (32 * kq + c) * 256;
#pragma unroll
    for (int j = 0; j < 4; ++j) {
      bf16x8 kf[2][4];
#pragma unroll
      for (int kt = 0; kt < 2; ++kt)
#pragma unroll
        for (int s = 0; s < 4; ++s) kf[kt][s] = lds_rd128(ka + j * L_SLOT + kt * 4096 + ((((4 * s) | g) ^ c) << 4));
      SCHED();
#pragma unroll
      for (int kt = 0; kt < 2; ++kt)
#pragma unroll
        for (int s = 0; s < 4; ++s)
#pragma unroll
          for (int qt = 0; qt < 3; ++qt)
            sacc[j][kt][qt] = mfma(kf[kt][s], qf[s][qt], s == 0 ? f32x4{0.f, 0.f, 0.f, 0.f} : sacc[j][kt][qt]);
      SCHED();
      if (j == 0) { waitv<8>(); }
      if (j == 1) { V_ISSUE(0, 0); V_ISSUE(1, 4); waitv<12>(); }
      if (j == 2) { V_ISSUE(2, 1); waitv<12>(); }
      if (j == 3) { V_ISSUE(3, 2); waitv<12>(); }
      BAR(); STAMP(7 + j);
    }
  }
  WO_ISSUE(0, 3);
  float mloc[3], lsum[3];
  bf16x8 pf[4][3];
#pragma unroll
  for (int qt = 0; qt < 3; ++qt) {
    float m = sacc[0][0][qt][0];
#pragma unroll
    for (int j = 0; j < 4; ++j)
#pragma unroll
      for (int kt = 0; kt < 2; ++kt)
#pragma unroll
        for (int r = 0; r < 4; ++r) m = fmaxf(m, sacc[j][kt][qt][r]);
    m = xmax_g(m) * SCL2E;
    mloc[qt] = m;
    float ls = 0.f;
#pragma unroll
    for (int j = 0; j < 4; ++j) {
#pragma unroll
      for (int kt = 0; kt < 2; ++kt)
#pragma unroll
        for (int r = 0; r < 4; ++r) {
          float p = __builtin_amdgcn_exp2f(fmaf(sacc[j][kt][qt][r], SCL2E, -m));
          sacc[j][kt][qt][r] = p;
          ls += p;
        }
      pf[j][qt] = cvt8(sacc[j][0][qt], sacc[j][1][qt]);
    }
    lsum[qt] = xsum_g(ls);
  }
  STAMP(12);

  f32x4 oacc[8][3];
  {
    const int fV = 4 * (g & 1) + qq;
    ldsp va = sm + (32 * kq + 4 * g + qq) * 256 + 8 * pp;
#pragma unroll
    for (int j = 0; j < 4; ++j) {
      const int slot = j == 0 ? 0 : (j == 1 ? 4 : j - 1);
      bf16x8 vf[8];
#pragma unroll
      for (int dt = 0; dt < 8; ++dt) {
        ldsp p = va + slot * L_SLOT + ((dt ^ fV) << 5);
        vf[dt] = lds_tr2(p, p + 4096);
      }
      SCHED();
#pragma unroll
      for (int dt = 0; dt < 8; ++dt)
#pragma unroll
        for (int qt = 0; qt < 3; ++qt) oacc[dt][qt] = mfma(vf[dt], pf[j][qt], j == 0 ? f32x4{0.f, 0.f, 0.f, 0.f} : oacc[dt][qt]);
      SCHED();
      if (j == 0) { waitv<12>(); }
      if (j == 1) { WO_ISSUE(1, 0); waitv<12>(); }
      if (j == 2) {
        if (g == 0) {
#pragma unroll
          for (int qt = 0; qt < 3; ++qt) {
            *(LDS_AS float*)(sm + L_ST2 + (w * 48 + 16 * qt + c) * 8) = mloc[qt];
            *(LDS_AS float*)(sm + L_ST2 + (w * 48 + 16 * qt + c) * 8 + 4) = lsum[qt];
          }
        }
        waitv<8>();
      }
      BAR(); STAMP(13 + j);
    }
  }
  {
    float sc[3];
#pragma unroll
    for (int qt = 0; qt < 3; ++qt) {
      float mk[4], lk[4];
#pragma unroll
      for (int k = 0; k < 4; ++k) {
        mk[k] = *(LDS_AS float*)(sm + L_ST2 + ((h * 4 + k) * 48 + 16 * qt + c) * 8);
        lk[k] = *(LDS_AS float*)(sm + L_ST2 + ((h * 4 + k) * 48 + 16 * qt + c) * 8 + 4);
      }
      const float m = fmaxf(fmaxf(mk[0], mk[1]), fmaxf(mk[2], mk[3]));
      float lt = 0.f;
#pragma unroll
      for (int k = 0; k < 4; ++k) lt += lk[k] * __builtin_amdgcn_exp2f(mk[k] - m);
      sc[qt] = __builtin_amdgcn_exp2f(mloc[qt] - m) / lt;
    }
#pragma unroll
    for (int r = 1; r < 4; ++r) {
#pragma unroll
      for (int kb = 0; kb < 4; ++kb) {
        if (kb == ((kq + r) & 3)) {
#pragma unroll
          for (int qt = 0; qt < 3; ++qt)
            *(LDS_AS bf16x8*)(sm + xoff(h * 4 + kb, r - 1, qt) + l * 16) = cvt8(oacc[2 * kb][qt] * sc[qt], oacc[2 * kb + 1][qt] * sc[qt]);
        }
      }
    }
    f32x4 own[2][3];
#pragma unroll
    for (int kb = 0; kb < 4; ++kb) {
      if (kb == kq) {
#pragma unroll
        for (int qt = 0; qt < 3; ++qt) { own[0][qt] = oacc[2 * kb][qt] * sc[qt]; own[1][qt] = oacc[2 * kb + 1][qt] * sc[qt]; }
      }
    }
    BAR(); STAMP(17);
#pragma unroll
    for (int qt = 0; qt < 3; ++qt) {
#pragma unroll
      for (int r = 0; r < 3; ++r) {
        bf16x8 t = lds_rd128(sm + xoff(w, r, qt) + l * 16);
#pragma unroll
        for (int e = 0; e < 4; ++e) { own[0][qt][e] += (float)t[e]; own[1][qt][e] += (float)t[4 + e]; }
      }
      *(LDS_AS bf16x8*)(sm + L_O2 + (w * 3 + qt) * 1024 + l * 16) = cvt8(own[0][qt], own[1][qt]);
    }
  }
  BAR(); STAMP(18);
  WO_ISSUE(2, 1); WO_ISSUE(3, 4);

  f32x4 cacc[4][3];
  {
    bf16x8 of[8][3];
#pragma unroll
    for (int u = 0; u < 8; ++u)
#pragma unroll
      for (int qt = 0; qt < 3; ++qt) of[u][qt] = lds_rd128(sm + L_O2 + (((u + b) & 7) * 3 + qt) * 1024 + l * 16);
    const int fC = 2 * (g & 1) + (qq >> 1);
    ldsp wra = sm + w * 4096 + (4 * g + qq) * 128 + 8 * pp;
    bf16x8 wf[2][4];
#define CSLOT(u) (((u) & 3) == 0 ? 3 : (((u) & 3) == 1 ? 0 : (((u) & 3) == 2 ? 1 : 4)))
#define C_READ(u)                                                                                                   \
  do {                                                                                                              \
    _Pragma("unroll") for (int t_ = 0; t_ < 4; ++t_) {                                                              \
      ldsp p_ = wra + CSLOT(u) * L_SLOT + ((t_ ^ fC) << 5);                                                         \
      wf[(u) & 1][t_] = lds_tr2(p_, p_ + 2048);                                                                     \
    }                                                                                                               \
  } while (0)
    waitv<12>();
    C_READ(0);
#pragma unroll
    for (int u = 0; u < 8; ++u) {
      if (u >= 1 && u + 3 < 8) WO_ISSUE(u + 3, CSLOT(u + 3));
      if (u + 1 < 8) {
        const int last = (u + 3 < 8) ? (u + 3) : 7;
        const int rem = last - (u + 1);
        if (rem == 2) waitv<8>(); else if (rem == 1) waitv<4>(); else waitv<0>();
        C_READ(u + 1);
      }
      SCHED();
#pragma unroll
      for (int t = 0; t < 4; ++t)
#pragma unroll
        for (int qt = 0; qt < 3; ++qt) cacc[t][qt] = mfma(wf[u & 1][t], of[u][qt], u == 0 ? f32x4{0.f, 0.f, 0.f, 0.f} : cacc[t][qt]);
      SCHED();
    }
  }
  STAMP(19);
  bf16x8* pp8 = (bf16x8*)part + ((size_t)(b * 16 + kvh * 2 + hp) * 8 + w) * 6 * 64 + l;
#pragma unroll
  for (int a = 0; a < 2; ++a)
#pragma unroll
    for (int qt = 0; qt < 3; ++qt) pp8[(a * 3 + qt) * 64] = cvt8(cacc[2 * a][qt], cacc[2 * a + 1][qt]);
}

__global__ __launch_bounds__(192) void gqa_reduce(const __bf16* __restrict__ part, __bf16* __restrict__ out) {
  const int idx = blockIdx.x * 192 + threadIdx.x;
  const int b = idx / 3072, rem = idx - b * 3072;
  const bf16x8* p = (const bf16x8*)part + (size_t)b * 16 * 3072 + rem;
  bf16x8 v[16];
#pragma unroll
  for (int s = 0; s < 16; ++s) v[s] = p[(size_t)s * 3072];
  f32x4 a0 = {0.f, 0.f, 0.f, 0.f}, a1 = {0.f, 0.f, 0.f, 0.f};
#pragma unroll
  for (int s = 0; s < 16; ++s)
#pragma unroll
    for (int e = 0; e < 4; ++e) { a0[e] += (float)v[s][e]; a1[e] += (float)v[s][4 + e]; }
  const int lane = rem & 63, tq = rem >> 6, qt = tq % 3, wa = tq / 3;
  const int q = 16 * qt + (lane & 15), n = 32 * wa + 4 * (lane >> 4);
  __bf16* o = out + ((size_t)b * SQ + q) * DM + n;
  *(bf16x4*)o = cvt4(a0);
  *(bf16x4*)(o + 16) = cvt4(a1);
}

extern "C" void kernel_launch(void* const* d_in, const int* in_sizes, int n_in, void* d_out, int out_size, void* d_ws, size_t ws_size,
                              hipStream_t stream) {
  const __bf16* x = (const __bf16*)d_in[0];
  const __bf16* kc = (const __bf16*)d_in[1];
  const __bf16* vc = (const __bf16*)d_in[2];
  const __bf16* wq = (const __bf16*)d_in[3];
  const __bf16* wo = (const __bf16*)d_in[4];
  __bf16* part = (__bf16*)d_ws;
  (void)hipFuncSetAttribute((const void*)gqa_fused, hipFuncAttributeMaxDynamicSharedMemorySize, LDS_BYTES);
  hipLaunchKernelGGL(gqa_fused, dim3(256), dim3(512), LDS_BYTES, stream, x, kc, vc, wq, wo, part);
  hipLaunchKernelGGL(gqa_reduce, dim3(256), dim3(192), 0, stream, (const __bf16*)part, (__bf16*)d_out);
}
```

```cpp
#include <hip/hip_runtime.h>
#include <hip/hip_bf16.h>
#include <stdint.h>

typedef __bf16 bf16x8 __attribute__((ext_vector_type(8)));
typedef __bf16 bf16x4 __attribute__((ext_vector_type(4)));
typedef float f32x4 __attribute__((ext_vector_type(4)));
typedef int i32x4 __attribute__((ext_vector_type(4)));
typedef unsigned u32x4 __attribute__((ext_vector_type(4)));
#define LDS_AS __attribute__((address_space(3)))
typedef LDS_AS char* ldsp;

constexpr int B_ = 16, SQ = 48, DM = 512, HQ = 32, HKV = 8, SK = 512, DH = 128;
constexpr float SCL2E = 0.08838834764831845f * 1.4426950408889634f;

constexpr int L_SLOT = 32768;
constexpr int L_RING = 32768;
constexpr int L_X = 114688;
constexpr int L_Q = 131072;
constexpr int L_STAT = 155648;
constexpr int L_P = 65536;
constexpr int L_LSUM = 0;
constexpr int L_O = 8192;
constexpr int L_WO = 32768;
constexpr int L_ST2 = 4 * 32768;
constexpr int L_O2 = 2 * 32768;
constexpr int LDS_BYTES = 163840;
constexpr int NRING = 5;

#define BAR() asm volatile("s_waitcnt lgkmcnt(0)\n\ts_barrier" ::: "memory")
#define SCHED() __builtin_amdgcn_sched_barrier(0)
#define STAMP(i)
template <int N> __device__ __forceinline__ void waitv() { asm volatile("s_waitcnt vmcnt(%0)" ::"i"(N) : "memory"); }

__device__ __forceinline__ void dma16(unsigned ldsaddr, unsigned voff, i32x4 srd, unsigned soff) {
  asm volatile("s_mov_b32 m0, %0\n\ts_nop 0\n\tbuffer_load_dwordx4 %1, %2, %3 offen lds" ::"s"(ldsaddr), "v"(voff), "s"(srd), "s"(soff) : "memory");
}
__device__ __forceinline__ void dma16nt(unsigned ldsaddr, unsigned voff, i32x4 srd, unsigned soff) {
  asm volatile("s_mov_b32 m0, %0\n\ts_nop 0\n\tbuffer_load_dwordx4 %1, %2, %3 offen nt lds" ::"s"(ldsaddr), "v"(voff), "s"(srd), "s"(soff) : "memory");
}
#define KV_NT 1
#define W_NT 0
#define dma_kv dma16nt
#define dma_w dma16
__device__ __forceinline__ i32x4 make_srd(const void* p, unsigned bytes) {
  unsigned long long a = (unsigned long long)p;
  i32x4 s;
  s[0] = (int)(unsigned)a; s[1] = (int)((unsigned)(a >> 32) & 0xffffu); s[2] = (int)bytes; s[3] = 0x00020000;
  return s;
}
__device__ __forceinline__ bf16x8 lds_rd128(ldsp p) { return *(LDS_AS bf16x8*)p; }
__device__ __forceinline__ bf16x8 lds_tr2(ldsp p0, ldsp p1) {
  bf16x4 a = __builtin_amdgcn_ds_read_tr16_b64_v4bf16((LDS_AS bf16x4*)p0);
  bf16x4 b = __builtin_amdgcn_ds_read_tr16_b64_v4bf16((LDS_AS bf16x4*)p1);
  return __builtin_shufflevector(a, b, 0, 1, 2, 3, 4, 5, 6, 7);
}
__device__ __forceinline__ f32x4 mfma(bf16x8 a, bf16x8 b, f32x4 c) { return __builtin_amdgcn_mfma_f32_16x16x32_bf16(a, b, c, 0, 0, 0); }
__device__ __forceinline__ bf16x4 cvt4(f32x4 v) {
  bf16x4 r; r[0] = (__bf16)v[0]; r[1] = (__bf16)v[1]; r[2] = (__bf16)v[2]; r[3] = (__bf16)v[3]; return r;
}
__device__ __forceinline__ bf16x8 cvt8(f32x4 a, f32x4 b) { return __builtin_shufflevector(cvt4(a), cvt4(b), 0, 1, 2, 3, 4, 5, 6, 7); }
__device__ __forceinline__ float xmax_g(float v) {
  auto r = __builtin_amdgcn_permlane16_swap(__float_as_uint(v), __float_as_uint(v), false, false);
  v = fmaxf(__uint_as_float(r[0]), __uint_as_float(r[1]));
  auto r2 = __builtin_amdgcn_permlane32_swap(__float_as_uint(v), __float_as_uint(v), false, false);
  return fmaxf(__uint_as_float(r2[0]), __uint_as_float(r2[1]));
}
__device__ __forceinline__ float xsum_g(float v) {
  auto r = __builtin_amdgcn_permlane16_swap(__float_as_uint(v), __float_as_uint(v), false, false);
  v = __uint_as_float(r[0]) + __uint_as_float(r[1]);
  auto r2 = __builtin_amdgcn_permlane32_swap(__float_as_uint(v), __float_as_uint(v), false, false);
  return __uint_as_float(r2[0]) + __uint_as_float(r2[1]);
}

__device__ __forceinline__ int xoff(int wd, int r, int qt) {
  const int idx = wd * 3 + qt;
  if (r == 0) return L_O2 + idx * 1024;
  if (r == 1) return L_SLOT + idx * 1024;
  return (idx < 8 ? L_SLOT + 24576 : (idx < 16 ? 2 * L_SLOT + 24576 - 8192 : 4 * L_SLOT + 4096 - 16384)) + idx * 1024;
}

__global__ __launch_bounds__(512, 2) void gqa_fused(const __bf16* __restrict__ x, const __bf16* __restrict__ kc, const __bf16* __restrict__ vc,
                                                    const __bf16* __restrict__ wq, const __bf16* __restrict__ wo, __bf16* __restrict__ part) {
  extern __shared__ __attribute__((aligned(16))) char smem_raw[];
  ldsp sm = (ldsp)smem_raw;
  const unsigned smbase = (unsigned)(uintptr_t)sm;
  const int tid = threadIdx.x, l = tid & 63, c = l & 15, g = l >> 4, qq = c >> 2, pp = l & 3;
  const int w = __builtin_amdgcn_readfirstlane(tid >> 6);
  const int bid = blockIdx.x, kvh = bid & 7, jj = bid >> 3, b = jj >> 1, hp = jj & 1;
  STAMP(0);
  const int n0 = kvh * 512 + hp * 256;
  const i32x4 srd_k = make_srd(kc + (size_t)(b * HKV + kvh) * SK * DH, SK * DH * 2);
  const i32x4 srd_v = make_srd(vc + (size_t)(b * HKV + kvh) * SK * DH, SK * DH * 2);
  const unsigned kvo = (unsigned)(g * 256 + ((c ^ g) << 4));
  const unsigned vvo = (unsigned)(g * 256 + ((c ^ (2 * g)) << 4));

  if (w < 4) {
    f32x4 qacc[4][3];
    const i32x4 srd_x = make_srd(x + (size_t)b * SQ * DM, SQ * DM * 2);
    const i32x4 srd_w = make_srd(wq, DM * HQ * DH * 2);
#pragma unroll
    for (int i = 0; i < 12; ++i) {
      const int r = w * 12 + i;
      dma_w(smbase + L_X + r * 1024, (unsigned)((l ^ (r & 15)) * 16), srd_x, (unsigned)(r * 1024));
    }
    const unsigned ring = smbase + L_RING + w * (NRING * 4096);
    const unsigned voff_e = (unsigned)((l >> 3) * 8192 + (((l & 7) ^ (((l >> 4) & 1) << 1)) * 16));
    const unsigned voff_o = voff_e ^ 64u;
    const unsigned scol = (unsigned)((n0 + 64 * w) * 2);
#define WQ_ISSUE(s)                                                                                                        \
  do {                                                                                                                     \
    _Pragma("unroll") for (int i_ = 0; i_ < 4; ++i_)                                                                       \
        dma_w(ring + ((s) % NRING) * 4096 + i_ * 1024, (i_ & 1) ? voff_o : voff_e, srd_w, scol + (unsigned)((32 * (((s) + b) & 15) + 8 * i_) * 8192)); \
  } while (0)
    WQ_ISSUE(0); WQ_ISSUE(1); WQ_ISSUE(2); WQ_ISSUE(3);
    waitv<16>();
    STAMP(1); BAR(); STAMP(2);
    const int fA = ((qq >> 1) & 1) | ((g & 1) << 1);
    ldsp wqa = sm + L_RING + w * (NRING * 4096) + (8 * g + qq) * 128 + 8 * pp;
    ldsp xa = sm + L_X + c * 1024;
#pragma unroll
    for (int t = 0; t < 4; ++t)
#pragma unroll
      for (int qt = 0; qt < 3; ++qt) qacc[t][qt] = f32x4{0.f, 0.f, 0.f, 0.f};
    bf16x8 fw[2][4], fx[2][3];
#define A_READ(s, buf)                                                                                          \
  do {                                                                                                          \
    _Pragma("unroll") for (int t_ = 0; t_ < 4; ++t_) {                                                          \
      ldsp p_ = wqa + ((s) % NRING) * 4096 + ((t_ ^ fA) << 5);                                                  \
      fw[buf][t_] = lds_tr2(p_, p_ + 512);                                                                      \
    }                                                                                                           \
    _Pragma("unroll") for (int q_ = 0; q_ < 3; ++q_)                                                            \
        fx[buf][q_] = lds_rd128(xa + q_ * 16384 + ((((4 * (((s) + b) & 15)) | g) ^ c) << 4));                   \
  } while (0)
    waitv<12>();
    A_READ(0, 0);
#pragma unroll
    for (int s = 0; s < 16; ++s) {
      if (s + 4 < 16) WQ_ISSUE(s + 4);
      if (s == 12) {
#pragma unroll
        for (int i = 0; i < 8; ++i)
          dma_kv(smbase + (32 * w + 4 * i) * 256, (kvo ^ (unsigned)(64 * (i & 3))), srd_k, (unsigned)((128 * w + 4 * i) * 256));
      }
      if (s + 1 < 16) {
        const int rem = ((s + 4 < 16) ? (s + 4) : 15) - (s + 1);
        if (s < 12) waitv<12>(); else if (rem == 2) waitv<16>(); else if (rem == 1) waitv<12>(); else waitv<8>();
        A_READ(s + 1, (s + 1) & 1);
      }
      SCHED();
#pragma unroll
      for (int t = 0; t < 4; ++t)
#pragma unroll
        for (int qt = 0; qt < 3; ++qt) qacc[t][qt] = mfma(fw[s & 1][t], fx[s & 1][qt], qacc[t][qt]);
      SCHED();
    }
    STAMP(3); BAR(); STAMP(4);
    {
      const int h = w >> 1;
#pragma unroll
      for (int t = 0; t < 4; ++t)
#pragma unroll
        for (int qt = 0; qt < 3; ++qt) {
          const int c16 = 8 * (w & 1) + 2 * t + (g >> 1);
          *(LDS_AS bf16x4*)(sm + L_Q + (48 * h + 16 * qt + c) * 256 + ((c16 ^ c) << 4) + 8 * (g & 1)) = cvt4(qacc[t][qt]);
        }
    }
  } else {
    STAMP(1); BAR(); STAMP(2);
    STAMP(3); BAR(); STAMP(4);
  }
  const unsigned kvrow = (unsigned)((128 * (w >> 1) + 16 * (w & 1)) * 256);
#define K_ISSUE(j)                                                                                                      \
  do {                                                                                                                  \
    _Pragma("unroll") for (int i_ = 0; i_ < 4; ++i_)                                                                    \
        dma_kv(smbase + (j) * L_SLOT + (16 * w + 4 * i_) * 256, kvo ^ (unsigned)(64 * i_), srd_k, kvrow + (unsigned)((32 * (j) + 4 * i_) * 256)); \
  } while (0)
#define V_ISSUE(j, slot)                                                                                                \
  do {                                                                                                                  \
    _Pragma("unroll") for (int i_ = 0; i_ < 4; ++i_)                                                                    \
        dma_kv(smbase + (slot) * L_SLOT + (16 * w + 4 * i_) * 256, vvo ^ (unsigned)(128 * (i_ & 1)), srd_v, kvrow + (unsigned)((32 * (j) + 4 * i_) * 256)); \
  } while (0)
  const i32x4 srd_o = make_srd(wo + (size_t)n0 * DM, 256 * DM * 2);
  const unsigned wvo = (unsigned)((l >> 3) * 1024 + (((l & 7) ^ (g << 1)) << 4));
#define WO_ISSUE(u, slot)                                                                                               \
  do {                                                                                                                  \
    _Pragma("unroll") for (int i_ = 0; i_ < 4; ++i_)                                                                    \
        dma_w(smbase + (slot) * L_SLOT + w * 4096 + i_ * 1024, wvo, srd_o, (unsigned)(w * 128 + (32 * (((u) + b) & 7) + 8 * i_) * 1024)); \
  } while (0)
  K_ISSUE(1); K_ISSUE(2); K_ISSUE(3);
  waitv<12>();
  STAMP(5); BAR(); STAMP(6);

  const int h = w >> 2, kq = w & 3;
  f32x4 sacc[4][2][3];
  {
    bf16x8 qf[4][3];
#pragma unroll
    for (int s = 0; s < 4; ++s)
#pragma unroll
      for (int qt = 0; qt < 3; ++qt) qf[s][qt] = lds_rd128(sm + L_Q + (48 * h + 16 * qt + c) * 256 + ((((4 * s) | g) ^ c) << 4));
    ldsp ka = sm + (32 * kq + c) * 256;
#pragma unroll
    for (int j = 0; j < 4; ++j) {
      bf16x8 kf[2][4];
#pragma unroll
      for (int kt = 0; kt < 2; ++kt)
#pragma unroll
        for (int s = 0; s < 4; ++s) kf[kt][s] = lds_rd128(ka + j * L_SLOT + kt * 4096 + ((((4 * s) | g) ^ c) << 4));
      SCHED();
#pragma unroll
      for (int kt = 0; kt < 2; ++kt)
#pragma unroll
        for (int s = 0; s < 4; ++s)
#pragma unroll
          for (int qt = 0; qt < 3; ++qt)
            sacc[j][kt][qt] = mfma(kf[kt][s], qf[s][qt], s == 0 ? f32x4{0.f, 0.f, 0.f, 0.f} : sacc[j][kt][qt]);
      SCHED();
      if (j == 0) { waitv<8>(); }
      if (j == 1) { V_ISSUE(0, 0); V_ISSUE(1, 4); waitv<12>(); }
      if (j == 2) { V_ISSUE(2, 1); waitv<12>(); }
      if (j == 3) { V_ISSUE(3, 2); waitv<12>(); }
      BAR(); STAMP(7 + j);
    }
  }
  WO_ISSUE(0, 3);
  float mloc[3], lsum[3];
  bf16x8 pf[4][3];
#pragma unroll
  for (int qt = 0; qt < 3; ++qt) {
    float m = sacc[0][0][qt][0];
#pragma unroll
    for (int j = 0; j < 4; ++j)
#pragma unroll
      for (int kt = 0; kt < 2; ++kt)
#pragma unroll
        for (int r = 0; r < 4; ++r) m = fmaxf(m, sacc[j][kt][qt][r]);
    m = xmax_g(m) * SCL2E;
    mloc[qt] = m;
    float ls = 0.f;
#pragma unroll
    for (int j = 0; j < 4; ++j) {
#pragma unroll
      for (int kt = 0; kt < 2; ++kt)
#pragma unroll
        for (int r = 0; r < 4; ++r) {
          float p = __builtin_amdgcn_exp2f(fmaf(sacc[j][kt][qt][r], SCL2E, -m));
          sacc[j][kt][qt][r] = p;
          ls += p;
        }
      pf[j][qt] = cvt8(sacc[j][0][qt], sacc[j][1][qt]);
    }
    lsum[qt] = xsum_g(ls);
  }
  STAMP(12);

  f32x4 oacc[8][3];
  {
    const int fV = 4 * (g & 1) + qq;
    ldsp va = sm + (32 * kq + 4 * g + qq) * 256 + 8 * pp;
#pragma unroll
    for (int j = 0; j < 4; ++j) {
      const int slot = j == 0 ? 0 : (j == 1 ? 4 : j - 1);
      bf16x8 vf[8];
#pragma unroll
      for (int dt = 0; dt < 8; ++dt) {
        ldsp p = va + slot * L_SLOT + ((dt ^ fV) << 5);
        vf[dt] = lds_tr2(p, p + 4096);
      }
      SCHED();
#pragma unroll
      for (int dt = 0; dt < 8; ++dt)
#pragma unroll
        for (int qt = 0; qt < 3; ++qt) oacc[dt][qt] = mfma(vf[dt], pf[j][qt], j == 0 ? f32x4{0.f, 0.f, 0.f, 0.f} : oacc[dt][qt]);
      SCHED();
      if (j == 0) { waitv<12>(); }
      if (j == 1) { WO_ISSUE(1, 0); waitv<12>(); }
      if (j == 2) {
        if (g == 0) {
#pragma unroll
          for (int qt = 0; qt < 3; ++qt) {
            *(LDS_AS float*)(sm + L_ST2 + (w * 48 + 16 * qt + c) * 8) = mloc[qt];
            *(LDS_AS float*)(sm + L_ST2 + (w * 48 + 16 * qt + c) * 8 + 4) = lsum[qt];
          }
        }
        waitv<8>();
      }
      BAR(); STAMP(13 + j);
    }
  }
  {
    float sc[3];
#pragma unroll
    for (int qt = 0; qt < 3; ++qt) {
      float mk[4], lk[4];
#pragma unroll
      for (int k = 0; k < 4; ++k) {
        mk[k] = *(LDS_AS float*)(sm + L_ST2 + ((h * 4 + k) * 48 + 16 * qt + c) * 8);
        lk[k] = *(LDS_AS float*)(sm + L_ST2 + ((h * 4 + k) * 48 + 16 * qt + c) * 8 + 4);
      }
      const float m = fmaxf(fmaxf(mk[0], mk[1]), fmaxf(mk[2], mk[3]));
      float lt = 0.f;
#pragma unroll
      for (int k = 0; k < 4; ++k) lt += lk[k] * __builtin_amdgcn_exp2f(mk[k] - m);
      sc[qt] = __builtin_amdgcn_exp2f(mloc[qt] - m) / lt;
    }
#pragma unroll
    for (int r = 1; r < 4; ++r) {
#pragma unroll
      for (int kb = 0; kb < 4; ++kb) {
        if (kb == ((kq + r) & 3)) {
#pragma unroll
          for (int qt = 0; qt < 3; ++qt)
            *(LDS_AS bf16x8*)(sm + xoff(h * 4 + kb, r - 1, qt) + l * 16) = cvt8(oacc[2 * kb][qt] * sc[qt], oacc[2 * kb + 1][qt] * sc[qt]);
        }
      }
    }
    f32x4 own[2][3];
#pragma unroll
    for (int kb = 0; kb < 4; ++kb) {
      if (kb == kq) {
#pragma unroll
        for (int qt = 0; qt < 3; ++qt) { own[0][qt] = oacc[2 * kb][qt] * sc[qt]; own[1][qt] = oacc[2 * kb + 1][qt] * sc[qt]; }
      }
    }
    BAR(); STAMP(17);
#pragma unroll
    for (int qt = 0; qt < 3; ++qt) {
#pragma unroll
      for (int r = 0; r < 3; ++r) {
        bf16x8 t = lds_rd128(sm + xoff(w, r, qt) + l * 16);
#pragma unroll
        for (int e = 0; e < 4; ++e) { own[0][qt][e] += (float)t[e]; own[1][qt][e] += (float)t[4 + e]; }
      }
      *(LDS_AS bf16x8*)(sm + L_O2 + (w * 3 + qt) * 1024 + l * 16) = cvt8(own[0][qt], own[1][qt]);
    }
  }
  BAR(); STAMP(18);
  WO_ISSUE(2, 1); WO_ISSUE(3, 4);

  f32x4 cacc[4][3];
  {
    bf16x8 of[8][3];
#pragma unroll
    for (int u = 0; u < 8; ++u)
#pragma unroll
      for (int qt = 0; qt < 3; ++qt) of[u][qt] = lds_rd128(sm + L_O2 + (((u + b) & 7) * 3 + qt) * 1024 + l * 16);
    const int fC = 2 * (g & 1) + (qq >> 1);
    ldsp wra = sm + w * 4096 + (4 * g + qq) * 128 + 8 * pp;
    bf16x8 wf[2][4];
#define CSLOT(u) (((u) & 3) == 0 ? 3 : (((u) & 3) == 1 ? 0 : (((u) & 3) == 2 ? 1 : 4)))
#define C_READ(u)                                                                                                   \
  do {                                                                                                              \
    _Pragma("unroll") for (int t_ = 0; t_ < 4; ++t_) {                                                              \
      ldsp p_ = wra + CSLOT(u) * L_SLOT + ((t_ ^ fC) << 5);                                                         \
      wf[(u) & 1][t_] = lds_tr2(p_, p_ + 2048);                                                                     \
    }                                                                                                               \
  } while (0)
    waitv<12>();
    C_READ(0);
#pragma unroll
    for (int u = 0; u < 8; ++u) {
      if (u >= 1 && u + 3 < 8) WO_ISSUE(u + 3, CSLOT(u + 3));
      if (u + 1 < 8) {
        const int last = (u + 3 < 8) ? (u + 3) : 7;
        const int rem = last - (u + 1);
        if (rem == 2) waitv<8>(); else if (rem == 1) waitv<4>(); else waitv<0>();
        C_READ(u + 1);
      }
      SCHED();
#pragma unroll
      for (int t = 0; t < 4; ++t)
#pragma unroll
        for (int qt = 0; qt < 3; ++qt) cacc[t][qt] = mfma(wf[u & 1][t], of[u][qt], u == 0 ? f32x4{0.f, 0.f, 0.f, 0.f} : cacc[t][qt]);
      SCHED();
    }
  }
  STAMP(19);
  bf16x8* pp8 = (bf16x8*)part + ((size_t)(b * 16 + kvh * 2 + hp) * 8 + w) * 6 * 64 + l;
#pragma unroll
  for (int a = 0; a < 2; ++a)
#pragma unroll
    for (int qt = 0; qt < 3; ++qt) pp8[(a * 3 + qt) * 64] = cvt8(cacc[2 * a][qt], cacc[2 * a + 1][qt]);
}

__global__ __launch_bounds__(192) void gqa_reduce(const __bf16* __restrict__ part, __bf16* __restrict__ out) {
  const int idx = blockIdx.x * 192 + threadIdx.x;
  const int b = idx / 3072, rem = idx - b * 3072;
  const bf16x8* p = (const bf16x8*)part + (size_t)b * 16 * 3072 + rem;
  bf16x8 v[16];
#pragma unroll
  for (int s = 0; s < 16; ++s) v[s] = p[(size_t)s * 3072];
  f32x4 a0 = {0.f, 0.f, 0.f, 0.f}, a1 = {0.f, 0.f, 0.f, 0.f};
#pragma unroll
  for (int s = 0; s < 16; ++s)
#pragma unroll
    for (int e = 0; e < 4; ++e) { a0[e] += (float)v[s][e]; a1[e] += (float)v[s][4 + e]; }
  const int lane = rem & 63, tq = rem >> 6, qt = tq % 3, wa = tq / 3;
  const int q = 16 * qt + (lane & 15), n = 32 * wa + 4 * (lane >> 4);
  __bf16* o = out + ((size_t)b * SQ + q) * DM + n;
  *(bf16x4*)o = cvt4(a0);
  *(bf16x4*)(o + 16) = cvt4(a1);
}

extern "C" void kernel_launch(void* const* d_in, const int* in_sizes, int n_in, void* d_out, int out_size, void* d_ws, size_t ws_size,
                              hipStream_t stream) {
  const __bf16* x = (const __bf16*)d_in[0];
  const __bf16* kc = (const __bf16*)d_in[1];
  const __bf16* vc = (const __bf16*)d_in[2];
  const __bf16* wq = (const __bf16*)d_in[3];
  const __bf16* wo = (const __bf16*)d_in[4];
  __bf16* part = (__bf16*)d_ws;
  (void)hipFuncSetAttribute((const void*)gqa_fused, hipFuncAttributeMaxDynamicSharedMemorySize, LDS_BYTES);
  hipLaunchKernelGGL(gqa_fused, dim3(256), dim3(512), LDS_BYTES, stream, x, kc, vc, wq, wo, part);
  hipLaunchKernelGGL(gqa_reduce, dim3(256), dim3(192), 0, stream, (const __bf16*)part, (__bf16*)d_out);
}
```

```cpp
#include <hip/hip_runtime.h>
#include <hip/hip_bf16.h>
#include <stdint.h>

typedef __bf16 bf16x8 __attribute__((ext_vector_type(8)));
typedef __bf16 bf16x4 __attribute__((ext_vector_type(4)));
typedef float f32x4 __attribute__((ext_vector_type(4)));
typedef float f32x2 __attribute__((ext_vector_type(2)));
typedef int i32x4 __attribute__((ext_vector_type(4)));
typedef unsigned u32x4 __attribute__((ext_vector_type(4)));
#define LDS_AS __attribute__((address_space(3)))
typedef LDS_AS char* ldsp;

constexpr int B_ = 16, SQ = 48, DM = 512, HQ = 32, HKV = 8, SK = 512, DH = 128;
constexpr float SCL2E = 0.08838834764831845f * 1.4426950408889634f;

constexpr int L_SLOT = 32768;
constexpr int L_RING = 32768;
constexpr int L_X = 114688;
constexpr int L_Q = 131072;
constexpr int L_STAT = 155648;
constexpr int L_P = 65536;
constexpr int L_LSUM = 0;
constexpr int L_O = 8192;
constexpr int L_WO = 32768;
constexpr int L_ST2 = 4 * 32768;
constexpr int L_O2 = 2 * 32768;
constexpr int LDS_BYTES = 163840;
constexpr int NRING = 5;

#define BAR() asm volatile("s_waitcnt lgkmcnt(0)\n\ts_barrier" ::: "memory")
#define SCHED() __builtin_amdgcn_sched_barrier(0)
#define STAMP(i)
template <int N> __device__ __forceinline__ void waitv() { asm volatile("s_waitcnt vmcnt(%0)" ::"i"(N) : "memory"); }

__device__ __forceinline__ void dma16(unsigned ldsaddr, unsigned voff, i32x4 srd, unsigned soff) {
  asm volatile("s_mov_b32 m0, %0\n\ts_nop 0\n\tbuffer_load_dwordx4 %1, %2, %3 offen lds" ::"s"(ldsaddr), "v"(voff), "s"(srd), "s"(soff) : "memory");
}
__device__ __forceinline__ void dma16nt(unsigned ldsaddr, unsigned voff, i32x4 srd, unsigned soff) {
  asm volatile("s_mov_b32 m0, %0\n\ts_nop 0\n\tbuffer_load_dwordx4 %1, %2, %3 offen nt lds" ::"s"(ldsaddr), "v"(voff), "s"(srd), "s"(soff) : "memory");
}
#define KV_NT 1
#define W_NT 0
#define dma_kv dma16nt
#define dma_w dma16
__device__ __forceinline__ i32x4 make_srd(const void* p, unsigned bytes) {
  unsigned long long a = (unsigned long long)p;
  i32x4 s;
  s[0] = (int)(unsigned)a; s[1] = (int)((unsigned)(a >> 32) & 0xffffu); s[2] = (int)bytes; s[3] = 0x00020000;
  return s;
}
__device__ __forceinline__ bf16x8 lds_rd128(ldsp p) { return *(LDS_AS bf16x8*)p; }
__device__ __forceinline__ bf16x8 lds_tr2(ldsp p0, ldsp p1) {
  bf16x4 a = __builtin_amdgcn_ds_read_tr16_b64_v4bf16((LDS_AS bf16x4*)p0);
  bf16x4 b = __builtin_amdgcn_ds_read_tr16_b64_v4bf16((LDS_AS bf16x4*)p1);
  return __builtin_shufflevector(a, b, 0, 1, 2, 3, 4, 5, 6, 7);
}
__device__ __forceinline__ f32x4 mfma(bf16x8 a, bf16x8 b, f32x4 c) { return __builtin_amdgcn_mfma_f32_16x16x32_bf16(a, b, c, 0, 0, 0); }
__device__ __forceinline__ bf16x4 cvt4(f32x4 v) {
  bf16x4 r; r[0] = (__bf16)v[0]; r[1] = (__bf16)v[1]; r[2] = (__bf16)v[2]; r[3] = (__bf16)v[3]; return r;
}
__device__ __forceinline__ bf16x8 cvt8(f32x4 a, f32x4 b) { return __builtin_shufflevector(cvt4(a), cvt4(b), 0, 1, 2, 3, 4, 5, 6, 7); }
__device__ __forceinline__ float xmax_g(float v) {
  auto r = __builtin_amdgcn_permlane16_swap(__float_as_uint(v), __float_as_uint(v), false, false);
  v = fmaxf(__uint_as_float(r[0]), __uint_as_float(r[1]));
  auto r2 = __builtin_amdgcn_permlane32_swap(__float_as_uint(v), __float_as_uint(v), false, false);
  return fmaxf(__uint_as_float(r2[0]), __uint_as_float(r2[1]));
}
__device__ __forceinline__ float xsum_g(float v) {
  auto r = __builtin_amdgcn_permlane16_swap(__float_as_uint(v), __float_as_uint(v), false, false);
  v = __uint_as_float(r[0]) + __uint_as_float(r[1]);
  auto r2 = __builtin_amdgcn_permlane32_swap(__float_as_uint(v), __float_as_uint(v), false, false);
  return __uint_as_float(r2[0]) + __uint_as_float(r2[1]);
}

__device__ __forceinline__ int xoff(int wd, int r, int qt) {
  const int idx = wd * 3 + qt;
  if (r == 0) return L_O2 + idx * 1024;
  if (r == 1) return L_SLOT + idx * 1024;
  return (idx < 8 ? L_SLOT + 24576 : (idx < 16 ? 2 * L_SLOT + 24576 - 8192 : 4 * L_SLOT + 4096 - 16384)) + idx * 1024;
}

__global__ __launch_bounds__(512, 2) void gqa_fused(const __bf16* __restrict__ x, const __bf16* __restrict__ kc, const __bf16* __restrict__ vc,
                                                    const __bf16* __restrict__ wq, const __bf16* __restrict__ wo, __bf16* __restrict__ part) {
  extern __shared__ __attribute__((aligned(16))) char smem_raw[];
  ldsp sm = (ldsp)smem_raw;
  const unsigned smbase = (unsigned)(uintptr_t)sm;
  const int tid = threadIdx.x, l = tid & 63, c = l & 15, g = l >> 4, qq = c >> 2, pp = l & 3;
  const int w = __builtin_amdgcn_readfirstlane(tid >> 6);
  const int bid = blockIdx.x, kvh = bid & 7, jj = bid >> 3, b = jj >> 1, hp = jj & 1;
  STAMP(0);
  const int n0 = kvh * 512 + hp * 256;
  const i32x4 srd_k = make_srd(kc + (size_t)(b * HKV + kvh) * SK * DH, SK * DH * 2);
  const i32x4 srd_v = make_srd(vc + (size_t)(b * HKV + kvh) * SK * DH, SK * DH * 2);
  const unsigned kvo = (unsigned)(g * 256 + ((c ^ g) << 4));
  const unsigned vvo = (unsigned)(g * 256 + ((c ^ (2 * g)) << 4));

  if (w < 4) {
    f32x4 qacc[4][3];
    const i32x4 srd_x = make_srd(x + (size_t)b * SQ * DM, SQ * DM * 2);
    const i32x4 srd_w = make_srd(wq, DM * HQ * DH * 2);
#pragma unroll
    for (int i = 0; i < 12; ++i) {
      const int r = w * 12 + i;
      dma_w(smbase + L_X + r * 1024, (unsigned)((l ^ (r & 15)) * 16), srd_x, (unsigned)(r * 1024));
    }
    const unsigned ring = smbase + L_RING + w * (NRING * 4096);
    const unsigned voff_e = (unsigned)((l >> 3) * 8192 + (((l & 7) ^ (((l >> 4) & 1) << 1)) * 16));
    const unsigned voff_o = voff_e ^ 64u;
    const unsigned scol = (unsigned)((n0 + 64 * w) * 2);
#define WQ_ISSUE(s)                                                                                                        \
  do {                                                                                                                     \
    _Pragma("unroll") for (int i_ = 0; i_ < 4; ++i_)                                                                       \
        dma_w(ring + ((s) % NRING) * 4096 + i_ * 1024, (i_ & 1) ? voff_o : voff_e, srd_w, scol + (unsigned)((32 * (((s) + b) & 15) + 8 * i_) * 8192)); \
  } while (0)
    WQ_ISSUE(0); WQ_ISSUE(1); WQ_ISSUE(2); WQ_ISSUE(3);
    waitv<16>();
    STAMP(1); BAR(); STAMP(2);
    const int fA = ((qq >> 1) & 1) | ((g & 1) << 1);
    ldsp wqa = sm + L_RING + w * (NRING * 4096) + (8 * g + qq) * 128 + 8 * pp;
    ldsp xa = sm + L_X + c * 1024;
#pragma unroll
    for (int t = 0; t < 4; ++t)
#pragma unroll
      for (int qt = 0; qt < 3; ++qt) qacc[t][qt] = f32x4{0.f, 0.f, 0.f, 0.f};
    bf16x8 fw[2][4], fx[2][3];
#define A_READ(s, buf)                                                                                          \
  do {                                                                                                          \
    _Pragma("unroll") for (int t_ = 0; t_ < 4; ++t_) {                                                          \
      ldsp p_ = wqa + ((s) % NRING) * 4096 + ((t_ ^ fA) << 5);                                                  \
      fw[buf][t_] = lds_tr2(p_, p_ + 512);                                                                      \
    }                                                                                                           \
    _Pragma("unroll") for (int q_ = 0; q_ < 3; ++q_)                                                            \
        fx[buf][q_] = lds_rd128(xa + q_ * 16384 + ((((4 * (((s) + b) & 15)) | g) ^ c) << 4));                   \
  } while (0)
    waitv<12>();
    A_READ(0, 0);
#pragma unroll
    for (int s = 0; s < 16; ++s) {
      if (s + 4 < 16) WQ_ISSUE(s + 4);
      if (s == 12) {
#pragma unroll
        for (int i = 0; i < 8; ++i)
          dma_kv(smbase + (32 * w + 4 * i) * 256, (kvo ^ (unsigned)(64 * (i & 3))), srd_k, (unsigned)((128 * w + 4 * i) * 256));
      }
      if (s + 1 < 16) {
        const int rem = ((s + 4 < 16) ? (s + 4) : 15) - (s + 1);
        if (s < 12) waitv<12>(); else if (rem == 2) waitv<16>(); else if (rem == 1) waitv<12>(); else waitv<8>();
        A_READ(s + 1, (s + 1) & 1);
      }
      SCHED();
#pragma unroll
      for (int t = 0; t < 4; ++t)
#pragma unroll
        for (int qt = 0; qt < 3; ++qt) qacc[t][qt] = mfma(fw[s & 1][t], fx[s & 1][qt], qacc[t][qt]);
      SCHED();
    }
    STAMP(3); BAR(); STAMP(4);
    {
      const int h = w >> 1;
#pragma unroll
      for (int t = 0; t < 4; ++t)
#pragma unroll
        for (int qt = 0; qt < 3; ++qt) {
          const int c16 = 8 * (w & 1) + 2 * t + (g >> 1);
          *(LDS_AS bf16x4*)(sm + L_Q + (48 * h + 16 * qt + c) * 256 + ((c16 ^ c) << 4) + 8 * (g & 1)) = cvt4(qacc[t][qt]);
        }
    }
  } else {
    STAMP(1); BAR(); STAMP(2);
    STAMP(3); BAR(); STAMP(4);
  }
  const unsigned kvrow = (unsigned)((128 * (w >> 1) + 16 * (w & 1)) * 256);
#define K_ISSUE(j)                                                                                                      \
  do {                                                                                                                  \
    _Pragma("unroll") for (int i_ = 0; i_ < 4; ++i_)                                                                    \
        dma_kv(smbase + (j) * L_SLOT + (16 * w + 4 * i_) * 256, kvo ^ (unsigned)(64 * i_), srd_k, kvrow + (unsigned)((32 * (j) + 4 * i_) * 256)); \
  } while (0)
#define V_ISSUE(j, slot)                                                                                                \
  do {                                                                                                                  \
    _Pragma("unroll") for (int i_ = 0; i_ < 4; ++i_)                                                                    \
        dma_kv(smbase + (slot) * L_SLOT + (16 * w + 4 * i_) * 256, vvo ^ (unsigned)(128 * (i_ & 1)), srd_v, kvrow + (unsigned)((32 * (j) + 4 * i_) * 256)); \
  } while (0)
  const i32x4 srd_o = make_srd(wo + (size_t)n0 * DM, 256 * DM * 2);
  const unsigned wvo = (unsigned)((l >> 3) * 1024 + (((l & 7) ^ (g << 1)) << 4));
#define WO_ISSUE(u, slot)                                                                                               \
  do {                                                                                                                  \
    _Pragma("unroll") for (int i_ = 0; i_ < 4; ++i_)                                                                    \
        dma_w(smbase + (slot) * L_SLOT + w * 4096 + i_ * 1024, wvo, srd_o, (unsigned)(w * 128 + (32 * (((u) + b) & 7) + 8 * i_) * 1024)); \
  } while (0)
  K_ISSUE(1); K_ISSUE(2); K_ISSUE(3);
  waitv<12>();
  STAMP(5); BAR(); STAMP(6);

  const int h = w >> 2, kq = w & 3;
  f32x4 sacc[4][2][3];
  {
    bf16x8 qf[4][3];
#pragma unroll
    for (int s = 0; s < 4; ++s)
#pragma unroll
      for (int qt = 0; qt < 3; ++qt) qf[s][qt] = lds_rd128(sm + L_Q + (48 * h + 16 * qt + c) * 256 + ((((4 * s) | g) ^ c) << 4));
    ldsp ka = sm + (32 * kq + c) * 256;
#pragma unroll
    for (int j = 0; j < 4; ++j) {
      bf16x8 kf[2][4];
#pragma unroll
      for (int kt = 0; kt < 2; ++kt)
#pragma unroll
        for (int s = 0; s < 4; ++s) kf[kt][s] = lds_rd128(ka + j * L_SLOT + kt * 4096 + ((((4 * s) | g) ^ c) << 4));
      SCHED();
#pragma unroll
      for (int kt = 0; kt < 2; ++kt)
#pragma unroll
        for (int s = 0; s < 4; ++s)
#pragma unroll
          for (int qt = 0; qt < 3; ++qt)
            sacc[j][kt][qt] = mfma(kf[kt][s], qf[s][qt], s == 0 ? f32x4{0.f, 0.f, 0.f, 0.f} : sacc[j][kt][qt]);
      SCHED();
      if (j == 0) { waitv<8>(); }
      if (j == 1) { V_ISSUE(0, 0); V_ISSUE(1, 4); waitv<12>(); }
      if (j == 2) { V_ISSUE(2, 1); waitv<12>(); }
      if (j == 3) { V_ISSUE(3, 2); waitv<12>(); }
      BAR(); STAMP(7 + j);
    }
  }
  WO_ISSUE(0, 3);
  float mloc[3];
  bf16x8 pf[4][3];
#pragma unroll
  for (int qt = 0; qt < 3; ++qt) {
    float m = sacc[0][0][qt][0];
#pragma unroll
    for (int j = 0; j < 4; ++j)
#pragma unroll
      for (int kt = 0; kt < 2; ++kt)
#pragma unroll
        for (int r = 0; r < 4; ++r) m = fmaxf(m, sacc[j][kt][qt][r]);
    m = xmax_g(m) * SCL2E;
    mloc[qt] = m;
    const f32x2 sc2 = {SCL2E, SCL2E}, nm2 = {-m, -m};
#pragma unroll
    for (int j = 0; j < 4; ++j) {
#pragma unroll
      for (int kt = 0; kt < 2; ++kt) {
        f32x2 lo = {sacc[j][kt][qt][0], sacc[j][kt][qt][1]}, hi = {sacc[j][kt][qt][2], sacc[j][kt][qt][3]};
        lo = __builtin_elementwise_fma(lo, sc2, nm2);
        hi = __builtin_elementwise_fma(hi, sc2, nm2);
        sacc[j][kt][qt][0] = __builtin_amdgcn_exp2f(lo[0]); sacc[j][kt][qt][1] = __builtin_amdgcn_exp2f(lo[1]);
        sacc[j][kt][qt][2] = __builtin_amdgcn_exp2f(hi[0]); sacc[j][kt][qt][3] = __builtin_amdgcn_exp2f(hi[1]);
      }
      pf[j][qt] = cvt8(sacc[j][0][qt], sacc[j][1][qt]);
    }
  }
  STAMP(12);

  f32x4 oacc[8][3];
  f32x4 lacc[3];
  {
    const int fV = 4 * (g & 1) + qq;
    ldsp va[4];
#pragma unroll
    for (int r = 0; r < 4; ++r) va[r] = sm + (32 * kq + 4 * g + qq) * 256 + 8 * pp + (((2 * ((r + kq) & 3)) ^ fV) << 5);
    bf16x8 ones;
#pragma unroll
    for (int e = 0; e < 8; ++e) ones[e] = (__bf16)1.0f;
#pragma unroll
    for (int j = 0; j < 4; ++j) {
      const int slot = j == 0 ? 0 : (j == 1 ? 4 : j - 1);
      bf16x8 vf[8];
#pragma unroll
      for (int i = 0; i < 8; ++i) {
        ldsp p = (ldsp)((unsigned)(uintptr_t)va[i >> 1] ^ (unsigned)((i & 1) << 5)) + slot * L_SLOT;
        vf[i] = lds_tr2(p, p + 4096);
      }
      SCHED();
#pragma unroll
      for (int qt = 0; qt < 3; ++qt) lacc[qt] = mfma(ones, pf[j][qt], j == 0 ? f32x4{0.f, 0.f, 0.f, 0.f} : lacc[qt]);
#pragma unroll
      for (int i = 0; i < 8; ++i)
#pragma unroll
        for (int qt = 0; qt < 3; ++qt) oacc[i][qt] = mfma(vf[i], pf[j][qt], j == 0 ? f32x4{0.f, 0.f, 0.f, 0.f} : oacc[i][qt]);
      SCHED();
      if (j == 0) { waitv<12>(); }
      if (j == 1) { WO_ISSUE(1, 0); waitv<12>(); }
      if (j == 2) { waitv<8>(); }
      if (j == 3) {
        if (g == 0) {
#pragma unroll
          for (int qt = 0; qt < 3; ++qt) *(LDS_AS f32x2*)(sm + L_ST2 + (w * 48 + 16 * qt + c) * 8) = f32x2{mloc[qt], lacc[qt][0]};
        }
      }
      BAR(); STAMP(13 + j);
    }
  }
  {
    float sc[3];
#pragma unroll
    for (int qt = 0; qt < 3; ++qt) {
      f32x2 ml[4];
#pragma unroll
      for (int k = 0; k < 4; ++k) ml[k] = *(LDS_AS f32x2*)(sm + L_ST2 + ((h * 4 + k) * 48 + 16 * qt + c) * 8);
      const float m = fmaxf(fmaxf(ml[0][0], ml[1][0]), fmaxf(ml[2][0], ml[3][0]));
      float lt = 0.f;
#pragma unroll
      for (int k = 0; k < 4; ++k) lt += ml[k][1] * __builtin_amdgcn_exp2f(ml[k][0] - m);
      sc[qt] = __builtin_amdgcn_exp2f(mloc[qt] - m) / lt;
    }
#pragma unroll
    for (int r = 1; r < 4; ++r) {
      const int wd = h * 4 + ((kq + r) & 3);
#pragma unroll
      for (int qt = 0; qt < 3; ++qt)
        *(LDS_AS bf16x8*)(sm + xoff(wd, r - 1, qt) + l * 16) = cvt8(oacc[2 * r][qt] * sc[qt], oacc[2 * r + 1][qt] * sc[qt]);
    }
    BAR(); STAMP(17);
#pragma unroll
    for (int qt = 0; qt < 3; ++qt) {
      f32x4 o0 = oacc[0][qt] * sc[qt], o1 = oacc[1][qt] * sc[qt];
#pragma unroll
      for (int r = 0; r < 3; ++r) {
        bf16x8 t = lds_rd128(sm + xoff(w, r, qt) + l * 16);
#pragma unroll
        for (int e = 0; e < 4; ++e) { o0[e] += (float)t[e]; o1[e] += (float)t[4 + e]; }
      }
      *(LDS_AS bf16x8*)(sm + L_O2 + (w * 3 + qt) * 1024 + l * 16) = cvt8(o0, o1);
    }
  }
  BAR(); STAMP(18);
  WO_ISSUE(2, 1); WO_ISSUE(3, 4);

  f32x4 cacc[4][3];
  {
    bf16x8 of[8][3];
#pragma unroll
    for (int u = 0; u < 8; ++u)
#pragma unroll
      for (int qt = 0; qt < 3; ++qt) of[u][qt] = lds_rd128(sm + L_O2 + (((u + b) & 7) * 3 + qt) * 1024 + l * 16);
    const int fC = 2 * (g & 1) + (qq >> 1);
    ldsp wra = sm + w * 4096 + (4 * g + qq) * 128 + 8 * pp;
    bf16x8 wf[2][4];
#define CSLOT(u) (((u) & 3) == 0 ? 3 : (((u) & 3) == 1 ? 0 : (((u) & 3) == 2 ? 1 : 4)))
#define C_READ(u)                                                                                                   \
  do {                                                                                                              \
    _Pragma("unroll") for (int t_ = 0; t_ < 4; ++t_) {                                                              \
      ldsp p_ = wra + CSLOT(u) * L_SLOT + ((t_ ^ fC) << 5);                                                         \
      wf[(u) & 1][t_] = lds_tr2(p_, p_ + 2048);                                                                     \
    }                                                                                                               \
  } while (0)
    waitv<12>();
    C_READ(0);
#pragma unroll
    for (int u = 0; u < 8; ++u) {
      if (u >= 1 && u + 3 < 8) WO_ISSUE(u + 3, CSLOT(u + 3));
      if (u + 1 < 8) {
        const int last = (u + 3 < 8) ? (u + 3) : 7;
        const int rem = last - (u + 1);
        if (rem == 2) waitv<8>(); else if (rem == 1) waitv<4>(); else waitv<0>();
        C_READ(u + 1);
      }
      SCHED();
#pragma unroll
      for (int t = 0; t < 4; ++t)
#pragma unroll
        for (int qt = 0; qt < 3; ++qt) cacc[t][qt] = mfma(wf[u & 1][t], of[u][qt], u == 0 ? f32x4{0.f, 0.f, 0.f, 0.f} : cacc[t][qt]);
      SCHED();
    }
  }
  STAMP(19);
  bf16x8* pp8 = (bf16x8*)part + ((size_t)(b * 16 + kvh * 2 + hp) * 8 + w) * 6 * 64 + l;
#pragma unroll
  for (int a = 0; a < 2; ++a)
#pragma unroll
    for (int qt = 0; qt < 3; ++qt) pp8[(a * 3 + qt) * 64] = cvt8(cacc[2 * a][qt], cacc[2 * a + 1][qt]);
}

__global__ __launch_bounds__(192) void gqa_reduce(const __bf16* __restrict__ part, __bf16* __restrict__ out) {
  const int idx = blockIdx.x * 192 + threadIdx.x;
  const int b = idx / 3072, rem = idx - b * 3072;
  const bf16x8* p = (const bf16x8*)part + (size_t)b * 16 * 3072 + rem;
  bf16x8 v[16];
#pragma unroll
  for (int s = 0; s < 16; ++s) v[s] = p[(size_t)s * 3072];
  f32x4 a0 = {0.f, 0.f, 0.f, 0.f}, a1 = {0.f, 0.f, 0.f, 0.f};
#pragma unroll
  for (int s = 0; s < 16; ++s)
#pragma unroll
    for (int e = 0; e < 4; ++e) { a0[e] += (float)v[s][e]; a1[e] += (float)v[s][4 + e]; }
  const int lane = rem & 63, tq = rem >> 6, qt = tq % 3, wa = tq / 3;
  const int q = 16 * qt + (lane & 15), n = 32 * wa + 4 * (lane >> 4);
  __bf16* o = out + ((size_t)b * SQ + q) * DM + n;
  *(bf16x4*)o = cvt4(a0);
  *(bf16x4*)(o + 16) = cvt4(a1);
}

extern "C" void kernel_launch(void* const* d_in, const int* in_sizes, int n_in, void* d_out, int out_size, void* d_ws, size_t ws_size,
                              hipStream_t stream) {
  const __bf16* x = (const __bf16*)d_in[0];
  const __bf16* kc = (const __bf16*)d_in[1];
  const __bf16* vc = (const __bf16*)d_in[2];
  const __bf16* wq = (const __bf16*)d_in[3];
  const __bf16* wo = (const __bf16*)d_in[4];
  __bf16* part = (__bf16*)d_ws;
  (void)hipFuncSetAttribute((const void*)gqa_fused, hipFuncAttributeMaxDynamicSharedMemorySize, LDS_BYTES);
  hipLaunchKernelGGL(gqa_fused, dim3(256), dim3(512), LDS_BYTES, stream, x, kc, vc, wq, wo, part);
  hipLaunchKernelGGL(gqa_reduce, dim3(256), dim3(192), 0, stream, (const __bf16*)part, (__bf16*)d_out);
}
```

```cpp
#include <hip/hip_runtime.h>
#include <hip/hip_bf16.h>
#include <stdint.h>

typedef __bf16 bf16x8 __attribute__((ext_vector_type(8)));
typedef __bf16 bf16x4 __attribute__((ext_vector_type(4)));
typedef float f32x4 __attribute__((ext_vector_type(4)));
typedef float f32x2 __attribute__((ext_vector_type(2)));
typedef int i32x4 __attribute__((ext_vector_type(4)));
typedef unsigned u32x4 __attribute__((ext_vector_type(4)));
#define LDS_AS __attribute__((address_space(3)))
typedef LDS_AS char* ldsp;

constexpr int B_ = 16, SQ = 48, DM = 512, HQ = 32, HKV = 8, SK = 512, DH = 128;
constexpr float SCL2E = 0.08838834764831845f * 1.4426950408889634f;

constexpr int L_SLOT = 32768;
constexpr int L_RING = 32768;
constexpr int L_X = 114688;
constexpr int L_Q = 131072;
constexpr int L_STAT = 155648;
constexpr int L_P = 65536;
constexpr int L_LSUM = 0;
constexpr int L_O = 8192;
constexpr int L_WO = 32768;
constexpr int L_ST2 = 4 * 32768;
constexpr int L_O2 = 2 * 32768;
constexpr int LDS_BYTES = 163840;
constexpr int NRING = 5;
constexpr float RESC_T = 16.0f;

#define BAR() asm volatile("s_waitcnt lgkmcnt(0)\n\ts_barrier" ::: "memory")
#define SCHED() __builtin_amdgcn_sched_barrier(0)
#define STAMP(i)
template <int N> __device__ __forceinline__ void waitv() { asm volatile("s_waitcnt vmcnt(%0)" ::"i"(N) : "memory"); }

__device__ __forceinline__ void dma16(unsigned ldsaddr, unsigned voff, i32x4 srd, unsigned soff) {
  asm volatile("s_mov_b32 m0, %0\n\ts_nop 0\n\tbuffer_load_dwordx4 %1, %2, %3 offen lds" ::"s"(ldsaddr), "v"(voff), "s"(srd), "s"(soff) : "memory");
}
__device__ __forceinline__ void dma16nt(unsigned ldsaddr, unsigned voff, i32x4 srd, unsigned soff) {
  asm volatile("s_mov_b32 m0, %0\n\ts_nop 0\n\tbuffer_load_dwordx4 %1, %2, %3 offen nt lds" ::"s"(ldsaddr), "v"(voff), "s"(srd), "s"(soff) : "memory");
}
#define KV_NT 1
#define W_NT 0
#define dma_kv dma16nt
#define dma_w dma16
__device__ __forceinline__ i32x4 make_srd(const void* p, unsigned bytes) {
  unsigned long long a = (unsigned long long)p;
  i32x4 s;
  s[0] = (int)(unsigned)a; s[1] = (int)((unsigned)(a >> 32) & 0xffffu); s[2] = (int)bytes; s[3] = 0x00020000;
  return s;
}
__device__ __forceinline__ bf16x8 lds_rd128(ldsp p) { return *(LDS_AS bf16x8*)p; }
__device__ __forceinline__ bf16x8 lds_tr2(ldsp p0, ldsp p1) {
  bf16x4 a = __builtin_amdgcn_ds_read_tr16_b64_v4bf16((LDS_AS bf16x4*)p0);
  bf16x4 b = __builtin_amdgcn_ds_read_tr16_b64_v4bf16((LDS_AS bf16x4*)p1);
  return __builtin_shufflevector(a, b, 0, 1, 2, 3, 4, 5, 6, 7);
}
__device__ __forceinline__ f32x4 mfma(bf16x8 a, bf16x8 b, f32x4 c) { return __builtin_amdgcn_mfma_f32_16x16x32_bf16(a, b, c, 0, 0, 0); }
__device__ __forceinline__ bf16x4 cvt4(f32x4 v) {
  bf16x4 r; r[0] = (__bf16)v[0]; r[1] = (__bf16)v[1]; r[2] = (__bf16)v[2]; r[3] = (__bf16)v[3]; return r;
}
__device__ __forceinline__ bf16x8 cvt8(f32x4 a, f32x4 b) { return __builtin_shufflevector(cvt4(a), cvt4(b), 0, 1, 2, 3, 4, 5, 6, 7); }
__device__ __forceinline__ float xmax_g(float v) {
  auto r = __builtin_amdgcn_permlane16_swap(__float_as_uint(v), __float_as_uint(v), false, false);
  v = fmaxf(__uint_as_float(r[0]), __uint_as_float(r[1]));
  auto r2 = __builtin_amdgcn_permlane32_swap(__float_as_uint(v), __float_as_uint(v), false, false);
  return fmaxf(__uint_as_float(r2[0]), __uint_as_float(r2[1]));
}
__device__ __forceinline__ float xsum_g(float v) {
  auto r = __builtin_amdgcn_permlane16_swap(__float_as_uint(v), __float_as_uint(v), false, false);
  v = __uint_as_float(r[0]) + __uint_as_float(r[1]);
  auto r2 = __builtin_amdgcn_permlane32_swap(__float_as_uint(v), __float_as_uint(v), false, false);
  return __uint_as_float(r2[0]) + __uint_as_float(r2[1]);
}

__device__ __forceinline__ int xoff(int wd, int r, int qt) {
  const int idx = wd * 3 + qt;
  if (r == 0) return L_O2 + idx * 1024;
  if (r == 1) return L_SLOT + idx * 1024;
  return (idx < 8 ? L_SLOT + 24576 : (idx < 16 ? 2 * L_SLOT + 24576 - 8192 : 4 * L_SLOT + 4096 - 16384)) + idx * 1024;
}

__global__ __launch_bounds__(512, 2) void gqa_fused(const __bf16* __restrict__ x, const __bf16* __restrict__ kc, const __bf16* __restrict__ vc,
                                                    const __bf16* __restrict__ wq, const __bf16* __restrict__ wo, __bf16* __restrict__ part) {
  extern __shared__ __attribute__((aligned(16))) char smem_raw[];
  ldsp sm = (ldsp)smem_raw;
  const unsigned smbase = (unsigned)(uintptr_t)sm;
  const int tid = threadIdx.x, l = tid & 63, c = l & 15, g = l >> 4, qq = c >> 2, pp = l & 3;
  const int w = __builtin_amdgcn_readfirstlane(tid >> 6);
  const int bid = blockIdx.x, kvh = bid & 7, jj = bid >> 3, b = jj >> 1, hp = jj & 1;
  STAMP(0);
  const int n0 = kvh * 512 + hp * 256;
  const i32x4 srd_k = make_srd(kc + (size_t)(b * HKV + kvh) * SK * DH, SK * DH * 2);
  const i32x4 srd_v = make_srd(vc + (size_t)(b * HKV + kvh) * SK * DH, SK * DH * 2);
  const unsigned kvo = (unsigned)(g * 256 + ((c ^ g) << 4));
  const unsigned vvo = (unsigned)(g * 256 + ((c ^ (2 * g)) << 4));

  if (w < 4) {
    f32x4 qacc[4][3];
    const i32x4 srd_x = make_srd(x + (size_t)b * SQ * DM, SQ * DM * 2);
    const i32x4 srd_w = make_srd(wq, DM * HQ * DH * 2);
#pragma unroll
    for (int i = 0; i < 12; ++i) {
      const int r = w * 12 + i;
      dma_w(smbase + L_X + r * 1024, (unsigned)((l ^ (r & 15)) * 16), srd_x, (unsigned)(r * 1024));
    }
    const unsigned ring = smbase + L_RING + w * (NRING * 4096);
    const unsigned voff_e = (unsigned)((l >> 3) * 8192 + (((l & 7) ^ (((l >> 4) & 1) << 1)) * 16));
    const unsigned voff_o = voff_e ^ 64u;
    const unsigned scol = (unsigned)((n0 + 64 * w) * 2);
#define WQ_ISSUE(s)                                                                                                        \
  do {                                                                                                                     \
    _Pragma("unroll") for (int i_ = 0; i_ < 4; ++i_)                                                                       \
        dma_w(ring + ((s) % NRING) * 4096 + i_ * 1024, (i_ & 1) ? voff_o : voff_e, srd_w, scol + (unsigned)((32 * (((s) + b) & 15) + 8 * i_) * 8192)); \
  } while (0)
    WQ_ISSUE(0); WQ_ISSUE(1); WQ_ISSUE(2); WQ_ISSUE(3);
    waitv<16>();
    STAMP(1); BAR(); STAMP(2);
    const int fA = ((qq >> 1) & 1) | ((g & 1) << 1);
    ldsp wqa = sm + L_RING + w * (NRING * 4096) + (8 * g + qq) * 128 + 8 * pp;
    ldsp xa = sm + L_X + c * 1024;
#pragma unroll
    for (int t = 0; t < 4; ++t)
#pragma unroll
      for (int qt = 0; qt < 3; ++qt) qacc[t][qt] = f32x4{0.f, 0.f, 0.f, 0.f};
    bf16x8 fw[2][4], fx[2][3];
#define A_READ(s, buf)                                                                                          \
  do {                                                                                                          \
    _Pragma("unroll") for (int t_ = 0; t_ < 4; ++t_) {                                                          \
      ldsp p_ = wqa + ((s) % NRING) * 4096 + ((t_ ^ fA) << 5);                                                  \
      fw[buf][t_] = lds_tr2(p_, p_ + 512);                                                                      \
    }                                                                                                           \
    _Pragma("unroll") for (int q_ = 0; q_ < 3; ++q_)                                                            \
        fx[buf][q_] = lds_rd128(xa + q_ * 16384 + ((((4 * (((s) + b) & 15)) | g) ^ c) << 4));                   \
  } while (0)
    waitv<12>();
    A_READ(0, 0);
#pragma unroll
    for (int s = 0; s < 16; ++s) {
      if (s + 4 < 16) WQ_ISSUE(s + 4);
      if (s == 12) {
#pragma unroll
        for (int i = 0; i < 8; ++i)
          dma_kv(smbase + (32 * w + 4 * i) * 256, (kvo ^ (unsigned)(64 * (i & 3))), srd_k, (unsigned)((128 * w + 4 * i) * 256));
      }
      if (s + 1 < 16) {
        const int rem = ((s + 4 < 16) ? (s + 4) : 15) - (s + 1);
        if (s < 12) waitv<12>(); else if (rem == 2) waitv<16>(); else if (rem == 1) waitv<12>(); else waitv<8>();
        A_READ(s + 1, (s + 1) & 1);
      }
      SCHED();
#pragma unroll
      for (int t = 0; t < 4; ++t)
#pragma unroll
        for (int qt = 0; qt < 3; ++qt) qacc[t][qt] = mfma(fw[s & 1][t], fx[s & 1][qt], qacc[t][qt]);
      SCHED();
    }
    STAMP(3); BAR(); STAMP(4);
    {
      const int h = w >> 1;
#pragma unroll
      for (int t = 0; t < 4; ++t)
#pragma unroll
        for (int qt = 0; qt < 3; ++qt) {
          const int c16 = 8 * (w & 1) + 2 * t + (g >> 1);
          *(LDS_AS bf16x4*)(sm + L_Q + (48 * h + 16 * qt + c) * 256 + ((c16 ^ c) << 4) + 8 * (g & 1)) = cvt4(qacc[t][qt]);
        }
    }
  } else {
    STAMP(1); BAR(); STAMP(2);
    STAMP(3); BAR(); STAMP(4);
  }
  const unsigned kvrow = (unsigned)((128 * (w >> 1) + 16 * (w & 1)) * 256);
#define K_ISSUE(j, slot)                                                                                                \
  do {                                                                                                                  \
    _Pragma("unroll") for (int i_ = 0; i_ < 4; ++i_)                                                                    \
        dma_kv(smbase + (slot) * L_SLOT + (16 * w + 4 * i_) * 256, kvo ^ (unsigned)(64 * i_), srd_k, kvrow + (unsigned)((32 * (j) + 4 * i_) * 256)); \
  } while (0)
#define V_ISSUE(j, slot)                                                                                                \
  do {                                                                                                                  \
    _Pragma("unroll") for (int i_ = 0; i_ < 4; ++i_)                                                                    \
        dma_kv(smbase + (slot) * L_SLOT + (16 * w + 4 * i_) * 256, vvo ^ (unsigned)(128 * (i_ & 1)), srd_v, kvrow + (unsigned)((32 * (j) + 4 * i_) * 256)); \
  } while (0)
  const i32x4 srd_o = make_srd(wo + (size_t)n0 * DM, 256 * DM * 2);
  const unsigned wvo = (unsigned)((l >> 3) * 1024 + (((l & 7) ^ (g << 1)) << 4));
#define WO_ISSUE(u, slot)                                                                                               \
  do {                                                                                                                  \
    _Pragma("unroll") for (int i_ = 0; i_ < 4; ++i_)                                                                    \
        dma_w(smbase + (slot) * L_SLOT + w * 4096 + i_ * 1024, wvo, srd_o, (unsigned)(w * 128 + (32 * (((u) + b) & 7) + 8 * i_) * 1024)); \
  } while (0)
  K_ISSUE(1, 1); V_ISSUE(0, 2); K_ISSUE(2, 3);
  waitv<12>();
  STAMP(5); BAR(); STAMP(6);

  const int h = w >> 2, kq = w & 3;
  float mloc[3];
  f32x4 oacc[8][3];
  f32x2 lacc[3];
  {
    bf16x8 qf[4][3];
#pragma unroll
    for (int s = 0; s < 4; ++s)
#pragma unroll
      for (int qt = 0; qt < 3; ++qt) qf[s][qt] = lds_rd128(sm + L_Q + (48 * h + 16 * qt + c) * 256 + ((((4 * s) | g) ^ c) << 4));
    ldsp ka = sm + (32 * kq + c) * 256;
    const int fV = 4 * (g & 1) + qq;
    ldsp va[4];
#pragma unroll
    for (int r = 0; r < 4; ++r) va[r] = sm + (32 * kq + 4 * g + qq) * 256 + 8 * pp + (((2 * ((r + kq) & 3)) ^ fV) << 5);
#pragma unroll
    for (int i = 0; i < 8; ++i)
#pragma unroll
      for (int qt = 0; qt < 3; ++qt) oacc[i][qt] = f32x4{0.f, 0.f, 0.f, 0.f};
#pragma unroll
    for (int qt = 0; qt < 3; ++qt) lacc[qt] = f32x2{0.f, 0.f};
    bf16x8 pprev[3];
#pragma unroll
    for (int it = 0; it < 5; ++it) {
      constexpr int kslot[4] = {0, 1, 3, 4}, vslot[4] = {2, 0, 1, 2};
      f32x4 sacc[2][3];
      if (it < 4) {
        if (it == 1) { V_ISSUE(1, 0); K_ISSUE(3, 4); }
        if (it == 2) { V_ISSUE(2, 1); V_ISSUE(3, 2); }
        if (it == 3) { WO_ISSUE(0, 3); WO_ISSUE(1, 0); }
        unsigned kso = (unsigned)(kslot[it] * L_SLOT);
        asm volatile("" : "+s"(kso));
#pragma unroll
        for (int kt = 0; kt < 2; ++kt) {
          bf16x8 kf[4];
#pragma unroll
          for (int s = 0; s < 4; ++s) kf[s] = lds_rd128(ka + kso + kt * 4096 + ((((4 * s) | g) ^ c) << 4));
          SCHED();
#pragma unroll
          for (int s = 0; s < 4; ++s)
#pragma unroll
            for (int qt = 0; qt < 3; ++qt)
              sacc[kt][qt] = mfma(kf[s], qf[s][qt], s == 0 ? f32x4{0.f, 0.f, 0.f, 0.f} : sacc[kt][qt]);
        }
        float tm[3];
#pragma unroll
        for (int qt = 0; qt < 3; ++qt) {
          float m = fmaxf(fmaxf(fmaxf(sacc[0][qt][0], sacc[0][qt][1]), fmaxf(sacc[0][qt][2], sacc[0][qt][3])),
                          fmaxf(fmaxf(sacc[1][qt][0], sacc[1][qt][1]), fmaxf(sacc[1][qt][2], sacc[1][qt][3])));
          tm[qt] = xmax_g(m) * SCL2E;
        }
        if (it == 0) {
#pragma unroll
          for (int qt = 0; qt < 3; ++qt) mloc[qt] = tm[qt];
        } else {
          const bool grow = fmaxf(fmaxf(tm[0] - mloc[0], tm[1] - mloc[1]), tm[2] - mloc[2]) > RESC_T;
          if (__builtin_expect(__any(grow), 0)) {
#pragma unroll
            for (int qt = 0; qt < 3; ++qt) {
              const float mn = fmaxf(mloc[qt], tm[qt]);
              const float al = __builtin_amdgcn_exp2f(mloc[qt] - mn);
              mloc[qt] = mn;
#pragma unroll
              for (int i = 0; i < 8; ++i) oacc[i][qt] *= al;
              lacc[qt] *= al;
              u32x4 pu = __builtin_bit_cast(u32x4, pprev[qt]);
              f32x4 plo, phi;
#pragma unroll
              for (int e = 0; e < 2; ++e) {
                plo[2 * e] = __uint_as_float(pu[e] << 16) * al; plo[2 * e + 1] = __uint_as_float(pu[e] & 0xffff0000u) * al;
                phi[2 * e] = __uint_as_float(pu[2 + e] << 16) * al; phi[2 * e + 1] = __uint_as_float(pu[2 + e] & 0xffff0000u) * al;
              }
              pprev[qt] = cvt8(plo, phi);
            }
          }
        }
      }
      SCHED();
      bf16x8 pnew[3];
      unsigned vso = (unsigned)(vslot[it > 0 ? it - 1 : 0] * L_SLOT);
      asm volatile("" : "+s"(vso));
#pragma unroll
      for (int ch = 0; ch < 3; ++ch) {
        if (it > 0) {
#pragma unroll
          for (int i = (ch * 8) / 3; i < ((ch + 1) * 8) / 3; ++i) {
            ldsp p = (ldsp)(((unsigned)(uintptr_t)va[i >> 1] ^ (unsigned)((i & 1) << 5)) + vso);
            bf16x8 vf = lds_tr2(p, p + 4096);
#pragma unroll
            for (int qt = 0; qt < 3; ++qt) oacc[i][qt] = mfma(vf, pprev[qt], oacc[i][qt]);
          }
        }
        if (it < 4) {
          const int qt = ch;
          const f32x2 sc2 = {SCL2E, SCL2E}, nm2 = {-mloc[qt], -mloc[qt]};
#pragma unroll
          for (int kt = 0; kt < 2; ++kt) {
            f32x2 lo = {sacc[kt][qt][0], sacc[kt][qt][1]}, hi = {sacc[kt][qt][2], sacc[kt][qt][3]};
            lo = __builtin_elementwise_fma(lo, sc2, nm2);
            hi = __builtin_elementwise_fma(hi, sc2, nm2);
            sacc[kt][qt][0] = __builtin_amdgcn_exp2f(lo[0]); sacc[kt][qt][1] = __builtin_amdgcn_exp2f(lo[1]);
            sacc[kt][qt][2] = __builtin_amdgcn_exp2f(hi[0]); sacc[kt][qt][3] = __builtin_amdgcn_exp2f(hi[1]);
            lacc[qt] += f32x2{sacc[kt][qt][0], sacc[kt][qt][1]} + f32x2{sacc[kt][qt][2], sacc[kt][qt][3]};
          }
          pnew[qt] = cvt8(sacc[0][qt], sacc[1][qt]);
        }
        SCHED();
      }
      if (it < 4) {
#pragma unroll
        for (int qt = 0; qt < 3; ++qt) { pprev[qt] = pnew[qt]; asm volatile("" : "+v"(pprev[qt])); }
      }
      if (it < 3) waitv<4>();
      if (it == 3) waitv<8>();
      if (it == 4) {
#pragma unroll
        for (int qt = 0; qt < 3; ++qt) {
          const float ls = xsum_g(lacc[qt][0] + lacc[qt][1]);
          if (g == 0) *(LDS_AS f32x2*)(sm + L_ST2 + (w * 48 + 16 * qt + c) * 8) = f32x2{mloc[qt], ls};
        }
      }
      BAR(); STAMP(7 + it);
    }
  }
  int tz = threadIdx.x;
  asm volatile("" : "+v"(tz));
  const int l2 = tz & 63, c2 = l2 & 15, g2 = l2 >> 4, qq2 = c2 >> 2, pp2 = l2 & 3;
#define l l2
#define c c2
#define g g2
#define qq qq2
#define pp pp2
  {
    float sc[3];
#pragma unroll
    for (int qt = 0; qt < 3; ++qt) {
      f32x2 ml[4];
#pragma unroll
      for (int k = 0; k < 4; ++k) ml[k] = *(LDS_AS f32x2*)(sm + L_ST2 + ((h * 4 + k) * 48 + 16 * qt + c) * 8);
      const float m = fmaxf(fmaxf(ml[0][0], ml[1][0]), fmaxf(ml[2][0], ml[3][0]));
      float lt = 0.f;
#pragma unroll
      for (int k = 0; k < 4; ++k) lt += ml[k][1] * __builtin_amdgcn_exp2f(ml[k][0] - m);
      sc[qt] = __builtin_amdgcn_exp2f(mloc[qt] - m) / lt;
    }
#pragma unroll
    for (int r = 1; r < 4; ++r) {
      const int wd = h * 4 + ((kq + r) & 3);
#pragma unroll
      for (int qt = 0; qt < 3; ++qt)
        *(LDS_AS bf16x8*)(sm + xoff(wd, r - 1, qt) + l * 16) = cvt8(oacc[2 * r][qt] * sc[qt], oacc[2 * r + 1][qt] * sc[qt]);
    }
    BAR(); STAMP(17);
#pragma unroll
    for (int qt = 0; qt < 3; ++qt) {
      f32x4 o0 = oacc[0][qt] * sc[qt], o1 = oacc[1][qt] * sc[qt];
#pragma unroll
      for (int r = 0; r < 3; ++r) {
        bf16x8 t = lds_rd128(sm + xoff(w, r, qt) + l * 16);
#pragma unroll
        for (int e = 0; e < 4; ++e) { o0[e] += (float)t[e]; o1[e] += (float)t[4 + e]; }
      }
      *(LDS_AS bf16x8*)(sm + L_O2 + (w * 3 + qt) * 1024 + l * 16) = cvt8(o0, o1);
    }
  }
  BAR(); STAMP(18);
  WO_ISSUE(2, 1); WO_ISSUE(3, 4);

  f32x4 cacc[4][3];
  {
    bf16x8 of[8][3];
#pragma unroll
    for (int u = 0; u < 8; ++u)
#pragma unroll
      for (int qt = 0; qt < 3; ++qt) of[u][qt] = lds_rd128(sm + L_O2 + (((u + b) & 7) * 3 + qt) * 1024 + l * 16);
    const int fC = 2 * (g & 1) + (qq >> 1);
    ldsp wra = sm + w * 4096 + (4 * g + qq) * 128 + 8 * pp;
    bf16x8 wf[2][4];
#define CSLOT(u) (((u) & 3) == 0 ? 3 : (((u) & 3) == 1 ? 0 : (((u) & 3) == 2 ? 1 : 4)))
#define C_READ(u)                                                                                                   \
  do {                                                                                                              \
    _Pragma("unroll") for (int t_ = 0; t_ < 4; ++t_) {                                                              \
      ldsp p_ = wra + CSLOT(u) * L_SLOT + ((t_ ^ fC) << 5);                                                         \
      wf[(u) & 1][t_] = lds_tr2(p_, p_ + 2048);                                                                     \
    }                                                                                                               \
  } while (0)
    waitv<12>();
    C_READ(0);
#pragma unroll
    for (int u = 0; u < 8; ++u) {
      if (u >= 1 && u + 3 < 8) WO_ISSUE(u + 3, CSLOT(u + 3));
      if (u + 1 < 8) {
        const int last = (u + 3 < 8) ? (u + 3) : 7;
        const int rem = last - (u + 1);
        if (rem == 2) waitv<8>(); else if (rem == 1) waitv<4>(); else waitv<0>();
        C_READ(u + 1);
      }
      SCHED();
#pragma unroll
      for (int t = 0; t < 4; ++t)
#pragma unroll
        for (int qt = 0; qt < 3; ++qt) cacc[t][qt] = mfma(wf[u & 1][t], of[u][qt], u == 0 ? f32x4{0.f, 0.f, 0.f, 0.f} : cacc[t][qt]);
      SCHED();
    }
  }
  STAMP(19);
  bf16x8* pp8 = (bf16x8*)part + ((size_t)(b * 16 + kvh * 2 + hp) * 8 + w) * 6 * 64 + l;
#pragma unroll
  for (int a = 0; a < 2; ++a)
#pragma unroll
    for (int qt = 0; qt < 3; ++qt) pp8[(a * 3 + qt) * 64] = cvt8(cacc[2 * a][qt], cacc[2 * a + 1][qt]);
}

#undef l
#undef c
#undef g
#undef qq
#undef pp
__global__ __launch_bounds__(192) void gqa_reduce(const __bf16* __restrict__ part, __bf16* __restrict__ out) {
  const int idx = blockIdx.x * 192 + threadIdx.x;
  const int b = idx / 3072, rem = idx - b * 3072;
  const bf16x8* p = (const bf16x8*)part + (size_t)b * 16 * 3072 + rem;
  bf16x8 v[16];
#pragma unroll
  for (int s = 0; s < 16; ++s) v[s] = p[(size_t)s * 3072];
  f32x4 a0 = {0.f, 0.f, 0.f, 0.f}, a1 = {0.f, 0.f, 0.f, 0.f};
#pragma unroll
  for (int s = 0; s < 16; ++s)
#pragma unroll
    for (int e = 0; e < 4; ++e) { a0[e] += (float)v[s][e]; a1[e] += (float)v[s][4 + e]; }
  const int lane = rem & 63, tq = rem >> 6, qt = tq % 3, wa = tq / 3;
  const int q = 16 * qt + (lane & 15), n = 32 * wa + 4 * (lane >> 4);
  __bf16* o = out + ((size_t)b * SQ + q) * DM + n;
  *(bf16x4*)o = cvt4(a0);
  *(bf16x4*)(o + 16) = cvt4(a1);
}

extern "C" void kernel_launch(void* const* d_in, const int* in_sizes, int n_in, void* d_out, int out_size, void* d_ws, size_t ws_size,
                              hipStream_t stream) {
  const __bf16* x = (const __bf16*)d_in[0];
  const __bf16* kc = (const __bf16*)d_in[1];
  const __bf16* vc = (const __bf16*)d_in[2];
  const __bf16* wq = (const __bf16*)d_in[3];
  const __bf16* wo = (const __bf16*)d_in[4];
  __bf16* part = (__bf16*)d_ws;
  (void)hipFuncSetAttribute((const void*)gqa_fused, hipFuncAttributeMaxDynamicSharedMemorySize, LDS_BYTES);
  hipLaunchKernelGGL(gqa_fused, dim3(256), dim3(512), LDS_BYTES, stream, x, kc, vc, wq, wo, part);
  hipLaunchKernelGGL(gqa_reduce, dim3(256), dim3(192), 0, stream, (const __bf16*)part, (__bf16*)d_out);
}
```

```cpp
#include <hip/hip_runtime.h>
#include <hip/hip_bf16.h>
#include <stdint.h>

typedef __bf16 bf16x8 __attribute__((ext_vector_type(8)));
typedef __bf16 bf16x4 __attribute__((ext_vector_type(4)));
typedef float f32x4 __attribute__((ext_vector_type(4)));
typedef float f32x2 __attribute__((ext_vector_type(2)));
typedef int i32x4 __attribute__((ext_vector_type(4)));
typedef unsigned u32x4 __attribute__((ext_vector_type(4)));
#define LDS_AS __attribute__((address_space(3)))
typedef LDS_AS char* ldsp;

constexpr int B_ = 16, SQ = 48, DM = 512, HQ = 32, HKV = 8, SK = 512, DH = 128;
constexpr float SCL2E = 0.08838834764831845f * 1.4426950408889634f;

constexpr int L_SLOT = 32768;
constexpr int L_RING = 32768;
constexpr int L_X = 114688;
constexpr int L_Q = 131072;
constexpr int L_STAT = 155648;
constexpr int L_P = 65536;
constexpr int L_LSUM = 0;
constexpr int L_O = 8192;
constexpr int L_WO = 32768;
constexpr int L_ST2 = 32768 + 24576;
constexpr int L_O2 = 2 * 32768;
constexpr int LDS_BYTES = 163840;
constexpr int NRING = 5;
constexpr float RESC_T = 16.0f;

#define BAR() asm volatile("s_waitcnt lgkmcnt(0)\n\ts_barrier" ::: "memory")
#define SCHED() __builtin_amdgcn_sched_barrier(0)
#define STAMP(i)
template <int N> __device__ __forceinline__ void waitv() { asm volatile("s_waitcnt vmcnt(%0)" ::"i"(N) : "memory"); }

__device__ __forceinline__ void dma16(unsigned ldsaddr, unsigned voff, i32x4 srd, unsigned soff) {
  asm volatile("s_mov_b32 m0, %0\n\ts_nop 0\n\tbuffer_load_dwordx4 %1, %2, %3 offen lds" ::"s"(ldsaddr), "v"(voff), "s"(srd), "s"(soff) : "memory");
}
__device__ __forceinline__ void dma16nt(unsigned ldsaddr, unsigned voff, i32x4 srd, unsigned soff) {
  asm volatile("s_mov_b32 m0, %0\n\ts_nop 0\n\tbuffer_load_dwordx4 %1, %2, %3 offen nt lds" ::"s"(ldsaddr), "v"(voff), "s"(srd), "s"(soff) : "memory");
}
#define KV_NT 1
#define W_NT 0
#define dma_kv dma16nt
#define dma_w dma16
__device__ __forceinline__ i32x4 make_srd(const void* p, unsigned bytes) {
  unsigned long long a = (unsigned long long)p;
  i32x4 s;
  s[0] = (int)(unsigned)a; s[1] = (int)((unsigned)(a >> 32) & 0xffffu); s[2] = (int)bytes; s[3] = 0x00020000;
  return s;
}
__device__ __forceinline__ bf16x8 lds_rd128(ldsp p) { return *(LDS_AS bf16x8*)p; }
__device__ __forceinline__ bf16x8 lds_tr2(ldsp p0, ldsp p1) {
  bf16x4 a = __builtin_amdgcn_ds_read_tr16_b64_v4bf16((LDS_AS bf16x4*)p0);
  bf16x4 b = __builtin_amdgcn_ds_read_tr16_b64_v4bf16((LDS_AS bf16x4*)p1);
  return __builtin_shufflevector(a, b, 0, 1, 2, 3, 4, 5, 6, 7);
}
__device__ __forceinline__ f32x4 mfma(bf16x8 a, bf16x8 b, f32x4 c) { return __builtin_amdgcn_mfma_f32_16x16x32_bf16(a, b, c, 0, 0, 0); }
__device__ __forceinline__ bf16x4 cvt4(f32x4 v) {
  bf16x4 r; r[0] = (__bf16)v[0]; r[1] = (__bf16)v[1]; r[2] = (__bf16)v[2]; r[3] = (__bf16)v[3]; return r;
}
__device__ __forceinline__ bf16x8 cvt8(f32x4 a, f32x4 b) { return __builtin_shufflevector(cvt4(a), cvt4(b), 0, 1, 2, 3, 4, 5, 6, 7); }
__device__ __forceinline__ float xmax_g(float v) {
  auto r = __builtin_amdgcn_permlane16_swap(__float_as_uint(v), __float_as_uint(v), false, false);
  v = fmaxf(__uint_as_float(r[0]), __uint_as_float(r[1]));
  auto r2 = __builtin_amdgcn_permlane32_swap(__float_as_uint(v), __float_as_uint(v), false, false);
  return fmaxf(__uint_as_float(r2[0]), __uint_as_float(r2[1]));
}
__device__ __forceinline__ float xsum_g(float v) {
  auto r = __builtin_amdgcn_permlane16_swap(__float_as_uint(v), __float_as_uint(v), false, false);
  v = __uint_as_float(r[0]) + __uint_as_float(r[1]);
  auto r2 = __builtin_amdgcn_permlane32_swap(__float_as_uint(v), __float_as_uint(v), false, false);
  return __uint_as_float(r2[0]) + __uint_as_float(r2[1]);
}

__device__ __forceinline__ int xoff(int wd, int r, int qt) {
  return (r == 0 ? L_O2 : (r == 1 ? L_SLOT : 3 * L_SLOT)) + (wd * 3 + qt) * 1024;
}

__global__ __launch_bounds__(512, 2) void gqa_fused(const __bf16* __restrict__ x, const __bf16* __restrict__ kc, const __bf16* __restrict__ vc,
                                                    const __bf16* __restrict__ wq, const __bf16* __restrict__ wo, __bf16* __restrict__ part) {
  extern __shared__ __attribute__((aligned(16))) char smem_raw[];
  ldsp sm = (ldsp)smem_raw;
  const unsigned smbase = (unsigned)(uintptr_t)sm;
  const int tid = threadIdx.x, l = tid & 63, c = l & 15, g = l >> 4, qq = c >> 2, pp = l & 3;
  const int w = __builtin_amdgcn_readfirstlane(tid >> 6);
  const int bid = blockIdx.x, kvh = bid & 7, jj = bid >> 3, b = jj >> 1, hp = jj & 1;
  STAMP(0);
  const int n0 = kvh * 512 + hp * 256;
  const i32x4 srd_k = make_srd(kc + (size_t)(b * HKV + kvh) * SK * DH, SK * DH * 2);
  const i32x4 srd_v = make_srd(vc + (size_t)(b * HKV + kvh) * SK * DH, SK * DH * 2);
  const unsigned kvo = (unsigned)(g * 256 + ((c ^ g) << 4));
  const unsigned vvo = (unsigned)(g * 256 + ((c ^ (2 * g)) << 4));

  if (w < 4) {
    f32x4 qacc[4][3];
    const i32x4 srd_x = make_srd(x + (size_t)b * SQ * DM, SQ * DM * 2);
    const i32x4 srd_w = make_srd(wq, DM * HQ * DH * 2);
#pragma unroll
    for (int i = 0; i < 12; ++i) {
      const int r = w * 12 + i;
      dma_w(smbase + L_X + r * 1024, (unsigned)((l ^ (r & 15)) * 16), srd_x, (unsigned)(r * 1024));
    }
    const unsigned ring = smbase + L_RING + w * (NRING * 4096);
    const unsigned voff_e = (unsigned)((l >> 3) * 8192 + (((l & 7) ^ (((l >> 4) & 1) << 1)) * 16));
    const unsigned voff_o = voff_e ^ 64u;
    const unsigned scol = (unsigned)((n0 + 64 * w) * 2);
#define WQ_ISSUE(s)                                                                                                        \
  do {                                                                                                                     \
    _Pragma("unroll") for (int i_ = 0; i_ < 4; ++i_)                                                                       \
        dma_w(ring + ((s) % NRING) * 4096 + i_ * 1024, (i_ & 1) ? voff_o : voff_e, srd_w, scol + (unsigned)((32 * (((s) + b) & 15) + 8 * i_) * 8192)); \
  } while (0)
    WQ_ISSUE(0); WQ_ISSUE(1); WQ_ISSUE(2); WQ_ISSUE(3);
    waitv<16>();
    STAMP(1); BAR(); STAMP(2);
    const int fA = ((qq >> 1) & 1) | ((g & 1) << 1);
    ldsp wqa = sm + L_RING + w * (NRING * 4096) + (8 * g + qq) * 128 + 8 * pp;
    ldsp xa = sm + L_X + c * 1024;
#pragma unroll
    for (int t = 0; t < 4; ++t)
#pragma unroll
      for (int qt = 0; qt < 3; ++qt) qacc[t][qt] = f32x4{0.f, 0.f, 0.f, 0.f};
    bf16x8 fw[2][4], fx[2][3];
#define A_READ(s, buf)                                                                                          \
  do {                                                                                                          \
    _Pragma("unroll") for (int t_ = 0; t_ < 4; ++t_) {                                                          \
      ldsp p_ = wqa + ((s) % NRING) * 4096 + ((t_ ^ fA) << 5);                                                  \
      fw[buf][t_] = lds_tr2(p_, p_ + 512);                                                                      \
    }                                                                                                           \
    _Pragma("unroll") for (int q_ = 0; q_ < 3; ++q_)                                                            \
        fx[buf][q_] = lds_rd128(xa + q_ * 16384 + ((((4 * (((s) + b) & 15)) | g) ^ c) << 4));                   \
  } while (0)
    waitv<12>();
    A_READ(0, 0);
#pragma unroll
    for (int s = 0; s < 16; ++s) {
      if (s + 4 < 16) WQ_ISSUE(s + 4);
      if (s == 12) {
#pragma unroll
        for (int i = 0; i < 8; ++i)
          dma_kv(smbase + (32 * w + 4 * i) * 256, (kvo ^ (unsigned)(64 * (i & 3))), srd_k, (unsigned)((128 * w + 4 * i) * 256));
      }
      if (s + 1 < 16) {
        const int rem = ((s + 4 < 16) ? (s + 4) : 15) - (s + 1);
        if (s < 12) waitv<12>(); else if (rem == 2) waitv<16>(); else if (rem == 1) waitv<12>(); else waitv<8>();
        A_READ(s + 1, (s + 1) & 1);
      }
      SCHED();
#pragma unroll
      for (int t = 0; t < 4; ++t)
#pragma unroll
        for (int qt = 0; qt < 3; ++qt) qacc[t][qt] = mfma(fw[s & 1][t], fx[s & 1][qt], qacc[t][qt]);
      SCHED();
    }
    STAMP(3); BAR(); STAMP(4);
    {
      const int h = w >> 1;
#pragma unroll
      for (int t = 0; t < 4; ++t)
#pragma unroll
        for (int qt = 0; qt < 3; ++qt) {
          const int c16 = 8 * (w & 1) + 2 * t + (g >> 1);
          *(LDS_AS bf16x4*)(sm + L_Q + (48 * h + 16 * qt + c) * 256 + ((c16 ^ c) << 4) + 8 * (g & 1)) = cvt4(qacc[t][qt]);
        }
    }
  } else {
    STAMP(1); BAR(); STAMP(2);
    STAMP(3); BAR(); STAMP(4);
  }
  const unsigned kvrow = (unsigned)((128 * (w >> 1) + 16 * (w & 1)) * 256);
#define K_ISSUE(j, slot)                                                                                                \
  do {                                                                                                                  \
    _Pragma("unroll") for (int i_ = 0; i_ < 4; ++i_)                                                                    \
        dma_kv(smbase + (slot) * L_SLOT + (16 * w + 4 * i_) * 256, kvo ^ (unsigned)(64 * i_), srd_k, kvrow + (unsigned)((32 * (j) + 4 * i_) * 256)); \
  } while (0)
#define V_ISSUE(j, slot)                                                                                                \
  do {                                                                                                                  \
    _Pragma("unroll") for (int i_ = 0; i_ < 4; ++i_)                                                                    \
        dma_kv(smbase + (slot) * L_SLOT + (16 * w + 4 * i_) * 256, vvo ^ (unsigned)(128 * (i_ & 1)), srd_v, kvrow + (unsigned)((32 * (j) + 4 * i_) * 256)); \
  } while (0)
  const i32x4 srd_o = make_srd(wo + (size_t)n0 * DM, 256 * DM * 2);
  const unsigned wvo = (unsigned)((l >> 3) * 1024 + (((l & 7) ^ (g << 1)) << 4));
#define WO_ISSUE(u, slot)                                                                                               \
  do {                                                                                                                  \
    _Pragma("unroll") for (int i_ = 0; i_ < 4; ++i_)                                                                    \
        dma_w(smbase + (slot) * L_SLOT + w * 4096 + i_ * 1024, wvo, srd_o, (unsigned)(w * 128 + (32 * (((u) + b) & 7) + 8 * i_) * 1024)); \
  } while (0)
  K_ISSUE(1, 1); V_ISSUE(0, 2); K_ISSUE(2, 3);
  waitv<12>();
  STAMP(5); BAR(); STAMP(6);

  const int h = w >> 2, kq = w & 3;
  float mloc[3];
  f32x4 oacc[8][3];
  f32x2 lacc[3];
  {
    ldsp qa = sm + L_Q + (48 * h + c) * 256;
    ldsp ka = sm + (32 * kq + c) * 256;
    const int fV = 4 * (g & 1) + qq;
    ldsp va[4];
#pragma unroll
    for (int r = 0; r < 4; ++r) va[r] = sm + (32 * kq + 4 * g + qq) * 256 + 8 * pp + (((2 * ((r + kq) & 3)) ^ fV) << 5);
#pragma unroll
    for (int i = 0; i < 8; ++i)
#pragma unroll
      for (int qt = 0; qt < 3; ++qt) oacc[i][qt] = f32x4{0.f, 0.f, 0.f, 0.f};
#pragma unroll
    for (int qt = 0; qt < 3; ++qt) lacc[qt] = f32x2{0.f, 0.f};
    constexpr int kslot[4] = {0, 1, 3, 1}, vslot[4] = {2, 0, 2, 3};
#define SYNC(j)                                                          \
  do {                                                                   \
    if ((j) == 0) waitv<4>(); else if ((j) == 3) waitv<4>(); else waitv<0>(); \
    BAR(); STAMP(7 + (j));                                               \
    if ((j) == 0) { V_ISSUE(1, 0); }                                     \
    if ((j) == 1) { K_ISSUE(3, 1); V_ISSUE(2, 2); }                      \
    if ((j) == 2) { V_ISSUE(3, 3); WO_ISSUE(0, 0); }                     \
    if ((j) == 3) { WO_ISSUE(1, 4); }                                    \
  } while (0)
#pragma unroll
    for (int j = 0; j < 4; ++j) {
      f32x4 sacc[2][3];
      {
        unsigned kso = (unsigned)(kslot[j] * L_SLOT);
        asm volatile("" : "+s"(kso));
        bf16x8 kf[2][4], qf[4][3];
#pragma unroll
        for (int s = 0; s < 4; ++s) {
#pragma unroll
          for (int kt = 0; kt < 2; ++kt) kf[kt][s] = lds_rd128(ka + kso + kt * 4096 + ((((4 * s) | g) ^ c) << 4));
#pragma unroll
          for (int qt = 0; qt < 3; ++qt) qf[s][qt] = lds_rd128(qa + qt * 4096 + ((((4 * s) | g) ^ c) << 4));
        }
        SCHED();
#pragma unroll
        for (int s = 0; s < 4; ++s)
#pragma unroll
          for (int kt = 0; kt < 2; ++kt)
#pragma unroll
            for (int qt = 0; qt < 3; ++qt)
              sacc[kt][qt] = mfma(kf[kt][s], qf[s][qt], s == 0 ? f32x4{0.f, 0.f, 0.f, 0.f} : sacc[kt][qt]);
        SCHED();
      }
      if (h == 0) SYNC(j);
      float tm[3];
#pragma unroll
      for (int qt = 0; qt < 3; ++qt) {
        float m = fmaxf(fmaxf(fmaxf(sacc[0][qt][0], sacc[0][qt][1]), fmaxf(sacc[0][qt][2], sacc[0][qt][3])),
                        fmaxf(fmaxf(sacc[1][qt][0], sacc[1][qt][1]), fmaxf(sacc[1][qt][2], sacc[1][qt][3])));
        tm[qt] = xmax_g(m) * SCL2E;
      }
      bool resc = false;
      float al[3];
      if (j == 0) {
#pragma unroll
        for (int qt = 0; qt < 3; ++qt) mloc[qt] = tm[qt];
      } else {
        const bool grow = fmaxf(fmaxf(tm[0] - mloc[0], tm[1] - mloc[1]), tm[2] - mloc[2]) > RESC_T;
        resc = __any(grow);
#pragma unroll
        for (int qt = 0; qt < 3; ++qt) {
          const float mn = resc ? fmaxf(mloc[qt], tm[qt]) : mloc[qt];
          al[qt] = __builtin_amdgcn_exp2f(mloc[qt] - mn);
          mloc[qt] = mn;
          lacc[qt] *= al[qt];
        }
      }
      bf16x8 pf[3];
#pragma unroll
      for (int qt = 0; qt < 3; ++qt) {
        const f32x2 sc2 = {SCL2E, SCL2E}, nm2 = {-mloc[qt], -mloc[qt]};
#pragma unroll
        for (int kt = 0; kt < 2; ++kt) {
          f32x2 lo = {sacc[kt][qt][0], sacc[kt][qt][1]}, hi = {sacc[kt][qt][2], sacc[kt][qt][3]};
          lo = __builtin_elementwise_fma(lo, sc2, nm2);
          hi = __builtin_elementwise_fma(hi, sc2, nm2);
          sacc[kt][qt][0] = __builtin_amdgcn_exp2f(lo[0]); sacc[kt][qt][1] = __builtin_amdgcn_exp2f(lo[1]);
          sacc[kt][qt][2] = __builtin_amdgcn_exp2f(hi[0]); sacc[kt][qt][3] = __builtin_amdgcn_exp2f(hi[1]);
          lacc[qt] += f32x2{sacc[kt][qt][0], sacc[kt][qt][1]} + f32x2{sacc[kt][qt][2], sacc[kt][qt][3]};
        }
        pf[qt] = cvt8(sacc[0][qt], sacc[1][qt]);
        asm volatile("" : "+v"(pf[qt]), "+v"(lacc[qt]));
      }
      SCHED();
      if (h == 1) SYNC(j);
      if (j > 0 && __builtin_expect(resc, 0)) {
#pragma unroll
        for (int qt = 0; qt < 3; ++qt)
#pragma unroll
          for (int i = 0; i < 8; ++i) oacc[i][qt] *= al[qt];
      }
      {
        unsigned vso = (unsigned)(vslot[j] * L_SLOT);
        asm volatile("" : "+s"(vso));
        bf16x8 vf[8];
#pragma unroll
        for (int i = 0; i < 8; ++i) {
          ldsp p = (ldsp)(((unsigned)(uintptr_t)va[i >> 1] ^ (unsigned)((i & 1) << 5)) + vso);
          vf[i] = lds_tr2(p, p + 4096);
        }
        SCHED();
#pragma unroll
        for (int i = 0; i < 8; ++i)
#pragma unroll
          for (int qt = 0; qt < 3; ++qt) oacc[i][qt] = mfma(vf[i], pf[qt], oacc[i][qt]);
        SCHED();
      }
    }
#pragma unroll
    for (int qt = 0; qt < 3; ++qt) {
      const float ls = xsum_g(lacc[qt][0] + lacc[qt][1]);
      if (g == 0) *(LDS_AS f32x2*)(sm + L_ST2 + (w * 48 + 16 * qt + c) * 8) = f32x2{mloc[qt], ls};
    }
    BAR(); STAMP(11);
  }
  int tz = threadIdx.x;
  asm volatile("" : "+v"(tz));
  const int l2 = tz & 63, c2 = l2 & 15, g2 = l2 >> 4, qq2 = c2 >> 2, pp2 = l2 & 3;
#define l l2
#define c c2
#define g g2
#define qq qq2
#define pp pp2
  {
    float sc[3];
#pragma unroll
    for (int qt = 0; qt < 3; ++qt) {
      f32x2 ml[4];
#pragma unroll
      for (int k = 0; k < 4; ++k) ml[k] = *(LDS_AS f32x2*)(sm + L_ST2 + ((h * 4 + k) * 48 + 16 * qt + c) * 8);
      const float m = fmaxf(fmaxf(ml[0][0], ml[1][0]), fmaxf(ml[2][0], ml[3][0]));
      float lt = 0.f;
#pragma unroll
      for (int k = 0; k < 4; ++k) lt += ml[k][1] * __builtin_amdgcn_exp2f(ml[k][0] - m);
      sc[qt] = __builtin_amdgcn_exp2f(mloc[qt] - m) / lt;
    }
#pragma unroll
    for (int r = 1; r < 4; ++r) {
      const int wd = h * 4 + ((kq + r) & 3);
#pragma unroll
      for (int qt = 0; qt < 3; ++qt)
        *(LDS_AS bf16x8*)(sm + xoff(wd, r - 1, qt) + l * 16) = cvt8(oacc[2 * r][qt] * sc[qt], oacc[2 * r + 1][qt] * sc[qt]);
    }
    BAR(); STAMP(17);
#pragma unroll
    for (int qt = 0; qt < 3; ++qt) {
      f32x4 o0 = oacc[0][qt] * sc[qt], o1 = oacc[1][qt] * sc[qt];
#pragma unroll
      for (int r = 0; r < 3; ++r) {
        bf16x8 t = lds_rd128(sm + xoff(w, r, qt) + l * 16);
#pragma unroll
        for (int e = 0; e < 4; ++e) { o0[e] += (float)t[e]; o1[e] += (float)t[4 + e]; }
      }
      *(LDS_AS bf16x8*)(sm + L_O2 + (w * 3 + qt) * 1024 + l * 16) = cvt8(o0, o1);
    }
  }
  BAR(); STAMP(18);
  WO_ISSUE(2, 1); WO_ISSUE(3, 3);

  f32x4 cacc[4][3];
  {
    bf16x8 of[8][3];
#pragma unroll
    for (int u = 0; u < 8; ++u)
#pragma unroll
      for (int qt = 0; qt < 3; ++qt) of[u][qt] = lds_rd128(sm + L_O2 + (((u + b) & 7) * 3 + qt) * 1024 + l * 16);
    const int fC = 2 * (g & 1) + (qq >> 1);
    ldsp wra = sm + w * 4096 + (4 * g + qq) * 128 + 8 * pp;
    bf16x8 wf[2][4];
#define CSLOT(u) (((u) & 3) == 0 ? 0 : (((u) & 3) == 1 ? 4 : (((u) & 3) == 2 ? 1 : 3)))
#define C_READ(u)                                                                                                   \
  do {                                                                                                              \
    _Pragma("unroll") for (int t_ = 0; t_ < 4; ++t_) {                                                              \
      ldsp p_ = wra + CSLOT(u) * L_SLOT + ((t_ ^ fC) << 5);                                                         \
      wf[(u) & 1][t_] = lds_tr2(p_, p_ + 2048);                                                                     \
    }                                                                                                               \
  } while (0)
    waitv<12>();
    C_READ(0);
#pragma unroll
    for (int u = 0; u < 8; ++u) {
      if (u >= 1 && u + 3 < 8) WO_ISSUE(u + 3, CSLOT(u + 3));
      if (u + 1 < 8) {
        const int last = (u + 3 < 8) ? (u + 3) : 7;
        const int rem = last - (u + 1);
        if (rem == 2) waitv<8>(); else if (rem == 1) waitv<4>(); else waitv<0>();
        C_READ(u + 1);
      }
      SCHED();
#pragma unroll
      for (int t = 0; t < 4; ++t)
#pragma unroll
        for (int qt = 0; qt < 3; ++qt) cacc[t][qt] = mfma(wf[u & 1][t], of[u][qt], u == 0 ? f32x4{0.f, 0.f, 0.f, 0.f} : cacc[t][qt]);
      SCHED();
    }
  }
  STAMP(19);
  bf16x8* pp8 = (bf16x8*)part + ((size_t)(b * 16 + kvh * 2 + hp) * 8 + w) * 6 * 64 + l;
#pragma unroll
  for (int a = 0; a < 2; ++a)
#pragma unroll
    for (int qt = 0; qt < 3; ++qt) pp8[(a * 3 + qt) * 64] = cvt8(cacc[2 * a][qt], cacc[2 * a + 1][qt]);
}

#undef l
#undef c
#undef g
#undef qq
#undef pp
__global__ __launch_bounds__(192) void gqa_reduce(const __bf16* __restrict__ part, __bf16* __restrict__ out) {
  const int idx = blockIdx.x * 192 + threadIdx.x;
  const int b = idx / 3072, rem = idx - b * 3072;
  const bf16x8* p = (const bf16x8*)part + (size_t)b * 16 * 3072 + rem;
  bf16x8 v[16];
#pragma unroll
  for (int s = 0; s < 16; ++s) v[s] = p[(size_t)s * 3072];
  f32x4 a0 = {0.f, 0.f, 0.f, 0.f}, a1 = {0.f, 0.f, 0.f, 0.f};
#pragma unroll
  for (int s = 0; s < 16; ++s)
#pragma unroll
    for (int e = 0; e < 4; ++e) { a0[e] += (float)v[s][e]; a1[e] += (float)v[s][4 + e]; }
  const int lane = rem & 63, tq = rem >> 6, qt = tq % 3, wa = tq / 3;
  const int q = 16 * qt + (lane & 15), n = 32 * wa + 4 * (lane >> 4);
  __bf16* o = out + ((size_t)b * SQ + q) * DM + n;
  *(bf16x4*)o = cvt4(a0);
  *(bf16x4*)(o + 16) = cvt4(a1);
}

extern "C" void kernel_launch(void* const* d_in, const int* in_sizes, int n_in, void* d_out, int out_size, void* d_ws, size_t ws_size,
                              hipStream_t stream) {
  const __bf16* x = (const __bf16*)d_in[0];
  const __bf16* kc = (const __bf16*)d_in[1];
  const __bf16* vc = (const __bf16*)d_in[2];
  const __bf16* wq = (const __bf16*)d_in[3];
  const __bf16* wo = (const __bf16*)d_in[4];
  __bf16* part = (__bf16*)d_ws;
  (void)hipFuncSetAttribute((const void*)gqa_fused, hipFuncAttributeMaxDynamicSharedMemorySize, LDS_BYTES);
  hipLaunchKernelGGL(gqa_fused, dim3(256), dim3(512), LDS_BYTES, stream, x, kc, vc, wq, wo, part);
  hipLaunchKernelGGL(gqa_reduce, dim3(256), dim3(192), 0, stream, (const __bf16*)part, (__bf16*)d_out);
}
```

```cpp
#include <hip/hip_runtime.h>
#include <hip/hip_bf16.h>
#include <stdint.h>

typedef __bf16 bf16x8 __attribute__((ext_vector_type(8)));
typedef __bf16 bf16x4 __attribute__((ext_vector_type(4)));
typedef float f32x4 __attribute__((ext_vector_type(4)));
typedef float f32x2 __attribute__((ext_vector_type(2)));
typedef int i32x4 __attribute__((ext_vector_type(4)));
typedef unsigned u32x4 __attribute__((ext_vector_type(4)));
#define LDS_AS __attribute__((address_space(3)))
typedef LDS_AS char* ldsp;

constexpr int B_ = 16, SQ = 48, DM = 512, HQ = 32, HKV = 8, SK = 512, DH = 128;
constexpr float SCL2E = 0.08838834764831845f * 1.4426950408889634f;

constexpr int L_SLOT = 32768;
constexpr int L_RING = 32768;
constexpr int L_X = 114688;
constexpr int L_Q = 131072;
constexpr int L_ST1 = 155648;
constexpr int L_ST2 = 32768 + 24576;
constexpr int L_O2 = 2 * 32768;
constexpr int LDS_BYTES = 163840;
constexpr int NRING = 5;
#define RESC_T 16.0f

#define BAR() asm volatile("s_waitcnt lgkmcnt(0)\n\ts_barrier" ::: "memory")
#define SCHED() __builtin_amdgcn_sched_barrier(0)
#define STAMP(i)
template <int N> __device__ __forceinline__ void waitv() { asm volatile("s_waitcnt vmcnt(%0)" ::"i"(N) : "memory"); }

__device__ __forceinline__ void dma4(unsigned lds, unsigned v0, unsigned v1, unsigned v2, unsigned v3, i32x4 srd, unsigned soff) {
  asm volatile("s_mov_b32 m0, %0\n\ts_nop 0\n\t"
               "buffer_load_dwordx4 %1, %5, %6 offen lds\n\t"
               "buffer_load_dwordx4 %2, %5, %6 offen offset:1024 lds\n\t"
               "buffer_load_dwordx4 %3, %5, %6 offen offset:2048 lds\n\t"
               "buffer_load_dwordx4 %4, %5, %6 offen offset:3072 lds"
               ::"s"(lds), "v"(v0), "v"(v1), "v"(v2), "v"(v3), "s"(srd), "s"(soff) : "memory");
}
__device__ __forceinline__ void dma4nt(unsigned lds, unsigned v0, unsigned v1, unsigned v2, unsigned v3, i32x4 srd, unsigned soff) {
  asm volatile("s_mov_b32 m0, %0\n\ts_nop 0\n\t"
               "buffer_load_dwordx4 %1, %5, %6 offen nt lds\n\t"
               "buffer_load_dwordx4 %2, %5, %6 offen offset:1024 nt lds\n\t"
               "buffer_load_dwordx4 %3, %5, %6 offen offset:2048 nt lds\n\t"
               "buffer_load_dwordx4 %4, %5, %6 offen offset:3072 nt lds"
               ::"s"(lds), "v"(v0), "v"(v1), "v"(v2), "v"(v3), "s"(srd), "s"(soff) : "memory");
}
__device__ __forceinline__ i32x4 make_srd(const void* p, unsigned bytes) {
  unsigned long long a = (unsigned long long)p;
  i32x4 s;
  s[0] = (int)(unsigned)a; s[1] = (int)((unsigned)(a >> 32) & 0xffffu); s[2] = (int)bytes; s[3] = 0x00020000;
  return s;
}
__device__ __forceinline__ bf16x8 lds_rd128(ldsp p) { return *(LDS_AS bf16x8*)p; }
__device__ __forceinline__ bf16x8 lds_tr2(ldsp p0, ldsp p1) {
  bf16x4 a = __builtin_amdgcn_ds_read_tr16_b64_v4bf16((LDS_AS bf16x4*)p0);
  bf16x4 b = __builtin_amdgcn_ds_read_tr16_b64_v4bf16((LDS_AS bf16x4*)p1);
  return __builtin_shufflevector(a, b, 0, 1, 2, 3, 4, 5, 6, 7);
}
__device__ __forceinline__ f32x4 mfma(bf16x8 a, bf16x8 b, f32x4 c) { return __builtin_amdgcn_mfma_f32_16x16x32_bf16(a, b, c, 0, 0, 0); }
__device__ __forceinline__ bf16x4 cvt4(f32x4 v) {
  bf16x4 r; r[0] = (__bf16)v[0]; r[1] = (__bf16)v[1]; r[2] = (__bf16)v[2]; r[3] = (__bf16)v[3]; return r;
}
__device__ __forceinline__ bf16x8 cvt8(f32x4 a, f32x4 b) { return __builtin_shufflevector(cvt4(a), cvt4(b), 0, 1, 2, 3, 4, 5, 6, 7); }
__device__ __forceinline__ float max3(float a, float b, float c) { return __builtin_elementwise_maximum(__builtin_elementwise_maximum(a, b), c); }
__device__ __forceinline__ float max8(f32x4 a, f32x4 b) {
  return max3(max3(max3(a[0], a[1], a[2]), a[3], b[0]), max3(b[1], b[2], b[3]), b[3]);
}
__device__ __forceinline__ float xmax_g(float v) {
  auto r = __builtin_amdgcn_permlane16_swap(__float_as_uint(v), __float_as_uint(v), false, false);
  v = fmaxf(__uint_as_float(r[0]), __uint_as_float(r[1]));
  auto r2 = __builtin_amdgcn_permlane32_swap(__float_as_uint(v), __float_as_uint(v), false, false);
  return fmaxf(__uint_as_float(r2[0]), __uint_as_float(r2[1]));
}
__device__ __forceinline__ int xoff(int wd, int r, int qt) { return (r == 0 ? L_O2 : (r == 1 ? L_SLOT : 3 * L_SLOT)) + (wd * 3 + qt) * 1024; }

__global__ __launch_bounds__(512, 2) void gqa_fused(const __bf16* __restrict__ x, const __bf16* __restrict__ kc, const __bf16* __restrict__ vc,
                                                    const __bf16* __restrict__ wq, const __bf16* __restrict__ wo, __bf16* __restrict__ part) {
  extern __shared__ __attribute__((aligned(16))) char smem_raw[];
  ldsp sm = (ldsp)smem_raw;
  const unsigned smbase = (unsigned)(uintptr_t)sm;
  const int w = __builtin_amdgcn_readfirstlane(threadIdx.x >> 6);
  const int bid = blockIdx.x, kvh = bid & 7, jj = bid >> 3, b = jj >> 1, hp = jj & 1;
  STAMP(0);
  const int n0 = kvh * 512 + hp * 256;
  const i32x4 srd_k = make_srd(kc + (size_t)(b * HKV + kvh) * SK * DH, SK * DH * 2);
  const i32x4 srd_v = make_srd(vc + (size_t)(b * HKV + kvh) * SK * DH, SK * DH * 2);
  const i32x4 srd_o = make_srd(wo + (size_t)n0 * DM, 256 * DM * 2);
  const int h = w >> 2, kq = w & 3;
  f32x4 oacc[8][3];
  f32x4 lacc[3];
  float mref[3];
  {
    const int l = threadIdx.x & 63, c = l & 15, g = l >> 4, qq = c >> 2, pp = l & 3;
    const unsigned kv0 = (unsigned)(g * 256 + ((c ^ g) << 4));
    const unsigned vv0 = (unsigned)(g * 256 + ((c ^ (2 * g)) << 4));
    if (w < 4) {
      f32x4 qacc[4][3];
      const i32x4 srd_x = make_srd(x + (size_t)b * SQ * DM, SQ * DM * 2);
      const i32x4 srd_w = make_srd(wq, DM * HQ * DH * 2);
#pragma unroll
      for (int i = 0; i < 3; ++i) {
        const int r = w * 12 + 4 * i;
        dma4(smbase + L_X + r * 1024, (unsigned)((l ^ (r & 15)) * 16), (unsigned)((l ^ ((r + 1) & 15)) * 16), (unsigned)((l ^ ((r + 2) & 15)) * 16),
             (unsigned)((l ^ ((r + 3) & 15)) * 16), srd_x, (unsigned)(r * 1024));
      }
      const unsigned ring = smbase + L_RING + w * (NRING * 4096);
      const unsigned wv0 = (unsigned)((l >> 3) * 8192 + (((l & 7) ^ (((l >> 4) & 1) << 1)) * 16));
      const unsigned wv1 = (wv0 ^ 64u) + (65536u - 1024u), wv2 = wv0 + 2u * (65536u - 1024u), wv3 = (wv0 ^ 64u) + 3u * (65536u - 1024u);
      const unsigned scol = (unsigned)((n0 + 64 * w) * 2);
#define WQ_ISSUE(s) dma4(ring + ((s) % NRING) * 4096, wv0, wv1, wv2, wv3, srd_w, scol + (unsigned)((((s) + b) & 15) * (32 * 8192)))
      WQ_ISSUE(0); WQ_ISSUE(1); WQ_ISSUE(2); WQ_ISSUE(3);
      waitv<16>();
      STAMP(1); BAR(); STAMP(2);
      const int fA = ((qq >> 1) & 1) | ((g & 1) << 1);
      ldsp wqa = sm + L_RING + w * (NRING * 4096) + (8 * g + qq) * 128 + 8 * pp;
      ldsp xa = sm + L_X + c * 1024;
      bf16x8 fw[2][4], fx[2][3];
#define A_READ(s, buf)                                                                                          \
  do {                                                                                                          \
    _Pragma("unroll") for (int t_ = 0; t_ < 4; ++t_) {                                                          \
      ldsp p_ = wqa + ((s) % NRING) * 4096 + ((t_ ^ fA) << 5);                                                  \
      fw[buf][t_] = lds_tr2(p_, p_ + 512);                                                                      \
    }                                                                                                           \
    _Pragma("unroll") for (int q_ = 0; q_ < 3; ++q_)                                                            \
        fx[buf][q_] = lds_rd128(xa + q_ * 16384 + ((((4 * (((s) + b) & 15)) | g) ^ c) << 4));                   \
  } while (0)
      waitv<12>();
      A_READ(0, 0);
#pragma unroll
      for (int s = 0; s < 16; ++s) {
        if (s + 4 < 16) WQ_ISSUE(s + 4);
        if (s == 12) {
          dma4nt(smbase + (32 * w) * 256, kv0, kv0 ^ 64u, kv0 ^ 128u, kv0 ^ 192u, srd_k, (unsigned)(128 * w * 256));
          dma4nt(smbase + (32 * w + 16) * 256, kv0, kv0 ^ 64u, kv0 ^ 128u, kv0 ^ 192u, srd_k, (unsigned)((128 * w + 16) * 256));
        }
        if (s + 1 < 16) {
          const int rem = ((s + 4 < 16) ? (s + 4) : 15) - (s + 1);
          if (s < 12) waitv<12>(); else if (rem == 2) waitv<16>(); else if (rem == 1) waitv<12>(); else waitv<8>();
          A_READ(s + 1, (s + 1) & 1);
        }
        SCHED();
#pragma unroll
        for (int t = 0; t < 4; ++t)
#pragma unroll
          for (int qt = 0; qt < 3; ++qt) qacc[t][qt] = mfma(fw[s & 1][t], fx[s & 1][qt], s == 0 ? f32x4{0.f, 0.f, 0.f, 0.f} : qacc[t][qt]);
        SCHED();
      }
      STAMP(3); BAR(); STAMP(4);
#pragma unroll
      for (int t = 0; t < 4; ++t)
#pragma unroll
        for (int qt = 0; qt < 3; ++qt) {
          const int c16 = 8 * (w & 1) + 2 * t + (g >> 1);
          *(LDS_AS bf16x4*)(sm + L_Q + (48 * (w >> 1) + 16 * qt + c) * 256 + ((c16 ^ c) << 4) + 8 * (g & 1)) = cvt4(qacc[t][qt] * SCL2E);
        }
    } else {
      STAMP(1); BAR(); STAMP(2);
      STAMP(3); BAR(); STAMP(4);
    }
    const unsigned kvrow = (unsigned)((128 * (w >> 1) + 16 * (w & 1)) * 256);
#define K_ISSUE(j, slot) dma4nt(smbase + (slot) * L_SLOT + w * 4096, kv0, kv0 ^ 64u, kv0 ^ 128u, kv0 ^ 192u, srd_k, kvrow + (unsigned)((j) * 8192))
#define V_ISSUE(j, slot) dma4nt(smbase + (slot) * L_SLOT + w * 4096, vv0, vv0 ^ 128u, vv0, vv0 ^ 128u, srd_v, kvrow + (unsigned)((j) * 8192))
    const unsigned ov0 = (unsigned)((l >> 3) * 1024 + (((l & 7) ^ (g << 1)) << 4));
#define WO_ISSUE(u, slot) dma4(smbase + (slot) * L_SLOT + w * 4096, ov0, ov0 + 7168u, ov0 + 14336u, ov0 + 21504u, srd_o, (unsigned)(w * 128 + (((u) + b) & 7) * 32768))
    K_ISSUE(1, 1); V_ISSUE(0, 2); K_ISSUE(2, 3);
    waitv<12>();
    STAMP(5); BAR(); STAMP(6);

    ldsp qa = sm + L_Q + (48 * h + c) * 256;
    ldsp ka = sm + (32 * kq + c) * 256;
    const int fV = 4 * (g & 1) + qq;
    unsigned va[4];
#pragma unroll
    for (int r = 0; r < 4; ++r) va[r] = smbase + (32 * kq + 4 * g + qq) * 256 + 8 * pp + (((2 * ((r + kq) & 3)) ^ fV) << 5);
    bf16x8 ones;
#pragma unroll
    for (int e = 0; e < 8; ++e) ones[e] = (__bf16)1.0f;
    f32x4 negm[3];
    constexpr int kslot[4] = {0, 1, 3, 1}, vslot[4] = {2, 0, 2, 3};
#define SYNC(j)                                                               \
  do {                                                                        \
    if ((j) == 0) waitv<4>(); else if ((j) == 3) waitv<4>(); else waitv<0>(); \
    BAR(); STAMP(7 + (j));                                                    \
    if ((j) == 0) { V_ISSUE(1, 0); }                                          \
    if ((j) == 1) { K_ISSUE(3, 1); V_ISSUE(2, 2); }                           \
    if ((j) == 2) { V_ISSUE(3, 3); WO_ISSUE(0, 0); }                          \
    if ((j) == 3) { WO_ISSUE(1, 4); }                                         \
  } while (0)
#pragma unroll
    for (int j = 0; j < 4; ++j) {
      f32x4 sacc[2][3];
      {
        unsigned kso = (unsigned)(kslot[j] * L_SLOT);
        asm volatile("" : "+s"(kso));
#pragma unroll
        for (int sh = 0; sh < 2; ++sh) {
          bf16x8 kf[2][2], qf[2][3];
#pragma unroll
          for (int s2 = 0; s2 < 2; ++s2) {
            const int s = 2 * sh + s2;
#pragma unroll
            for (int kt = 0; kt < 2; ++kt) kf[s2][kt] = lds_rd128(ka + kso + kt * 4096 + ((((4 * s) | g) ^ c) << 4));
#pragma unroll
            for (int qt = 0; qt < 3; ++qt) qf[s2][qt] = lds_rd128(qa + qt * 4096 + ((((4 * s) | g) ^ c) << 4));
          }
          if (sh == 0) SCHED();
#pragma unroll
          for (int s2 = 0; s2 < 2; ++s2)
#pragma unroll
            for (int kt = 0; kt < 2; ++kt)
#pragma unroll
              for (int qt = 0; qt < 3; ++qt)
                sacc[kt][qt] = mfma(kf[s2][kt], qf[s2][qt], (sh == 0 && s2 == 0) ? (j == 0 ? f32x4{0.f, 0.f, 0.f, 0.f} : negm[qt]) : sacc[kt][qt]);
        }
        SCHED();
      }
      bool resc = false;
      float al[3];
      if (j == 0) {
#pragma unroll
        for (int qt = 0; qt < 3; ++qt) {
          const float m = xmax_g(max8(sacc[0][qt], sacc[1][qt]));
          if (g == 0) *(LDS_AS float*)(sm + L_ST1 + ((w * 3 + qt) * 16 + c) * 4) = m;
        }
        SYNC(0);
#pragma unroll
        for (int qt = 0; qt < 3; ++qt) {
          float m = *(LDS_AS float*)(sm + L_ST1 + ((h * 12 + qt) * 16 + c) * 4);
#pragma unroll
          for (int k = 1; k < 4; ++k) m = fmaxf(m, *(LDS_AS float*)(sm + L_ST1 + ((h * 12 + 3 * k + qt) * 16 + c) * 4));
          mref[qt] = m;
          negm[qt] = f32x4{-m, -m, -m, -m};
          sacc[0][qt] += negm[qt];
          sacc[1][qt] += negm[qt];
        }
      } else {
        if (h == 0) SYNC(j);
        float t = max3(sacc[0][0][0], sacc[0][0][1], sacc[0][0][2]);
        t = max3(t, sacc[0][0][3], sacc[1][0][0]); t = max3(t, sacc[1][0][1], sacc[1][0][2]);
        t = max3(t, sacc[1][0][3], sacc[0][1][0]); t = max3(t, sacc[0][1][1], sacc[0][1][2]);
        t = max3(t, sacc[0][1][3], sacc[1][1][0]); t = max3(t, sacc[1][1][1], sacc[1][1][2]);
        t = max3(t, sacc[1][1][3], sacc[0][2][0]); t = max3(t, sacc[0][2][1], sacc[0][2][2]);
        t = max3(t, sacc[0][2][3], sacc[1][2][0]); t = max3(t, sacc[1][2][1], sacc[1][2][2]);
        t = max3(t, sacc[1][2][3], t);
        resc = __any(!(t <= RESC_T));
        if (__builtin_expect(resc, 0)) {
#pragma unroll
          for (int qt = 0; qt < 3; ++qt) {
            const float d = fmaxf(xmax_g(max8(sacc[0][qt], sacc[1][qt])), 0.f);
            al[qt] = __builtin_amdgcn_exp2f(-d);
            mref[qt] += d;
            negm[qt] -= d;
            sacc[0][qt] -= d;
            sacc[1][qt] -= d;
          }
        }
      }
      bf16x8 pf[3];
#pragma unroll
      for (int qt = 0; qt < 3; ++qt) {
#pragma unroll
        for (int kt = 0; kt < 2; ++kt)
#pragma unroll
          for (int r = 0; r < 4; ++r) sacc[kt][qt][r] = __builtin_amdgcn_exp2f(sacc[kt][qt][r]);
        pf[qt] = cvt8(sacc[0][qt], sacc[1][qt]);
        asm volatile("" : "+v"(pf[qt]));
      }
      SCHED();
      if (j > 0) {
        if (h == 1) SYNC(j);
        if (__builtin_expect(resc, 0)) {
#pragma unroll
          for (int qt = 0; qt < 3; ++qt) {
            lacc[qt] *= al[qt];
#pragma unroll
            for (int i = 0; i < 8; ++i) oacc[i][qt] *= al[qt];
          }
        }
      }
      {
        unsigned vso = (unsigned)(vslot[j] * L_SLOT);
        asm volatile("" : "+s"(vso));
        bf16x8 vf[8];
#pragma unroll
        for (int i = 0; i < 8; ++i) {
          ldsp p = (ldsp)((va[i >> 1] ^ (unsigned)((i & 1) << 5)) + vso);
          vf[i] = lds_tr2(p, p + 4096);
        }
        SCHED();
#pragma unroll
        for (int qt = 0; qt < 3; ++qt) lacc[qt] = mfma(ones, pf[qt], j == 0 ? f32x4{0.f, 0.f, 0.f, 0.f} : lacc[qt]);
#pragma unroll
        for (int i = 0; i < 8; ++i)
#pragma unroll
          for (int qt = 0; qt < 3; ++qt) oacc[i][qt] = mfma(vf[i], pf[qt], j == 0 ? f32x4{0.f, 0.f, 0.f, 0.f} : oacc[i][qt]);
        SCHED();
      }
    }
    if (g == 0) {
#pragma unroll
      for (int qt = 0; qt < 3; ++qt) *(LDS_AS f32x2*)(sm + L_ST2 + ((w * 3 + qt) * 16 + c) * 8) = f32x2{mref[qt], lacc[qt][0]};
    }
    BAR(); STAMP(11);
  }
  int tz = threadIdx.x;
  asm volatile("" : "+v"(tz));
  const int l = tz & 63, c = l & 15, g = l >> 4, qq = c >> 2, pp = l & 3;
  {
    float inv[3], sc[3];
    bool differ = false;
#pragma unroll
    for (int qt = 0; qt < 3; ++qt) {
      f32x2 ml[4];
#pragma unroll
      for (int k = 0; k < 4; ++k) ml[k] = *(LDS_AS f32x2*)(sm + L_ST2 + ((h * 12 + 3 * k + qt) * 16 + c) * 8);
      const float m = fmaxf(fmaxf(ml[0][0], ml[1][0]), fmaxf(ml[2][0], ml[3][0]));
      float lt = 0.f;
#pragma unroll
      for (int k = 0; k < 4; ++k) lt += ml[k][1] * __builtin_amdgcn_exp2f(ml[k][0] - m);
      inv[qt] = __builtin_amdgcn_rcpf(lt);
      sc[qt] = __builtin_amdgcn_exp2f(mref[qt] - m);
      differ = differ || (mref[qt] != m);
    }
    if (__builtin_expect(__any(differ), 0)) {
#pragma unroll
      for (int qt = 0; qt < 3; ++qt)
#pragma unroll
        for (int i = 0; i < 8; ++i) oacc[i][qt] *= sc[qt];
    }
#pragma unroll
    for (int r = 1; r < 4; ++r) {
      const int wd = h * 4 + ((kq + r) & 3);
#pragma unroll
      for (int qt = 0; qt < 3; ++qt) *(LDS_AS bf16x8*)(sm + xoff(wd, r - 1, qt) + l * 16) = cvt8(oacc[2 * r][qt], oacc[2 * r + 1][qt]);
    }
    bf16x8 sel0, sel1;
#pragma unroll
    for (int e = 0; e < 4; ++e) {
      const __bf16 v = (4 * g + e == c) ? (__bf16)1.0f : (__bf16)0.0f;
      sel0[e] = v; sel0[4 + e] = (__bf16)0.0f; sel1[e] = (__bf16)0.0f; sel1[4 + e] = v;
    }
    BAR(); STAMP(17);
#pragma unroll
    for (int qt = 0; qt < 3; ++qt) {
#pragma unroll
      for (int r = 0; r < 3; ++r) {
        bf16x8 t = lds_rd128(sm + xoff(w, r, qt) + l * 16);
        oacc[0][qt] = mfma(sel0, t, oacc[0][qt]);
        oacc[1][qt] = mfma(sel1, t, oacc[1][qt]);
      }
    }
#pragma unroll
    for (int qt = 0; qt < 3; ++qt) *(LDS_AS bf16x8*)(sm + L_O2 + (w * 3 + qt) * 1024 + l * 16) = cvt8(oacc[0][qt] * inv[qt], oacc[1][qt] * inv[qt]);
  }
  BAR(); STAMP(18);
  const unsigned ov0 = (unsigned)((l >> 3) * 1024 + (((l & 7) ^ (g << 1)) << 4));
  WO_ISSUE(2, 1); WO_ISSUE(3, 3);

  f32x4 cacc[4][3];
  {
    bf16x8 of[8][3];
#pragma unroll
    for (int u = 0; u < 8; ++u)
#pragma unroll
      for (int qt = 0; qt < 3; ++qt) of[u][qt] = lds_rd128(sm + L_O2 + (((u + b) & 7) * 3 + qt) * 1024 + l * 16);
    const int fC = 2 * (g & 1) + (qq >> 1);
    ldsp wra = sm + w * 4096 + (4 * g + qq) * 128 + 8 * pp;
    bf16x8 wf[2][4];
#define CSLOT(u) (((u) & 3) == 0 ? 0 : (((u) & 3) == 1 ? 4 : (((u) & 3) == 2 ? 1 : 3)))
#define C_READ(u)                                                                                                   \
  do {                                                                                                              \
    _Pragma("unroll") for (int t_ = 0; t_ < 4; ++t_) {                                                              \
      ldsp p_ = wra + CSLOT(u) * L_SLOT + ((t_ ^ fC) << 5);                                                         \
      wf[(u) & 1][t_] = lds_tr2(p_, p_ + 2048);                                                                     \
    }                                                                                                               \
  } while (0)
    waitv<12>();
    C_READ(0);
#pragma unroll
    for (int u = 0; u < 8; ++u) {
      if (u >= 1 && u + 3 < 8) WO_ISSUE(u + 3, CSLOT(u + 3));
      if (u + 1 < 8) {
        const int last = (u + 3 < 8) ? (u + 3) : 7;
        const int rem = last - (u + 1);
        if (rem == 2) waitv<8>(); else if (rem == 1) waitv<4>(); else waitv<0>();
        C_READ(u + 1);
      }
      SCHED();
#pragma unroll
      for (int t = 0; t < 4; ++t)
#pragma unroll
        for (int qt = 0; qt < 3; ++qt) cacc[t][qt] = mfma(wf[u & 1][t], of[u][qt], u == 0 ? f32x4{0.f, 0.f, 0.f, 0.f} : cacc[t][qt]);
      SCHED();
    }
  }
  STAMP(19);
  bf16x8* pp8 = (bf16x8*)part + ((size_t)(b * 16 + kvh * 2 + hp) * 8 + w) * 6 * 64 + l;
#pragma unroll
  for (int a = 0; a < 2; ++a)
#pragma unroll
    for (int qt = 0; qt < 3; ++qt) pp8[(a * 3 + qt) * 64] = cvt8(cacc[2 * a][qt], cacc[2 * a + 1][qt]);
}

__global__ __launch_bounds__(192) void gqa_reduce(const __bf16* __restrict__ part, __bf16* __restrict__ out) {
  const int idx = blockIdx.x * 192 + threadIdx.x;
  const int b = idx / 3072, rem = idx - b * 3072;
  const bf16x8* p = (const bf16x8*)part + (size_t)b * 16 * 3072 + rem;
  bf16x8 v[16];
#pragma unroll
  for (int s = 0; s < 16; ++s) v[s] = p[(size_t)s * 3072];
  f32x4 a0 = {0.f, 0.f, 0.f, 0.f}, a1 = {0.f, 0.f, 0.f, 0.f};
#pragma unroll
  for (int s = 0; s < 16; ++s)
#pragma unroll
    for (int e = 0; e < 4; ++e) { a0[e] += (float)v[s][e]; a1[e] += (float)v[s][4 + e]; }
  const int lane = rem & 63, tq = rem >> 6, qt = tq % 3, wa = tq / 3;
  const int q = 16 * qt + (lane & 15), n = 32 * wa + 4 * (lane >> 4);
  __bf16* o = out + ((size_t)b * SQ + q) * DM + n;
  *(bf16x4*)o = cvt4(a0);
  *(bf16x4*)(o + 16) = cvt4(a1);
}

extern "C" void kernel_launch(void* const* d_in, const int* in_sizes, int n_in, void* d_out, int out_size, void* d_ws, size_t ws_size,
                              hipStream_t stream) {
  const __bf16* x = (const __bf16*)d_in[0];
  const __bf16* kc = (const __bf16*)d_in[1];
  const __bf16* vc = (const __bf16*)d_in[2];
  const __bf16* wq = (const __bf16*)d_in[3];
  const __bf16* wo = (const __bf16*)d_in[4];
  __bf16* part = (__bf16*)d_ws;
  (void)hipFuncSetAttribute((const void*)gqa_fused, hipFuncAttributeMaxDynamicSharedMemorySize, LDS_BYTES);
  hipLaunchKernelGGL(gqa_fused, dim3(256), dim3(512), LDS_BYTES, stream, x, kc, vc, wq, wo, part);
  hipLaunchKernelGGL(gqa_reduce, dim3(256), dim3(192), 0, stream, (const __bf16*)part, (__bf16*)d_out);
}
```

```cpp
#include <hip/hip_runtime.h>
#include <hip/hip_bf16.h>
#include <stdint.h>

typedef __bf16 bf16x8 __attribute__((ext_vector_type(8)));
typedef __bf16 bf16x4 __attribute__((ext_vector_type(4)));
typedef float f32x4 __attribute__((ext_vector_type(4)));
typedef float f32x2 __attribute__((ext_vector_type(2)));
typedef int i32x4 __attribute__((ext_vector_type(4)));
typedef unsigned u32x4 __attribute__((ext_vector_type(4)));
#define LDS_AS __attribute__((address_space(3)))
typedef LDS_AS char* ldsp;

constexpr int B_ = 16, SQ = 48, DM = 512, HQ = 32, HKV = 8, SK = 512, DH = 128;
constexpr float SCL2E = 0.08838834764831845f * 1.4426950408889634f;

constexpr int L_SLOT = 32768;
constexpr int L_RING = 32768;
constexpr int L_X = 114688;
constexpr int L_Q = 131072;
constexpr int L_ST1 = 155648;
constexpr int L_ST2 = 32768 + 24576;
constexpr int L_O2 = 2 * 32768;
constexpr int LDS_BYTES = 163840;
constexpr int NRING = 5;
#define RESC_T 16.0f

#define BAR() asm volatile("s_waitcnt lgkmcnt(0)\n\ts_barrier" ::: "memory")
#define SCHED() __builtin_amdgcn_sched_barrier(0)
#define STAMP(i)
template <int N> __device__ __forceinline__ void waitv() { asm volatile("s_waitcnt vmcnt(%0)" ::"i"(N) : "memory"); }

__device__ __forceinline__ void dma4(unsigned lds, unsigned v0, unsigned v1, unsigned v2, unsigned v3, i32x4 srd, unsigned soff) {
  asm volatile("s_mov_b32 m0, %0\n\ts_nop 0\n\t"
               "buffer_load_dwordx4 %1, %5, %6 offen lds\n\t"
               "buffer_load_dwordx4 %2, %5, %6 offen offset:1024 lds\n\t"
               "buffer_load_dwordx4 %3, %5, %6 offen offset:2048 lds\n\t"
               "buffer_load_dwordx4 %4, %5, %6 offen offset:3072 lds"
               ::"s"(lds), "v"(v0), "v"(v1), "v"(v2), "v"(v3), "s"(srd), "s"(soff) : "memory");
}
__device__ __forceinline__ void dma4nt(unsigned lds, unsigned v0, unsigned v1, unsigned v2, unsigned v3, i32x4 srd, unsigned soff) {
  asm volatile("s_mov_b32 m0, %0\n\ts_nop 0\n\t"
               "buffer_load_dwordx4 %1, %5, %6 offen nt lds\n\t"
               "buffer_load_dwordx4 %2, %5, %6 offen offset:1024 nt lds\n\t"
               "buffer_load_dwordx4 %3, %5, %6 offen offset:2048 nt lds\n\t"
               "buffer_load_dwordx4 %4, %5, %6 offen offset:3072 nt lds"
               ::"s"(lds), "v"(v0), "v"(v1), "v"(v2), "v"(v3), "s"(srd), "s"(soff) : "memory");
}
__device__ __forceinline__ i32x4 make_srd(const void* p, unsigned bytes) {
  unsigned long long a = (unsigned long long)p;
  i32x4 s;
  s[0] = (int)(unsigned)a; s[1] = (int)((unsigned)(a >> 32) & 0xffffu); s[2] = (int)bytes; s[3] = 0x00020000;
  return s;
}
__device__ __forceinline__ bf16x8 lds_rd128(ldsp p) { return *(LDS_AS bf16x8*)p; }
__device__ __forceinline__ bf16x8 lds_tr2(ldsp p0, ldsp p1) {
  bf16x4 a = __builtin_amdgcn_ds_read_tr16_b64_v4bf16((LDS_AS bf16x4*)p0);
  bf16x4 b = __builtin_amdgcn_ds_read_tr16_b64_v4bf16((LDS_AS bf16x4*)p1);
  return __builtin_shufflevector(a, b, 0, 1, 2, 3, 4, 5, 6, 7);
}
__device__ __forceinline__ f32x4 mfma(bf16x8 a, bf16x8 b, f32x4 c) { return __builtin_amdgcn_mfma_f32_16x16x32_bf16(a, b, c, 0, 0, 0); }
__device__ __forceinline__ bf16x4 cvt4(f32x4 v) {
  bf16x4 r; r[0] = (__bf16)v[0]; r[1] = (__bf16)v[1]; r[2] = (__bf16)v[2]; r[3] = (__bf16)v[3]; return r;
}
__device__ __forceinline__ bf16x8 cvt8(f32x4 a, f32x4 b) { return __builtin_shufflevector(cvt4(a), cvt4(b), 0, 1, 2, 3, 4, 5, 6, 7); }
__device__ __forceinline__ float max3(float a, float b, float c) { return __builtin_elementwise_maximum(__builtin_elementwise_maximum(a, b), c); }
__device__ __forceinline__ float max8(f32x4 a, f32x4 b) {
  return max3(max3(max3(a[0], a[1], a[2]), a[3], b[0]), max3(b[1], b[2], b[3]), b[3]);
}
__device__ __forceinline__ float xmax_g(float v) {
  auto r = __builtin_amdgcn_permlane16_swap(__float_as_uint(v), __float_as_uint(v), false, false);
  v = fmaxf(__uint_as_float(r[0]), __uint_as_float(r[1]));
  auto r2 = __builtin_amdgcn_permlane32_swap(__float_as_uint(v), __float_as_uint(v), false, false);
  return fmaxf(__uint_as_float(r2[0]), __uint_as_float(r2[1]));
}
__device__ __forceinline__ int xoff(int wd, int r, int qt) { return (r == 0 ? L_O2 : (r == 1 ? L_SLOT : 4 * L_SLOT)) + (wd * 3 + qt) * 1024; }

__global__ __launch_bounds__(512, 2) void gqa_fused(const __bf16* __restrict__ x, const __bf16* __restrict__ kc, const __bf16* __restrict__ vc,
                                                    const __bf16* __restrict__ wq, const __bf16* __restrict__ wo, __bf16* __restrict__ part) {
  extern __shared__ __attribute__((aligned(16))) char smem_raw[];
  ldsp sm = (ldsp)smem_raw;
  const unsigned smbase = (unsigned)(uintptr_t)sm;
  const int w = __builtin_amdgcn_readfirstlane(threadIdx.x >> 6);
  const int bid = blockIdx.x, kvh = bid & 7, jj = bid >> 3, b = jj >> 1, hp = jj & 1;
  STAMP(0);
  const int n0 = kvh * 512 + hp * 256;
  const i32x4 srd_k = make_srd(kc + (size_t)(b * HKV + kvh) * SK * DH, SK * DH * 2);
  const i32x4 srd_v = make_srd(vc + (size_t)(b * HKV + kvh) * SK * DH, SK * DH * 2);
  const i32x4 srd_o = make_srd(wo + (size_t)n0 * DM, 256 * DM * 2);
  const int h = w >> 2, kq = w & 3;
  f32x4 oacc[8][3];
  f32x4 lacc[3];
  float mref[3];
  {
    const int l = threadIdx.x & 63, c = l & 15, g = l >> 4, qq = c >> 2, pp = l & 3;
    const unsigned kv0 = (unsigned)(g * 256 + ((c ^ g) << 4));
    const unsigned vv0 = (unsigned)(g * 256 + ((c ^ (2 * g)) << 4));
    if (w < 4) {
      f32x4 qacc[4][3];
      const i32x4 srd_x = make_srd(x + (size_t)b * SQ * DM, SQ * DM * 2);
      const i32x4 srd_w = make_srd(wq, DM * HQ * DH * 2);
#pragma unroll
      for (int i = 0; i < 3; ++i) {
        const int r = w * 12 + 4 * i;
        dma4(smbase + L_X + r * 1024, (unsigned)((l ^ (r & 15)) * 16), (unsigned)((l ^ ((r + 1) & 15)) * 16), (unsigned)((l ^ ((r + 2) & 15)) * 16),
             (unsigned)((l ^ ((r + 3) & 15)) * 16), srd_x, (unsigned)(r * 1024));
      }
      const unsigned ring = smbase + L_RING + w * (NRING * 4096);
      const unsigned wv0 = (unsigned)((l >> 3) * 8192 + (((l & 7) ^ (((l >> 4) & 1) << 1)) * 16));
      const unsigned wv1 = (wv0 ^ 64u) + (65536u - 1024u), wv2 = wv0 + 2u * (65536u - 1024u), wv3 = (wv0 ^ 64u) + 3u * (65536u - 1024u);
      const unsigned scol = (unsigned)((n0 + 64 * w) * 2);
#define WQ_ISSUE(s) dma4(ring + ((s) % NRING) * 4096, wv0, wv1, wv2, wv3, srd_w, scol + (unsigned)((((s) + b) & 15) * (32 * 8192)))
      WQ_ISSUE(0); WQ_ISSUE(1); WQ_ISSUE(2); WQ_ISSUE(3);
      waitv<16>();
      STAMP(1); BAR(); STAMP(2);
      const int fA = ((qq >> 1) & 1) | ((g & 1) << 1);
      ldsp wqa = sm + L_RING + w * (NRING * 4096) + (8 * g + qq) * 128 + 8 * pp;
      ldsp xa = sm + L_X + c * 1024;
      bf16x8 fw[2][4], fx[2][3];
#define A_READ(s, buf)                                                                                          \
  do {                                                                                                          \
    _Pragma("unroll") for (int t_ = 0; t_ < 4; ++t_) {                                                          \
      ldsp p_ = wqa + ((s) % NRING) * 4096 + ((t_ ^ fA) << 5);                                                  \
      fw[buf][t_] = lds_tr2(p_, p_ + 512);                                                                      \
    }                                                                                                           \
    _Pragma("unroll") for (int q_ = 0; q_ < 3; ++q_)                                                            \
        fx[buf][q_] = lds_rd128(xa + q_ * 16384 + ((((4 * (((s) + b) & 15)) | g) ^ c) << 4));                   \
  } while (0)
      waitv<12>();
      A_READ(0, 0);
#pragma unroll
      for (int s = 0; s < 16; ++s) {
        if (s + 4 < 16) WQ_ISSUE(s + 4);
        if (s == 12) {
          dma4nt(smbase + (32 * w) * 256, kv0, kv0 ^ 64u, kv0 ^ 128u, kv0 ^ 192u, srd_k, (unsigned)(128 * w * 256));
          dma4nt(smbase + (32 * w + 16) * 256, kv0, kv0 ^ 64u, kv0 ^ 128u, kv0 ^ 192u, srd_k, (unsigned)((128 * w + 16) * 256));
        }
        if (s + 1 < 16) {
          const int rem = ((s + 4 < 16) ? (s + 4) : 15) - (s + 1);
          if (s < 12) waitv<12>(); else if (rem == 2) waitv<16>(); else if (rem == 1) waitv<12>(); else waitv<8>();
          A_READ(s + 1, (s + 1) & 1);
        }
        SCHED();
#pragma unroll
        for (int t = 0; t < 4; ++t)
#pragma unroll
          for (int qt = 0; qt < 3; ++qt) qacc[t][qt] = mfma(fw[s & 1][t], fx[s & 1][qt], s == 0 ? f32x4{0.f, 0.f, 0.f, 0.f} : qacc[t][qt]);
        SCHED();
      }
      STAMP(3); BAR(); STAMP(4);
#pragma unroll
      for (int t = 0; t < 4; ++t)
#pragma unroll
        for (int qt = 0; qt < 3; ++qt) {
          const int c16 = 8 * (w & 1) + 2 * t + (g >> 1);
          *(LDS_AS bf16x4*)(sm + L_Q + (48 * (w >> 1) + 16 * qt + c) * 256 + ((c16 ^ c) << 4) + 8 * (g & 1)) = cvt4(qacc[t][qt] * SCL2E);
        }
    } else {
      STAMP(1); BAR(); STAMP(2);
      STAMP(3); BAR(); STAMP(4);
    }
    const unsigned kvrow = (unsigned)((128 * (w >> 1) + 16 * (w & 1)) * 256);
#define K_ISSUE(j, slot) dma4nt(smbase + (slot) * L_SLOT + w * 4096, kv0, kv0 ^ 64u, kv0 ^ 128u, kv0 ^ 192u, srd_k, kvrow + (unsigned)((j) * 8192))
#define V_ISSUE(j, slot) dma4nt(smbase + (slot) * L_SLOT + w * 4096, vv0, vv0 ^ 128u, vv0, vv0 ^ 128u, srd_v, kvrow + (unsigned)((j) * 8192))
    const unsigned ov0 = (unsigned)((l >> 3) * 1024 + (((l & 7) ^ (g << 1)) << 4));
#define WO_ISSUE(u, slot) dma4(smbase + (slot) * L_SLOT + w * 4096, ov0, ov0 + 7168u, ov0 + 14336u, ov0 + 21504u, srd_o, (unsigned)(w * 128 + (((u) + b) & 7) * 32768))
    K_ISSUE(1, 1);
    waitv<4>();
    STAMP(5); BAR(); STAMP(6);

    bf16x8 qf[4][3];
#pragma unroll
    for (int s = 0; s < 4; ++s)
#pragma unroll
      for (int qt = 0; qt < 3; ++qt) qf[s][qt] = lds_rd128(sm + L_Q + (48 * h + 16 * qt + c) * 256 + ((((4 * s) | g) ^ c) << 4));
    V_ISSUE(0, 2);
    ldsp ka = sm + (32 * kq + c) * 256;
    const int fV = 4 * (g & 1) + qq;
    unsigned va[4];
#pragma unroll
    for (int r = 0; r < 4; ++r) va[r] = smbase + (32 * kq + 4 * g + qq) * 256 + 8 * pp + (((2 * ((r + kq) & 3)) ^ fV) << 5);
    bf16x8 ones;
#pragma unroll
    for (int e = 0; e < 8; ++e) ones[e] = (__bf16)1.0f;
    f32x4 negm[3];
    constexpr int kslot[4] = {0, 1, 3, 4}, vslot[4] = {2, 0, 1, 2};
#define SYNC(j)                                                               \
  do {                                                                        \
    if ((j) < 3) waitv<4>(); else waitv<8>();                                 \
    if ((j) == 1 && w == 6) waitv<0>();                                       \
    BAR(); STAMP(7 + (j));                                                    \
    if ((j) == 0) { V_ISSUE(1, 0); }                                          \
    if ((j) == 1) { if (w == 6) K_ISSUE(3, 4); V_ISSUE(2, 1); }               \
    if ((j) == 2) { WO_ISSUE(0, 3); }                                         \
  } while (0)
#pragma unroll
    for (int j = 0; j < 4; ++j) {
      f32x4 sacc[2][3];
      {
        unsigned kso = (unsigned)(kslot[j] * L_SLOT);
        asm volatile("" : "+s"(kso));
        bf16x8 kf[2][4];
#pragma unroll
        for (int s = 0; s < 4; ++s)
#pragma unroll
          for (int kt = 0; kt < 2; ++kt) kf[kt][s] = lds_rd128(ka + kso + kt * 4096 + ((((4 * s) | g) ^ c) << 4));
        SCHED();
#pragma unroll
        for (int s = 0; s < 4; ++s)
#pragma unroll
          for (int kt = 0; kt < 2; ++kt)
#pragma unroll
            for (int qt = 0; qt < 3; ++qt)
              sacc[kt][qt] = mfma(kf[kt][s], qf[s][qt], s == 0 ? (j == 0 ? f32x4{0.f, 0.f, 0.f, 0.f} : negm[qt]) : sacc[kt][qt]);
        SCHED();
        if (j == 0) K_ISSUE(2, 3);
      }
      bool resc = false;
      float al[3];
      if (j == 0) {
#pragma unroll
        for (int qt = 0; qt < 3; ++qt) {
          const float m = xmax_g(max8(sacc[0][qt], sacc[1][qt]));
          if (g == 0) *(LDS_AS float*)(sm + L_ST1 + ((w * 3 + qt) * 16 + c) * 4) = m;
        }
        SYNC(0);
#pragma unroll
        for (int qt = 0; qt < 3; ++qt) {
          float m = *(LDS_AS float*)(sm + L_ST1 + ((h * 12 + qt) * 16 + c) * 4);
#pragma unroll
          for (int k = 1; k < 4; ++k) m = fmaxf(m, *(LDS_AS float*)(sm + L_ST1 + ((h * 12 + 3 * k + qt) * 16 + c) * 4));
          mref[qt] = m;
          negm[qt] = f32x4{-m, -m, -m, -m};
          sacc[0][qt] += negm[qt];
          sacc[1][qt] += negm[qt];
        }
      } else {
        if (h == 0) SYNC(j);
        float t = max3(sacc[0][0][0], sacc[0][0][1], sacc[0][0][2]);
        t = max3(t, sacc[0][0][3], sacc[1][0][0]); t = max3(t, sacc[1][0][1], sacc[1][0][2]);
        t = max3(t, sacc[1][0][3], sacc[0][1][0]); t = max3(t, sacc[0][1][1], sacc[0][1][2]);
        t = max3(t, sacc[0][1][3], sacc[1][1][0]); t = max3(t, sacc[1][1][1], sacc[1][1][2]);
        t = max3(t, sacc[1][1][3], sacc[0][2][0]); t = max3(t, sacc[0][2][1], sacc[0][2][2]);
        t = max3(t, sacc[0][2][3], sacc[1][2][0]); t = max3(t, sacc[1][2][1], sacc[1][2][2]);
        t = max3(t, sacc[1][2][3], t);
        resc = __any(!(t <= RESC_T));
        if (__builtin_expect(resc, 0)) {
#pragma unroll
          for (int qt = 0; qt < 3; ++qt) {
            const float d = fmaxf(xmax_g(max8(sacc[0][qt], sacc[1][qt])), 0.f);
            al[qt] = __builtin_amdgcn_exp2f(-d);
            mref[qt] += d;
            negm[qt] -= d;
            sacc[0][qt] -= d;
            sacc[1][qt] -= d;
          }
        }
      }
      bf16x8 pf[3];
#pragma unroll
      for (int qt = 0; qt < 3; ++qt) {
#pragma unroll
        for (int kt = 0; kt < 2; ++kt)
#pragma unroll
          for (int r = 0; r < 4; ++r) sacc[kt][qt][r] = __builtin_amdgcn_exp2f(sacc[kt][qt][r]);
        pf[qt] = cvt8(sacc[0][qt], sacc[1][qt]);
        asm volatile("" : "+v"(pf[qt]));
      }
      SCHED();
      if (j > 0) {
        if (h == 1) SYNC(j);
        if (__builtin_expect(resc, 0)) {
#pragma unroll
          for (int qt = 0; qt < 3; ++qt) {
            lacc[qt] *= al[qt];
#pragma unroll
            for (int i = 0; i < 8; ++i) oacc[i][qt] *= al[qt];
          }
        }
      }
      {
        unsigned vso = (unsigned)(vslot[j] * L_SLOT);
        asm volatile("" : "+s"(vso));
        bf16x8 vf[8];
#pragma unroll
        for (int i = 0; i < 8; ++i) {
          ldsp p = (ldsp)((va[i >> 1] ^ (unsigned)((i & 1) << 5)) + vso);
          vf[i] = lds_tr2(p, p + 4096);
        }
        SCHED();
#pragma unroll
        for (int qt = 0; qt < 3; ++qt) lacc[qt] = mfma(ones, pf[qt], j == 0 ? f32x4{0.f, 0.f, 0.f, 0.f} : lacc[qt]);
#pragma unroll
        for (int i = 0; i < 8; ++i)
#pragma unroll
          for (int qt = 0; qt < 3; ++qt) oacc[i][qt] = mfma(vf[i], pf[qt], j == 0 ? f32x4{0.f, 0.f, 0.f, 0.f} : oacc[i][qt]);
        SCHED();
        if (j == 0 && w != 6) K_ISSUE(3, 4);
        if (j == 1) V_ISSUE(3, 2);
        if (j == 2) WO_ISSUE(1, 0);
      }
    }
    if (g == 0) {
#pragma unroll
      for (int qt = 0; qt < 3; ++qt) *(LDS_AS f32x2*)(sm + L_ST2 + ((w * 3 + qt) * 16 + c) * 8) = f32x2{mref[qt], lacc[qt][0]};
    }
    BAR(); STAMP(11);
  }
  int tz = threadIdx.x;
  asm volatile("" : "+v"(tz));
  const int l = tz & 63, c = l & 15, g = l >> 4, qq = c >> 2, pp = l & 3;
  {
    float inv[3], sc[3];
    bool differ = false;
#pragma unroll
    for (int qt = 0; qt < 3; ++qt) {
      f32x2 ml[4];
#pragma unroll
      for (int k = 0; k < 4; ++k) ml[k] = *(LDS_AS f32x2*)(sm + L_ST2 + ((h * 12 + 3 * k + qt) * 16 + c) * 8);
      const float m = fmaxf(fmaxf(ml[0][0], ml[1][0]), fmaxf(ml[2][0], ml[3][0]));
      float lt = 0.f;
#pragma unroll
      for (int k = 0; k < 4; ++k) lt += ml[k][1] * __builtin_amdgcn_exp2f(ml[k][0] - m);
      inv[qt] = __builtin_amdgcn_rcpf(lt);
      sc[qt] = __builtin_amdgcn_exp2f(mref[qt] - m);
      differ = differ || (mref[qt] != m);
    }
    if (__builtin_expect(__any(differ), 0)) {
#pragma unroll
      for (int qt = 0; qt < 3; ++qt)
#pragma unroll
        for (int i = 0; i < 8; ++i) oacc[i][qt] *= sc[qt];
    }
#pragma unroll
    for (int r = 1; r < 4; ++r) {
      const int wd = h * 4 + ((kq + r) & 3);
#pragma unroll
      for (int qt = 0; qt < 3; ++qt) *(LDS_AS bf16x8*)(sm + xoff(wd, r - 1, qt) + l * 16) = cvt8(oacc[2 * r][qt], oacc[2 * r + 1][qt]);
    }
    bf16x8 sel0, sel1;
#pragma unroll
    for (int e = 0; e < 4; ++e) {
      const __bf16 v = (4 * g + e == c) ? (__bf16)1.0f : (__bf16)0.0f;
      sel0[e] = v; sel0[4 + e] = (__bf16)0.0f; sel1[e] = (__bf16)0.0f; sel1[4 + e] = v;
    }
    BAR(); STAMP(17);
#pragma unroll
    for (int qt = 0; qt < 3; ++qt) {
#pragma unroll
      for (int r = 0; r < 3; ++r) {
        bf16x8 t = lds_rd128(sm + xoff(w, r, qt) + l * 16);
        oacc[0][qt] = mfma(sel0, t, oacc[0][qt]);
        oacc[1][qt] = mfma(sel1, t, oacc[1][qt]);
      }
    }
#pragma unroll
    for (int qt = 0; qt < 3; ++qt) *(LDS_AS bf16x8*)(sm + L_O2 + (w * 3 + qt) * 1024 + l * 16) = cvt8(oacc[0][qt] * inv[qt], oacc[1][qt] * inv[qt]);
  }
  BAR(); STAMP(18);
  const unsigned ov0 = (unsigned)((l >> 3) * 1024 + (((l & 7) ^ (g << 1)) << 4));
  WO_ISSUE(2, 1); WO_ISSUE(3, 4);

  f32x4 cacc[4][3];
  {
    bf16x8 of[8][3];
#pragma unroll
    for (int u = 0; u < 8; ++u)
#pragma unroll
      for (int qt = 0; qt < 3; ++qt) of[u][qt] = lds_rd128(sm + L_O2 + (((u + b) & 7) * 3 + qt) * 1024 + l * 16);
    const int fC = 2 * (g & 1) + (qq >> 1);
    ldsp wra = sm + w * 4096 + (4 * g + qq) * 128 + 8 * pp;
    bf16x8 wf[2][4];
#define CSLOT(u) (((u) & 3) == 0 ? 3 : (((u) & 3) == 1 ? 0 : (((u) & 3) == 2 ? 1 : 4)))
#define C_READ(u)                                                                                                   \
  do {                                                                                                              \
    _Pragma("unroll") for (int t_ = 0; t_ < 4; ++t_) {                                                              \
      ldsp p_ = wra + CSLOT(u) * L_SLOT + ((t_ ^ fC) << 5);                                                         \
      wf[(u) & 1][t_] = lds_tr2(p_, p_ + 2048);                                                                     \
    }                                                                                                               \
  } while (0)
    waitv<12>();
    C_READ(0);
#pragma unroll
    for (int u = 0; u < 8; ++u) {
      if (u >= 1 && u + 3 < 8) WO_ISSUE(u + 3, CSLOT(u + 3));
      if (u + 1 < 8) {
        const int last = (u + 3 < 8) ? (u + 3) : 7;
        const int rem = last - (u + 1);
        if (rem == 2) waitv<8>(); else if (rem == 1) waitv<4>(); else waitv<0>();
        C_READ(u + 1);
      }
      SCHED();
#pragma unroll
      for (int t = 0; t < 4; ++t)
#pragma unroll
        for (int qt = 0; qt < 3; ++qt) cacc[t][qt] = mfma(wf[u & 1][t], of[u][qt], u == 0 ? f32x4{0.f, 0.f, 0.f, 0.f} : cacc[t][qt]);
      SCHED();
    }
  }
  STAMP(19);
  bf16x8* pp8 = (bf16x8*)part + ((size_t)(b * 16 + kvh * 2 + hp) * 8 + w) * 6 * 64 + l;
#pragma unroll
  for (int a = 0; a < 2; ++a)
#pragma unroll
    for (int qt = 0; qt < 3; ++qt) pp8[(a * 3 + qt) * 64] = cvt8(cacc[2 * a][qt], cacc[2 * a + 1][qt]);
}

__global__ __launch_bounds__(192) void gqa_reduce(const __bf16* __restrict__ part, __bf16* __restrict__ out) {
  const int idx = blockIdx.x * 192 + threadIdx.x;
  const int b = idx / 3072, rem = idx - b * 3072;
  const bf16x8* p = (const bf16x8*)part + (size_t)b * 16 * 3072 + rem;
  bf16x8 v[16];
#pragma unroll
  for (int s = 0; s < 16; ++s) v[s] = p[(size_t)s * 3072];
  f32x4 a0 = {0.f, 0.f, 0.f, 0.f}, a1 = {0.f, 0.f, 0.f, 0.f};
#pragma unroll
  for (int s = 0; s < 16; ++s)
#pragma unroll
    for (int e = 0; e < 4; ++e) { a0[e] += (float)v[s][e]; a1[e] += (float)v[s][4 + e]; }
  const int lane = rem & 63, tq = rem >> 6, qt = tq % 3, wa = tq / 3;
  const int q = 16 * qt + (lane & 15), n = 32 * wa + 4 * (lane >> 4);
  __bf16* o = out + ((size_t)b * SQ + q) * DM + n;
  *(bf16x4*)o = cvt4(a0);
  *(bf16x4*)(o + 16) = cvt4(a1);
}

extern "C" void kernel_launch(void* const* d_in, const int* in_sizes, int n_in, void* d_out, int out_size, void* d_ws, size_t ws_size,
                              hipStream_t stream) {
  const __bf16* x = (const __bf16*)d_in[0];
  const __bf16* kc = (const __bf16*)d_in[1];
  const __bf16* vc = (const __bf16*)d_in[2];
  const __bf16* wq = (const __bf16*)d_in[3];
  const __bf16* wo = (const __bf16*)d_in[4];
  __bf16* part = (__bf16*)d_ws;
  (void)hipFuncSetAttribute((const void*)gqa_fused, hipFuncAttributeMaxDynamicSharedMemorySize, LDS_BYTES);
  hipLaunchKernelGGL(gqa_fused, dim3(256), dim3(512), LDS_BYTES, stream, x, kc, vc, wq, wo, part);
  hipLaunchKernelGGL(gqa_reduce, dim3(256), dim3(192), 0, stream, (const __bf16*)part, (__bf16*)d_out);
}
```

```cpp
#include <hip/hip_runtime.h>
#include <hip/hip_bf16.h>
#include <stdint.h>

typedef __bf16 bf16x8 __attribute__((ext_vector_type(8)));
typedef __bf16 bf16x4 __attribute__((ext_vector_type(4)));
typedef float f32x4 __attribute__((ext_vector_type(4)));
typedef float f32x2 __attribute__((ext_vector_type(2)));
typedef int i32x4 __attribute__((ext_vector_type(4)));
typedef unsigned u32x4 __attribute__((ext_vector_type(4)));
#define LDS_AS __attribute__((address_space(3)))
typedef LDS_AS char* ldsp;

constexpr int B_ = 16, SQ = 48, DM = 512, HQ = 32, HKV = 8, SK = 512, DH = 128;
constexpr float SCL2E = 0.08838834764831845f * 1.4426950408889634f;

constexpr int L_SLOT = 32768;
constexpr int L_RING = 32768;
constexpr int L_X = 114688;
constexpr int L_Q = 131072;
constexpr int L_ST1 = 155648;
constexpr int L_ST2 = 32768 + 24576;
constexpr int L_O2 = 2 * 32768;
constexpr int LDS_BYTES = 163840;
constexpr int NRING = 5;
#define RESC_T 16.0f

#define BAR() asm volatile("s_waitcnt lgkmcnt(0)\n\ts_barrier" ::: "memory")
#define SCHED() __builtin_amdgcn_sched_barrier(0)
#define STAMP(i)
template <int N> __device__ __forceinline__ void waitv() { asm volatile("s_waitcnt vmcnt(%0)" ::"i"(N) : "memory"); }

__device__ __forceinline__ void dma4(unsigned lds, unsigned v0, unsigned v1, unsigned v2, unsigned v3, i32x4 srd, unsigned soff) {
  asm volatile("s_mov_b32 m0, %0\n\ts_nop 0\n\t"
               "buffer_load_dwordx4 %1, %5, %6 offen lds\n\t"
               "buffer_load_dwordx4 %2, %5, %6 offen offset:1024 lds\n\t"
               "buffer_load_dwordx4 %3, %5, %6 offen offset:2048 lds\n\t"
               "buffer_load_dwordx4 %4, %5, %6 offen offset:3072 lds"
               ::"s"(lds), "v"(v0), "v"(v1), "v"(v2), "v"(v3), "s"(srd), "s"(soff) : "memory");
}
__device__ __forceinline__ void dma4nt(unsigned lds, unsigned v0, unsigned v1, unsigned v2, unsigned v3, i32x4 srd, unsigned soff) {
  asm volatile("s_mov_b32 m0, %0\n\ts_nop 0\n\t"
               "buffer_load_dwordx4 %1, %5, %6 offen nt lds\n\t"
               "buffer_load_dwordx4 %2, %5, %6 offen offset:1024 nt lds\n\t"
               "buffer_load_dwordx4 %3, %5, %6 offen offset:2048 nt lds\n\t"
               "buffer_load_dwordx4 %4, %5, %6 offen offset:3072 nt lds"
               ::"s"(lds), "v"(v0), "v"(v1), "v"(v2), "v"(v3), "s"(srd), "s"(soff) : "memory");
}
__device__ __forceinline__ i32x4 make_srd(const void* p, unsigned bytes) {
  unsigned long long a = (unsigned long long)p;
  i32x4 s;
  s[0] = (int)(unsigned)a; s[1] = (int)((unsigned)(a >> 32) & 0xffffu); s[2] = (int)bytes; s[3] = 0x00020000;
  return s;
}
__device__ __forceinline__ bf16x8 lds_rd128(ldsp p) { return *(LDS_AS bf16x8*)p; }
__device__ __forceinline__ bf16x8 lds_tr2(ldsp p0, ldsp p1) {
  bf16x4 a = __builtin_amdgcn_ds_read_tr16_b64_v4bf16((LDS_AS bf16x4*)p0);
  bf16x4 b = __builtin_amdgcn_ds_read_tr16_b64_v4bf16((LDS_AS bf16x4*)p1);
  return __builtin_shufflevector(a, b, 0, 1, 2, 3, 4, 5, 6, 7);
}
__device__ __forceinline__ f32x4 mfma(bf16x8 a, bf16x8 b, f32x4 c) { return __builtin_amdgcn_mfma_f32_16x16x32_bf16(a, b, c, 0, 0, 0); }
__device__ __forceinline__ bf16x4 cvt4(f32x4 v) {
  bf16x4 r; r[0] = (__bf16)v[0]; r[1] = (__bf16)v[1]; r[2] = (__bf16)v[2]; r[3] = (__bf16)v[3]; return r;
}
__device__ __forceinline__ bf16x8 cvt8(f32x4 a, f32x4 b) { return __builtin_shufflevector(cvt4(a), cvt4(b), 0, 1, 2, 3, 4, 5, 6, 7); }
__device__ __forceinline__ float max3(float a, float b, float c) { return __builtin_elementwise_maximum(__builtin_elementwise_maximum(a, b), c); }
__device__ __forceinline__ float max8(f32x4 a, f32x4 b) {
  return max3(max3(max3(a[0], a[1], a[2]), a[3], b[0]), max3(b[1], b[2], b[3]), b[3]);
}
__device__ __forceinline__ float xmax_g(float v) {
  auto r = __builtin_amdgcn_permlane16_swap(__float_as_uint(v), __float_as_uint(v), false, false);
  v = fmaxf(__uint_as_float(r[0]), __uint_as_float(r[1]));
  auto r2 = __builtin_amdgcn_permlane32_swap(__float_as_uint(v), __float_as_uint(v), false, false);
  return fmaxf(__uint_as_float(r2[0]), __uint_as_float(r2[1]));
}
__device__ __forceinline__ int xoff(int wd, int r, int qt) { return (r == 0 ? L_O2 : (r == 1 ? L_SLOT : 4 * L_SLOT)) + (wd * 3 + qt) * 1024; }

__global__ __launch_bounds__(512, 2) void gqa_fused(const __bf16* __restrict__ x, const __bf16* __restrict__ kc, const __bf16* __restrict__ vc,
                                                    const __bf16* __restrict__ wq, const __bf16* __restrict__ wo, __bf16* __restrict__ part) {
  extern __shared__ __attribute__((aligned(16))) char smem_raw[];
  ldsp sm = (ldsp)smem_raw;
  const unsigned smbase = (unsigned)(uintptr_t)sm;
  const int w = __builtin_amdgcn_readfirstlane(threadIdx.x >> 6);
  const int bid = blockIdx.x, kvh = bid & 7, jj = bid >> 3, b = jj >> 1, hp = jj & 1;
  STAMP(0);
  const int n0 = kvh * 512 + hp * 256;
  const i32x4 srd_k = make_srd(kc + (size_t)(b * HKV + kvh) * SK * DH, SK * DH * 2);
  const i32x4 srd_v = make_srd(vc + (size_t)(b * HKV + kvh) * SK * DH, SK * DH * 2);
  const i32x4 srd_o = make_srd(wo + (size_t)n0 * DM, 256 * DM * 2);
  const int h = w >> 2, kq = w & 3;
  f32x4 oacc[8][3];
  f32x4 lacc[3];
  float mref[3];
  {
    const int l = threadIdx.x & 63, c = l & 15, g = l >> 4, qq = c >> 2, pp = l & 3;
    const unsigned kv0 = (unsigned)(g * 256 + ((c ^ g) << 4));
    const unsigned vv0 = (unsigned)(g * 256 + ((c ^ (2 * g)) << 4));
    if (w < 4) {
      f32x4 qacc[4][3];
      const i32x4 srd_x = make_srd(x + (size_t)b * SQ * DM, SQ * DM * 2);
      const i32x4 srd_w = make_srd(wq, DM * HQ * DH * 2);
#pragma unroll
      for (int i = 0; i < 3; ++i) {
        const int r = w * 12 + 4 * i;
        dma4(smbase + L_X + r * 1024, (unsigned)((l ^ (r & 15)) * 16), (unsigned)((l ^ ((r + 1) & 15)) * 16), (unsigned)((l ^ ((r + 2) & 15)) * 16),
             (unsigned)((l ^ ((r + 3) & 15)) * 16), srd_x, (unsigned)(r * 1024));
      }
      const unsigned ring = smbase + L_RING + w * (NRING * 4096);
      const unsigned wv0 = (unsigned)((l >> 3) * 8192 + (((l & 7) ^ (((l >> 4) & 1) << 1)) * 16));
      const unsigned wv1 = (wv0 ^ 64u) + (65536u - 1024u), wv2 = wv0 + 2u * (65536u - 1024u), wv3 = (wv0 ^ 64u) + 3u * (65536u - 1024u);
      const unsigned scol = (unsigned)((n0 + 64 * w) * 2);
#define WQ_ISSUE(s) dma4(ring + ((s) % NRING) * 4096, wv0, wv1, wv2, wv3, srd_w, scol + (unsigned)((((s) + b) & 15) * (32 * 8192)))
      WQ_ISSUE(0); WQ_ISSUE(1); WQ_ISSUE(2); WQ_ISSUE(3);
      waitv<16>();
      STAMP(1); BAR(); STAMP(2);
      const int fA = ((qq >> 1) & 1) | ((g & 1) << 1);
      ldsp wqa = sm + L_RING + w * (NRING * 4096) + (8 * g + qq) * 128 + 8 * pp;
      ldsp xa = sm + L_X + c * 1024;
      bf16x8 fw[2][4], fx[2][3];
#define A_READ(s, buf)                                                                                          \
  do {                                                                                                          \
    _Pragma("unroll") for (int t_ = 0; t_ < 4; ++t_) {                                                          \
      ldsp p_ = wqa + ((s) % NRING) * 4096 + ((t_ ^ fA) << 5);                                                  \
      fw[buf][t_] = lds_tr2(p_, p_ + 512);                                                                      \
    }                                                                                                           \
    _Pragma("unroll") for (int q_ = 0; q_ < 3; ++q_)                                                            \
        fx[buf][q_] = lds_rd128(xa + q_ * 16384 + ((((4 * (((s) + b) & 15)) | g) ^ c) << 4));                   \
  } while (0)
      waitv<12>();
      A_READ(0, 0);
#pragma unroll
      for (int s = 0; s < 16; ++s) {
        if (s + 4 < 16) WQ_ISSUE(s + 4);
        if (s == 12) {
          dma4nt(smbase + (32 * w) * 256, kv0, kv0 ^ 64u, kv0 ^ 128u, kv0 ^ 192u, srd_k, (unsigned)(128 * w * 256));
          dma4nt(smbase + (32 * w + 16) * 256, kv0, kv0 ^ 64u, kv0 ^ 128u, kv0 ^ 192u, srd_k, (unsigned)((128 * w + 16) * 256));
        }
        if (s + 1 < 16) {
          const int rem = ((s + 4 < 16) ? (s + 4) : 15) - (s + 1);
          if (s < 12) waitv<12>(); else if (rem == 2) waitv<16>(); else if (rem == 1) waitv<12>(); else waitv<8>();
          A_READ(s + 1, (s + 1) & 1);
        }
        SCHED();
#pragma unroll
        for (int t = 0; t < 4; ++t)
#pragma unroll
          for (int qt = 0; qt < 3; ++qt) qacc[t][qt] = mfma(fw[s & 1][t], fx[s & 1][qt], s == 0 ? f32x4{0.f, 0.f, 0.f, 0.f} : qacc[t][qt]);
        SCHED();
      }
      STAMP(3); BAR(); STAMP(4);
#pragma unroll
      for (int t = 0; t < 4; ++t)
#pragma unroll
        for (int qt = 0; qt < 3; ++qt) {
          const int c16 = 8 * (w & 1) + 2 * t + (g >> 1);
          *(LDS_AS bf16x4*)(sm + L_Q + (48 * (w >> 1) + 16 * qt + c) * 256 + ((c16 ^ c) << 4) + 8 * (g & 1)) = cvt4(qacc[t][qt] * SCL2E);
        }
    } else {
      STAMP(1); BAR(); STAMP(2);
      STAMP(3); BAR(); STAMP(4);
    }
    const unsigned kvrow = (unsigned)(128 * kq * 256);
#define K_ISSUE(j, slot)                                                                                                                  \
  do { if (h == 0) {                                                                                                                      \
    dma4nt(smbase + (slot) * L_SLOT + kq * 8192, kv0, kv0 ^ 64u, kv0 ^ 128u, kv0 ^ 192u, srd_k, kvrow + (unsigned)((j) * 8192));          \
    dma4nt(smbase + (slot) * L_SLOT + kq * 8192 + 4096, kv0, kv0 ^ 64u, kv0 ^ 128u, kv0 ^ 192u, srd_k, kvrow + (unsigned)((j) * 8192 + 4096)); \
  } } while (0)
#define V_ISSUE(j, slot)                                                                                                                  \
  do { if (h == 0) {                                                                                                                      \
    dma4nt(smbase + (slot) * L_SLOT + kq * 8192, vv0, vv0 ^ 128u, vv0, vv0 ^ 128u, srd_v, kvrow + (unsigned)((j) * 8192));                \
    dma4nt(smbase + (slot) * L_SLOT + kq * 8192 + 4096, vv0, vv0 ^ 128u, vv0, vv0 ^ 128u, srd_v, kvrow + (unsigned)((j) * 8192 + 4096));  \
  } } while (0)
    const unsigned ov0 = (unsigned)((l >> 3) * 1024 + (((l & 7) ^ (g << 1)) << 4));
#define WO_ISSUE_W(u, slot, ww) dma4(smbase + (slot) * L_SLOT + (ww) * 4096, ov0, ov0 + 7168u, ov0 + 14336u, ov0 + 21504u, srd_o, (unsigned)((ww) * 128 + (((u) + b) & 7) * 32768))
#define WO_ISSUE(u, slot) WO_ISSUE_W(u, slot, w)
#define WO_ISSUE2(u, slot) do { if (h == 0) { WO_ISSUE_W(u, slot, w); WO_ISSUE_W(u, slot, w + 4); } } while (0)
    K_ISSUE(1, 1);
    waitv<8>();
    STAMP(5); BAR(); STAMP(6);

    bf16x8 qf[4][3];
#pragma unroll
    for (int s = 0; s < 4; ++s)
#pragma unroll
      for (int qt = 0; qt < 3; ++qt) qf[s][qt] = lds_rd128(sm + L_Q + (48 * h + 16 * qt + c) * 256 + ((((4 * s) | g) ^ c) << 4));
    V_ISSUE(0, 2);
    ldsp ka = sm + (32 * kq + c) * 256;
    const int fV = 4 * (g & 1) + qq;
    unsigned va[4];
#pragma unroll
    for (int r = 0; r < 4; ++r) va[r] = smbase + (32 * kq + 4 * g + qq) * 256 + 8 * pp + (((2 * ((r + kq) & 3)) ^ fV) << 5);
    bf16x8 ones;
#pragma unroll
    for (int e = 0; e < 8; ++e) ones[e] = (__bf16)1.0f;
    f32x4 negm[3];
    constexpr int kslot[4] = {0, 1, 3, 4}, vslot[4] = {2, 0, 1, 2};
#define SYNC(j)                                                               \
  do {                                                                        \
    if ((j) < 3) waitv<8>(); else waitv<16>();                                \
    if ((j) == 1 && w == 3) waitv<0>();                                       \
    BAR(); STAMP(7 + (j));                                                    \
    if ((j) == 0) { V_ISSUE(1, 0); }                                          \
    if ((j) == 1) { if (w == 3) K_ISSUE(3, 4); V_ISSUE(2, 1); }               \
    if ((j) == 2) { WO_ISSUE2(0, 3); }                                        \
  } while (0)
#pragma unroll
    for (int j = 0; j < 4; ++j) {
      f32x4 sacc[2][3];
      {
        unsigned kso = (unsigned)(kslot[j] * L_SLOT);
        asm volatile("" : "+s"(kso));
        bf16x8 kf[2][4];
#pragma unroll
        for (int s = 0; s < 4; ++s)
#pragma unroll
          for (int kt = 0; kt < 2; ++kt) kf[kt][s] = lds_rd128(ka + kso + kt * 4096 + ((((4 * s) | g) ^ c) << 4));
        SCHED();
#pragma unroll
        for (int s = 0; s < 4; ++s)
#pragma unroll
          for (int kt = 0; kt < 2; ++kt)
#pragma unroll
            for (int qt = 0; qt < 3; ++qt)
              sacc[kt][qt] = mfma(kf[kt][s], qf[s][qt], s == 0 ? (j == 0 ? f32x4{0.f, 0.f, 0.f, 0.f} : negm[qt]) : sacc[kt][qt]);
        SCHED();
        if (j == 0) K_ISSUE(2, 3);
      }
      bool resc = false;
      float al[3];
      if (j == 0) {
#pragma unroll
        for (int qt = 0; qt < 3; ++qt) {
          const float m = xmax_g(max8(sacc[0][qt], sacc[1][qt]));
          if (g == 0) *(LDS_AS float*)(sm + L_ST1 + ((w * 3 + qt) * 16 + c) * 4) = m;
        }
        SYNC(0);
#pragma unroll
        for (int qt = 0; qt < 3; ++qt) {
          float m = *(LDS_AS float*)(sm + L_ST1 + ((h * 12 + qt) * 16 + c) * 4);
#pragma unroll
          for (int k = 1; k < 4; ++k) m = fmaxf(m, *(LDS_AS float*)(sm + L_ST1 + ((h * 12 + 3 * k + qt) * 16 + c) * 4));
          mref[qt] = m;
          negm[qt] = f32x4{-m, -m, -m, -m};
          sacc[0][qt] += negm[qt];
          sacc[1][qt] += negm[qt];
        }
      } else {
        if (h == 0) SYNC(j);
        float t = max3(sacc[0][0][0], sacc[0][0][1], sacc[0][0][2]);
        t = max3(t, sacc[0][0][3], sacc[1][0][0]); t = max3(t, sacc[1][0][1], sacc[1][0][2]);
        t = max3(t, sacc[1][0][3], sacc[0][1][0]); t = max3(t, sacc[0][1][1], sacc[0][1][2]);
        t = max3(t, sacc[0][1][3], sacc[1][1][0]); t = max3(t, sacc[1][1][1], sacc[1][1][2]);
        t = max3(t, sacc[1][1][3], sacc[0][2][0]); t = max3(t, sacc[0][2][1], sacc[0][2][2]);
        t = max3(t, sacc[0][2][3], sacc[1][2][0]); t = max3(t, sacc[1][2][1], sacc[1][2][2]);
        t = max3(t, sacc[1][2][3], t);
        resc = __any(!(t <= RESC_T));
        if (__builtin_expect(resc, 0)) {
#pragma unroll
          for (int qt = 0; qt < 3; ++qt) {
            const float d = fmaxf(xmax_g(max8(sacc[0][qt], sacc[1][qt])), 0.f);
            al[qt] = __builtin_amdgcn_exp2f(-d);
            mref[qt] += d;
            negm[qt] -= d;
            sacc[0][qt] -= d;
            sacc[1][qt] -= d;
          }
        }
      }
      bf16x8 pf[3];
#pragma unroll
      for (int qt = 0; qt < 3; ++qt) {
#pragma unroll
        for (int kt = 0; kt < 2; ++kt)
#pragma unroll
          for (int r = 0; r < 4; ++r) sacc[kt][qt][r] = __builtin_amdgcn_exp2f(sacc[kt][qt][r]);
        pf[qt] = cvt8(sacc[0][qt], sacc[1][qt]);
        asm volatile("" : "+v"(pf[qt]));
      }
      SCHED();
      if (j > 0) {
        if (h == 1) SYNC(j);
        if (__builtin_expect(resc, 0)) {
#pragma unroll
          for (int qt = 0; qt < 3; ++qt) {
            lacc[qt] *= al[qt];
#pragma unroll
            for (int i = 0; i < 8; ++i) oacc[i][qt] *= al[qt];
          }
        }
      }
      {
        unsigned vso = (unsigned)(vslot[j] * L_SLOT);
        asm volatile("" : "+s"(vso));
        bf16x8 vf[8];
#pragma unroll
        for (int i = 0; i < 8; ++i) {
          ldsp p = (ldsp)((va[i >> 1] ^ (unsigned)((i & 1) << 5)) + vso);
          vf[i] = lds_tr2(p, p + 4096);
        }
        SCHED();
#pragma unroll
        for (int qt = 0; qt < 3; ++qt) lacc[qt] = mfma(ones, pf[qt], j == 0 ? f32x4{0.f, 0.f, 0.f, 0.f} : lacc[qt]);
#pragma unroll
        for (int i = 0; i < 8; ++i)
#pragma unroll
          for (int qt = 0; qt < 3; ++qt) oacc[i][qt] = mfma(vf[i], pf[qt], j == 0 ? f32x4{0.f, 0.f, 0.f, 0.f} : oacc[i][qt]);
        SCHED();
        if (j == 0 && w != 3) K_ISSUE(3, 4);
        if (j == 1) V_ISSUE(3, 2);
        if (j == 2) WO_ISSUE2(1, 0);
      }
    }
    if (g == 0) {
#pragma unroll
      for (int qt = 0; qt < 3; ++qt) *(LDS_AS f32x2*)(sm + L_ST2 + ((w * 3 + qt) * 16 + c) * 8) = f32x2{mref[qt], lacc[qt][0]};
    }
    BAR(); STAMP(11);
  }
  int tz = threadIdx.x;
  asm volatile("" : "+v"(tz));
  const int l = tz & 63, c = l & 15, g = l >> 4, qq = c >> 2, pp = l & 3;
  {
    float inv[3], sc[3];
    bool differ = false;
#pragma unroll
    for (int qt = 0; qt < 3; ++qt) {
      f32x2 ml[4];
#pragma unroll
      for (int k = 0; k < 4; ++k) ml[k] = *(LDS_AS f32x2*)(sm + L_ST2 + ((h * 12 + 3 * k + qt) * 16 + c) * 8);
      const float m = fmaxf(fmaxf(ml[0][0], ml[1][0]), fmaxf(ml[2][0], ml[3][0]));
      float lt = 0.f;
#pragma unroll
      for (int k = 0; k < 4; ++k) lt += ml[k][1] * __builtin_amdgcn_exp2f(ml[k][0] - m);
      inv[qt] = __builtin_amdgcn_rcpf(lt);
      sc[qt] = __builtin_amdgcn_exp2f(mref[qt] - m);
      differ = differ || (mref[qt] != m);
    }
    if (__builtin_expect(__any(differ), 0)) {
#pragma unroll
      for (int qt = 0; qt < 3; ++qt)
#pragma unroll
        for (int i = 0; i < 8; ++i) oacc[i][qt] *= sc[qt];
    }
#pragma unroll
    for (int r = 1; r < 4; ++r) {
      const int wd = h * 4 + ((kq + r) & 3);
#pragma unroll
      for (int qt = 0; qt < 3; ++qt) *(LDS_AS bf16x8*)(sm + xoff(wd, r - 1, qt) + l * 16) = cvt8(oacc[2 * r][qt], oacc[2 * r + 1][qt]);
    }
    bf16x8 sel0, sel1;
#pragma unroll
    for (int e = 0; e < 4; ++e) {
      const __bf16 v = (4 * g + e == c) ? (__bf16)1.0f : (__bf16)0.0f;
      sel0[e] = v; sel0[4 + e] = (__bf16)0.0f; sel1[e] = (__bf16)0.0f; sel1[4 + e] = v;
    }
    BAR(); STAMP(17);
#pragma unroll
    for (int qt = 0; qt < 3; ++qt) {
#pragma unroll
      for (int r = 0; r < 3; ++r) {
        bf16x8 t = lds_rd128(sm + xoff(w, r, qt) + l * 16);
        oacc[0][qt] = mfma(sel0, t, oacc[0][qt]);
        oacc[1][qt] = mfma(sel1, t, oacc[1][qt]);
      }
    }
#pragma unroll
    for (int qt = 0; qt < 3; ++qt) *(LDS_AS bf16x8*)(sm + L_O2 + (w * 3 + qt) * 1024 + l * 16) = cvt8(oacc[0][qt] * inv[qt], oacc[1][qt] * inv[qt]);
  }
  waitv<0>();
  BAR(); STAMP(18);
  const unsigned ov0 = (unsigned)((l >> 3) * 1024 + (((l & 7) ^ (g << 1)) << 4));
  WO_ISSUE(2, 1); WO_ISSUE(3, 4);

  f32x4 cacc[4][3];
  {
    bf16x8 of[8][3];
#pragma unroll
    for (int u = 0; u < 8; ++u)
#pragma unroll
      for (int qt = 0; qt < 3; ++qt) of[u][qt] = lds_rd128(sm + L_O2 + (((u + b) & 7) * 3 + qt) * 1024 + l * 16);
    const int fC = 2 * (g & 1) + (qq >> 1);
    ldsp wra = sm + w * 4096 + (4 * g + qq) * 128 + 8 * pp;
    bf16x8 wf[2][4];
#define CSLOT(u) (((u) & 3) == 0 ? 3 : (((u) & 3) == 1 ? 0 : (((u) & 3) == 2 ? 1 : 4)))
#define C_READ(u)                                                                                                   \
  do {                                                                                                              \
    _Pragma("unroll") for (int t_ = 0; t_ < 4; ++t_) {                                                              \
      ldsp p_ = wra + CSLOT(u) * L_SLOT + ((t_ ^ fC) << 5);                                                         \
      wf[(u) & 1][t_] = lds_tr2(p_, p_ + 2048);                                                                     \
    }                                                                                                               \
  } while (0)
    C_READ(0);
#pragma unroll
    for (int u = 0; u < 8; ++u) {
      if (u >= 1 && u + 3 < 8) WO_ISSUE(u + 3, CSLOT(u + 3));
      if (u + 1 < 8) {
        const int last = (u + 3 < 8) ? (u + 3) : 7;
        const int rem = last - (u + 1);
        if (rem == 2) waitv<8>(); else if (rem == 1) waitv<4>(); else waitv<0>();
        C_READ(u + 1);
      }
      SCHED();
#pragma unroll
      for (int t = 0; t < 4; ++t)
#pragma unroll
        for (int qt = 0; qt < 3; ++qt) cacc[t][qt] = mfma(wf[u & 1][t], of[u][qt], u == 0 ? f32x4{0.f, 0.f, 0.f, 0.f} : cacc[t][qt]);
      SCHED();
    }
  }
  STAMP(19);
  bf16x8* pp8 = (bf16x8*)part + ((size_t)(b * 16 + kvh * 2 + hp) * 8 + w) * 6 * 64 + l;
#pragma unroll
  for (int a = 0; a < 2; ++a)
#pragma unroll
    for (int qt = 0; qt < 3; ++qt) pp8[(a * 3 + qt) * 64] = cvt8(cacc[2 * a][qt], cacc[2 * a + 1][qt]);
}

__global__ __launch_bounds__(192) void gqa_reduce(const __bf16* __restrict__ part, __bf16* __restrict__ out) {
  const int idx = blockIdx.x * 192 + threadIdx.x;
  const int b = idx / 3072, rem = idx - b * 3072;
  const bf16x8* p = (const bf16x8*)part + (size_t)b * 16 * 3072 + rem;
  bf16x8 v[16];
#pragma unroll
  for (int s = 0; s < 16; ++s) v[s] = p[(size_t)s * 3072];
  f32x4 a0 = {0.f, 0.f, 0.f, 0.f}, a1 = {0.f, 0.f, 0.f, 0.f};
#pragma unroll
  for (int s = 0; s < 16; ++s)
#pragma unroll
    for (int e = 0; e < 4; ++e) { a0[e] += (float)v[s][e]; a1[e] += (float)v[s][4 + e]; }
  const int lane = rem & 63, tq = rem >> 6, qt = tq % 3, wa = tq / 3;
  const int q = 16 * qt + (lane & 15), n = 32 * wa + 4 * (lane >> 4);
  __bf16* o = out + ((size_t)b * SQ + q) * DM + n;
  *(bf16x4*)o = cvt4(a0);
  *(bf16x4*)(o + 16) = cvt4(a1);
}

extern "C" void kernel_launch(void* const* d_in, const int* in_sizes, int n_in, void* d_out, int out_size, void* d_ws, size_t ws_size,
                              hipStream_t stream) {
  const __bf16* x = (const __bf16*)d_in[0];
  const __bf16* kc = (const __bf16*)d_in[1];
  const __bf16* vc = (const __bf16*)d_in[2];
  const __bf16* wq = (const __bf16*)d_in[3];
  const __bf16* wo = (const __bf16*)d_in[4];
  __bf16* part = (__bf16*)d_ws;
  (void)hipFuncSetAttribute((const void*)gqa_fused, hipFuncAttributeMaxDynamicSharedMemorySize, LDS_BYTES);
  hipLaunchKernelGGL(gqa_fused, dim3(256), dim3(512), LDS_BYTES, stream, x, kc, vc, wq, wo, part);
  hipLaunchKernelGGL(gqa_reduce, dim3(256), dim3(192), 0, stream, (const __bf16*)part, (__bf16*)d_out);
}
```

```cpp
#include <hip/hip_runtime.h>
#include <hip/hip_bf16.h>
#include <stdint.h>

typedef __bf16 bf16x8 __attribute__((ext_vector_type(8)));
typedef __bf16 bf16x4 __attribute__((ext_vector_type(4)));
typedef float f32x4 __attribute__((ext_vector_type(4)));
typedef float f32x2 __attribute__((ext_vector_type(2)));
typedef int i32x4 __attribute__((ext_vector_type(4)));
typedef unsigned u32x4 __attribute__((ext_vector_type(4)));
#define LDS_AS __attribute__((address_space(3)))
typedef LDS_AS char* ldsp;

constexpr int B_ = 16, SQ = 48, DM = 512, HQ = 32, HKV = 8, SK = 512, DH = 128;
constexpr float SCL2E = 0.08838834764831845f * 1.4426950408889634f;

constexpr int L_SLOT = 32768;
constexpr int L_RING = 32768;
constexpr int L_X = 114688;
constexpr int L_Q = 131072;
constexpr int L_ST1 = 155648;
constexpr int L_ST2 = 32768 + 24576;
constexpr int L_O2 = 2 * 32768;
constexpr int LDS_BYTES = 163840;
constexpr int NRING = 5;
#define RESC_T 16.0f

#define BAR() asm volatile("s_waitcnt lgkmcnt(0)\n\ts_barrier" ::: "memory")
#define SCHED() __builtin_amdgcn_sched_barrier(0)
#define STAMP(i)
template <int N> __device__ __forceinline__ void waitv() { asm volatile("s_waitcnt vmcnt(%0)" ::"i"(N) : "memory"); }

__device__ __forceinline__ void dma4(unsigned lds, unsigned v0, unsigned v1, unsigned v2, unsigned v3, i32x4 srd, unsigned soff) {
  asm volatile("s_mov_b32 m0, %0\n\ts_nop 0\n\t"
               "buffer_load_dwordx4 %1, %5, %6 offen lds\n\t"
               "buffer_load_dwordx4 %2, %5, %6 offen offset:1024 lds\n\t"
               "buffer_load_dwordx4 %3, %5, %6 offen offset:2048 lds\n\t"
               "buffer_load_dwordx4 %4, %5, %6 offen offset:3072 lds"
               ::"s"(lds), "v"(v0), "v"(v1), "v"(v2), "v"(v3), "s"(srd), "s"(soff) : "memory");
}
__device__ __forceinline__ void dma4nt(unsigned lds, unsigned v0, unsigned v1, unsigned v2, unsigned v3, i32x4 srd, unsigned soff) {
  asm volatile("s_mov_b32 m0, %0\n\ts_nop 0\n\t"
               "buffer_load_dwordx4 %1, %5, %6 offen nt lds\n\t"
               "buffer_load_dwordx4 %2, %5, %6 offen offset:1024 nt lds\n\t"
               "buffer_load_dwordx4 %3, %5, %6 offen offset:2048 nt lds\n\t"
               "buffer_load_dwordx4 %4, %5, %6 offen offset:3072 nt lds"
               ::"s"(lds), "v"(v0), "v"(v1), "v"(v2), "v"(v3), "s"(srd), "s"(soff) : "memory");
}
__device__ __forceinline__ i32x4 make_srd(const void* p, unsigned bytes) {
  unsigned long long a = (unsigned long long)p;
  i32x4 s;
  s[0] = (int)(unsigned)a; s[1] = (int)((unsigned)(a >> 32) & 0xffffu); s[2] = (int)bytes; s[3] = 0x00020000;
  return s;
}
__device__ __forceinline__ bf16x8 lds_rd128(ldsp p) { return *(LDS_AS bf16x8*)p; }
__device__ __forceinline__ bf16x8 lds_tr2(ldsp p0, ldsp p1) {
  bf16x4 a = __builtin_amdgcn_ds_read_tr16_b64_v4bf16((LDS_AS bf16x4*)p0);
  bf16x4 b = __builtin_amdgcn_ds_read_tr16_b64_v4bf16((LDS_AS bf16x4*)p1);
  return __builtin_shufflevector(a, b, 0, 1, 2, 3, 4, 5, 6, 7);
}
__device__ __forceinline__ f32x4 mfma(bf16x8 a, bf16x8 b, f32x4 c) { return __builtin_amdgcn_mfma_f32_16x16x32_bf16(a, b, c, 0, 0, 0); }
__device__ __forceinline__ bf16x4 cvt4(f32x4 v) {
  bf16x4 r; r[0] = (__bf16)v[0]; r[1] = (__bf16)v[1]; r[2] = (__bf16)v[2]; r[3] = (__bf16)v[3]; return r;
}
__device__ __forceinline__ bf16x8 cvt8(f32x4 a, f32x4 b) { return __builtin_shufflevector(cvt4(a), cvt4(b), 0, 1, 2, 3, 4, 5, 6, 7); }
__device__ __forceinline__ float max3(float a, float b, float c) { return __builtin_elementwise_maximum(__builtin_elementwise_maximum(a, b), c); }
__device__ __forceinline__ float max8(f32x4 a, f32x4 b) {
  return max3(max3(max3(a[0], a[1], a[2]), a[3], b[0]), max3(b[1], b[2], b[3]), b[3]);
}
__device__ __forceinline__ float xmax_g(float v) {
  auto r = __builtin_amdgcn_permlane16_swap(__float_as_uint(v), __float_as_uint(v), false, false);
  v = fmaxf(__uint_as_float(r[0]), __uint_as_float(r[1]));
  auto r2 = __builtin_amdgcn_permlane32_swap(__float_as_uint(v), __float_as_uint(v), false, false);
  return fmaxf(__uint_as_float(r2[0]), __uint_as_float(r2[1]));
}
__device__ __forceinline__ int xoff(int wd, int r, int qt) { return (r == 0 ? L_O2 : (r == 1 ? L_SLOT : 4 * L_SLOT)) + (wd * 3 + qt) * 1024; }

__global__ __launch_bounds__(512, 2) void gqa_fused(const __bf16* __restrict__ x, const __bf16* __restrict__ kc, const __bf16* __restrict__ vc,
                                                    const __bf16* __restrict__ wq, const __bf16* __restrict__ wo, __bf16* __restrict__ part) {
  extern __shared__ __attribute__((aligned(16))) char smem_raw[];
  ldsp sm = (ldsp)smem_raw;
  const unsigned smbase = (unsigned)(uintptr_t)sm;
  const int w = __builtin_amdgcn_readfirstlane(threadIdx.x >> 6);
  const int bid = blockIdx.x, kvh = bid & 7, jj = bid >> 3, b = jj >> 1, hp = jj & 1;
  STAMP(0);
  const int n0 = kvh * 512 + hp * 256;
  const i32x4 srd_k = make_srd(kc + (size_t)(b * HKV + kvh) * SK * DH, SK * DH * 2);
  const i32x4 srd_v = make_srd(vc + (size_t)(b * HKV + kvh) * SK * DH, SK * DH * 2);
  const i32x4 srd_o = make_srd(wo + (size_t)n0 * DM, 256 * DM * 2);
  const int h = w >> 2, kq = w & 3;
  f32x4 oacc[8][3];
  f32x4 lacc[3];
  float mref[3];
  {
    const int l = threadIdx.x & 63, c = l & 15, g = l >> 4, qq = c >> 2, pp = l & 3;
    const unsigned kv0 = (unsigned)(g * 256 + ((c ^ g) << 4));
    const unsigned vv0 = (unsigned)(g * 256 + ((c ^ (2 * g)) << 4));
    if (w < 4) {
      f32x4 qacc[4][3];
      const i32x4 srd_x = make_srd(x + (size_t)b * SQ * DM, SQ * DM * 2);
      const i32x4 srd_w = make_srd(wq, DM * HQ * DH * 2);
#pragma unroll
      for (int i = 0; i < 3; ++i) {
        const int r = w * 12 + 4 * i;
        dma4(smbase + L_X + r * 1024, (unsigned)((l ^ (r & 15)) * 16), (unsigned)((l ^ ((r + 1) & 15)) * 16), (unsigned)((l ^ ((r + 2) & 15)) * 16),
             (unsigned)((l ^ ((r + 3) & 15)) * 16), srd_x, (unsigned)(r * 1024));
      }
      const unsigned ring = smbase + L_RING + w * (NRING * 4096);
      const unsigned wv0 = (unsigned)((l >> 3) * 8192 + (((l & 7) ^ (((l >> 4) & 1) << 1)) * 16));
      const unsigned wv1 = (wv0 ^ 64u) + (65536u - 1024u), wv2 = wv0 + 2u * (65536u - 1024u), wv3 = (wv0 ^ 64u) + 3u * (65536u - 1024u);
      const unsigned scol = (unsigned)((n0 + 64 * w) * 2);
#define WQ_ISSUE(s) dma4(ring + ((s) % NRING) * 4096, wv0, wv1, wv2, wv3, srd_w, scol + (unsigned)((((s) + b) & 15) * (32 * 8192)))
      WQ_ISSUE(0); WQ_ISSUE(1); WQ_ISSUE(2); WQ_ISSUE(3);
      waitv<16>();
      STAMP(1); BAR(); STAMP(2);
      const int fA = ((qq >> 1) & 1) | ((g & 1) << 1);
      ldsp wqa = sm + L_RING + w * (NRING * 4096) + (8 * g + qq) * 128 + 8 * pp;
      ldsp xa = sm + L_X + c * 1024;
      bf16x8 fw[2][4], fx[2][3];
#define A_READ(s, buf)                                                                                          \
  do {                                                                                                          \
    _Pragma("unroll") for (int t_ = 0; t_ < 4; ++t_) {                                                          \
      ldsp p_ = wqa + ((s) % NRING) * 4096 + ((t_ ^ fA) << 5);                                                  \
      fw[buf][t_] = lds_tr2(p_, p_ + 512);                                                                      \
    }                                                                                                           \
    _Pragma("unroll") for (int q_ = 0; q_ < 3; ++q_)                                                            \
        fx[buf][q_] = lds_rd128(xa + q_ * 16384 + ((((4 * (((s) + b) & 15)) | g) ^ c) << 4));                   \
  } while (0)
      waitv<12>();
      A_READ(0, 0);
#pragma unroll
      for (int s = 0; s < 16; ++s) {
        if (s + 4 < 16) WQ_ISSUE(s + 4);
        if (s == 12) {
          dma4nt(smbase + (32 * w) * 256, kv0, kv0 ^ 64u, kv0 ^ 128u, kv0 ^ 192u, srd_k, (unsigned)(128 * w * 256));
          dma4nt(smbase + (32 * w + 16) * 256, kv0, kv0 ^ 64u, kv0 ^ 128u, kv0 ^ 192u, srd_k, (unsigned)((128 * w + 16) * 256));
        }
        if (s + 1 < 16) {
          const int rem = ((s + 4 < 16) ? (s + 4) : 15) - (s + 1);
          if (s < 12) waitv<12>(); else if (rem == 2) waitv<16>(); else if (rem == 1) waitv<12>(); else waitv<8>();
          A_READ(s + 1, (s + 1) & 1);
        }
        SCHED();
#pragma unroll
        for (int t = 0; t < 4; ++t)
#pragma unroll
          for (int qt = 0; qt < 3; ++qt) qacc[t][qt] = mfma(fw[s & 1][t], fx[s & 1][qt], s == 0 ? f32x4{0.f, 0.f, 0.f, 0.f} : qacc[t][qt]);
        SCHED();
      }
      STAMP(3); BAR(); STAMP(4);
#pragma unroll
      for (int t = 0; t < 4; ++t)
#pragma unroll
        for (int qt = 0; qt < 3; ++qt) {
          const int c16 = 8 * (w & 1) + 2 * t + (g >> 1);
          *(LDS_AS bf16x4*)(sm + L_Q + (48 * (w >> 1) + 16 * qt + c) * 256 + ((c16 ^ c) << 4) + 8 * (g & 1)) = cvt4(qacc[t][qt] * SCL2E);
        }
    } else {
      STAMP(1); BAR(); STAMP(2);
      STAMP(3); BAR(); STAMP(4);
    }
    const unsigned kvrow = (unsigned)(128 * kq * 256);
#define K_ISSUE(j, slot)                                                                                                                  \
  do { if (h == 0) {                                                                                                                      \
    dma4nt(smbase + (slot) * L_SLOT + kq * 8192, kv0, kv0 ^ 64u, kv0 ^ 128u, kv0 ^ 192u, srd_k, kvrow + (unsigned)((j) * 8192));          \
    dma4nt(smbase + (slot) * L_SLOT + kq * 8192 + 4096, kv0, kv0 ^ 64u, kv0 ^ 128u, kv0 ^ 192u, srd_k, kvrow + (unsigned)((j) * 8192 + 4096)); \
  } } while (0)
#define V_ISSUE(j, slot)                                                                                                                  \
  do { if (h == 0) {                                                                                                                      \
    dma4nt(smbase + (slot) * L_SLOT + kq * 8192, vv0, vv0 ^ 128u, vv0, vv0 ^ 128u, srd_v, kvrow + (unsigned)((j) * 8192));                \
    dma4nt(smbase + (slot) * L_SLOT + kq * 8192 + 4096, vv0, vv0 ^ 128u, vv0, vv0 ^ 128u, srd_v, kvrow + (unsigned)((j) * 8192 + 4096));  \
  } } while (0)
    const unsigned ov0 = (unsigned)((l >> 3) * 1024 + (((l & 7) ^ (g << 1)) << 4));
#define WO_ISSUE_W(u, slot, ww) dma4(smbase + (slot) * L_SLOT + (ww) * 4096, ov0, ov0 + 7168u, ov0 + 14336u, ov0 + 21504u, srd_o, (unsigned)((ww) * 128 + (((u) + b) & 7) * 32768))
#define WO_ISSUE(u, slot) WO_ISSUE_W(u, slot, w)
#define WO_ISSUE2(u, slot) do { if (h == 0) { WO_ISSUE_W(u, slot, w); WO_ISSUE_W(u, slot, w + 4); } } while (0)
    K_ISSUE(1, 1);
    waitv<8>();
    STAMP(5); BAR(); STAMP(6);

    bf16x8 qf[4][3];
#pragma unroll
    for (int s = 0; s < 4; ++s)
#pragma unroll
      for (int qt = 0; qt < 3; ++qt) qf[s][qt] = lds_rd128(sm + L_Q + (48 * h + 16 * qt + c) * 256 + ((((4 * s) | g) ^ c) << 4));
    V_ISSUE(0, 2);
    ldsp ka = sm + (32 * kq + c) * 256;
    const int fV = 4 * (g & 1) + qq;
    unsigned va[4];
#pragma unroll
    for (int r = 0; r < 4; ++r) va[r] = smbase + (32 * kq + 4 * g + qq) * 256 + 8 * pp + (((2 * ((r + kq) & 3)) ^ fV) << 5);
    bf16x8 ones;
#pragma unroll
    for (int e = 0; e < 8; ++e) ones[e] = (__bf16)1.0f;
    f32x4 negm[3];
    constexpr int kslot[4] = {0, 1, 3, 4}, vslot[4] = {2, 0, 1, 2};
#define SYNC(j)                                                               \
  do {                                                                        \
    if ((j) < 3) waitv<8>(); else waitv<16>();                                \
    if ((j) == 1 && w == 3) waitv<0>();                                       \
    BAR(); STAMP(7 + (j));                                                    \
    if ((j) == 0) { V_ISSUE(1, 0); }                                          \
    if ((j) == 1) { if (w == 3) K_ISSUE(3, 4); V_ISSUE(2, 1); }               \
    if ((j) == 2) { WO_ISSUE2(0, 3); }                                        \
  } while (0)
#pragma unroll
    for (int j = 0; j < 4; ++j) {
      f32x4 sacc[2][3];
      {
        unsigned kso = (unsigned)(kslot[j] * L_SLOT);
        asm volatile("" : "+s"(kso));
        bf16x8 kf[2][4];
#pragma unroll
        for (int s = 0; s < 4; ++s)
#pragma unroll
          for (int kt = 0; kt < 2; ++kt) kf[kt][s] = lds_rd128(ka + kso + kt * 4096 + ((((4 * s) | g) ^ c) << 4));
        SCHED();
#pragma unroll
        for (int s = 0; s < 4; ++s)
#pragma unroll
          for (int kt = 0; kt < 2; ++kt)
#pragma unroll
            for (int qt = 0; qt < 3; ++qt)
              sacc[kt][qt] = mfma(kf[kt][s], qf[s][qt], s == 0 ? (j == 0 ? f32x4{0.f, 0.f, 0.f, 0.f} : negm[qt]) : sacc[kt][qt]);
        SCHED();
        if (j == 0) K_ISSUE(2, 3);
      }
      bool resc = false;
      float al[3];
      if (j == 0) {
#pragma unroll
        for (int qt = 0; qt < 3; ++qt) {
          const float m = xmax_g(max8(sacc[0][qt], sacc[1][qt]));
          if (g == 0) *(LDS_AS float*)(sm + L_ST1 + ((w * 3 + qt) * 16 + c) * 4) = m;
        }
        SYNC(0);
#pragma unroll
        for (int qt = 0; qt < 3; ++qt) {
          float m = *(LDS_AS float*)(sm + L_ST1 + ((h * 12 + qt) * 16 + c) * 4);
#pragma unroll
          for (int k = 1; k < 4; ++k) m = fmaxf(m, *(LDS_AS float*)(sm + L_ST1 + ((h * 12 + 3 * k + qt) * 16 + c) * 4));
          mref[qt] = m;
          negm[qt] = f32x4{-m, -m, -m, -m};
          sacc[0][qt] += negm[qt];
          sacc[1][qt] += negm[qt];
        }
      } else {
        if (h == 0) SYNC(j);
        float t = max3(sacc[0][0][0], sacc[0][0][1], sacc[0][0][2]);
        t = max3(t, sacc[0][0][3], sacc[1][0][0]); t = max3(t, sacc[1][0][1], sacc[1][0][2]);
        t = max3(t, sacc[1][0][3], sacc[0][1][0]); t = max3(t, sacc[0][1][1], sacc[0][1][2]);
        t = max3(t, sacc[0][1][3], sacc[1][1][0]); t = max3(t, sacc[1][1][1], sacc[1][1][2]);
        t = max3(t, sacc[1][1][3], sacc[0][2][0]); t = max3(t, sacc[0][2][1], sacc[0][2][2]);
        t = max3(t, sacc[0][2][3], sacc[1][2][0]); t = max3(t, sacc[1][2][1], sacc[1][2][2]);
        t = max3(t, sacc[1][2][3], t);
        resc = __any(!(t <= RESC_T));
        if (__builtin_expect(resc, 0)) {
#pragma unroll
          for (int qt = 0; qt < 3; ++qt) {
            const float d = fmaxf(xmax_g(max8(sacc[0][qt], sacc[1][qt])), 0.f);
            al[qt] = __builtin_amdgcn_exp2f(-d);
            mref[qt] += d;
            negm[qt] -= d;
            sacc[0][qt] -= d;
            sacc[1][qt] -= d;
          }
        }
      }
      bf16x8 pf[3];
#pragma unroll
      for (int qt = 0; qt < 3; ++qt) {
#pragma unroll
        for (int kt = 0; kt < 2; ++kt)
#pragma unroll
          for (int r = 0; r < 4; ++r) sacc[kt][qt][r] = __builtin_amdgcn_exp2f(sacc[kt][qt][r]);
        pf[qt] = cvt8(sacc[0][qt], sacc[1][qt]);
        asm volatile("" : "+v"(pf[qt]));
      }
      SCHED();
      if (j > 0) {
        if (h == 1) SYNC(j);
        if (__builtin_expect(resc, 0)) {
#pragma unroll
          for (int qt = 0; qt < 3; ++qt) {
            lacc[qt] *= al[qt];
#pragma unroll
            for (int i = 0; i < 8; ++i) oacc[i][qt] *= al[qt];
          }
        }
      }
      {
        unsigned vso = (unsigned)(vslot[j] * L_SLOT);
        asm volatile("" : "+s"(vso));
        bf16x8 vf[8];
#pragma unroll
        for (int i = 0; i < 8; ++i) {
          ldsp p = (ldsp)((va[i >> 1] ^ (unsigned)((i & 1) << 5)) + vso);
          vf[i] = lds_tr2(p, p + 4096);
        }
        SCHED();
#pragma unroll
        for (int qt = 0; qt < 3; ++qt) lacc[qt] = mfma(ones, pf[qt], j == 0 ? f32x4{0.f, 0.f, 0.f, 0.f} : lacc[qt]);
#pragma unroll
        for (int i = 0; i < 8; ++i)
#pragma unroll
          for (int qt = 0; qt < 3; ++qt) oacc[i][qt] = mfma(vf[i], pf[qt], j == 0 ? f32x4{0.f, 0.f, 0.f, 0.f} : oacc[i][qt]);
        SCHED();
        if (j == 0 && w != 3) K_ISSUE(3, 4);
        if (j == 1) V_ISSUE(3, 2);
        if (j == 2) WO_ISSUE2(1, 0);
      }
    }
    if (g == 0) {
#pragma unroll
      for (int qt = 0; qt < 3; ++qt) *(LDS_AS f32x2*)(sm + L_ST2 + ((w * 3 + qt) * 16 + c) * 8) = f32x2{mref[qt], lacc[qt][0]};
    }
    BAR(); STAMP(11);
  }
  int tz = threadIdx.x;
  asm volatile("" : "+v"(tz));
  const int l = tz & 63, c = l & 15, g = l >> 4, qq = c >> 2, pp = l & 3;
  {
    float inv[3], sc[3];
    bool differ = false;
#pragma unroll
    for (int qt = 0; qt < 3; ++qt) {
      f32x2 ml[4];
#pragma unroll
      for (int k = 0; k < 4; ++k) ml[k] = *(LDS_AS f32x2*)(sm + L_ST2 + ((h * 12 + 3 * k + qt) * 16 + c) * 8);
      const float m = fmaxf(fmaxf(ml[0][0], ml[1][0]), fmaxf(ml[2][0], ml[3][0]));
      float lt = 0.f;
#pragma unroll
      for (int k = 0; k < 4; ++k) lt += ml[k][1] * __builtin_amdgcn_exp2f(ml[k][0] - m);
      inv[qt] = __builtin_amdgcn_rcpf(lt);
      sc[qt] = __builtin_amdgcn_exp2f(mref[qt] - m);
      differ = differ || (mref[qt] != m);
    }
    if (__builtin_expect(__any(differ), 0)) {
#pragma unroll
      for (int qt = 0; qt < 3; ++qt)
#pragma unroll
        for (int i = 0; i < 8; ++i) oacc[i][qt] *= sc[qt];
    }
#pragma unroll
    for (int r = 1; r < 4; ++r) {
      const int wd = h * 4 + ((kq + r) & 3);
#pragma unroll
      for (int qt = 0; qt < 3; ++qt) *(LDS_AS bf16x8*)(sm + xoff(wd, r - 1, qt) + l * 16) = cvt8(oacc[2 * r][qt], oacc[2 * r + 1][qt]);
    }
    bf16x8 sel0, sel1;
#pragma unroll
    for (int e = 0; e < 4; ++e) {
      const __bf16 v = (4 * g + e == c) ? (__bf16)1.0f : (__bf16)0.0f;
      sel0[e] = v; sel0[4 + e] = (__bf16)0.0f; sel1[e] = (__bf16)0.0f; sel1[4 + e] = v;
    }
    BAR(); STAMP(17);
#pragma unroll
    for (int qt = 0; qt < 3; ++qt) {
#pragma unroll
      for (int r = 0; r < 3; ++r) {
        bf16x8 t = lds_rd128(sm + xoff(w, r, qt) + l * 16);
        oacc[0][qt] = mfma(sel0, t, oacc[0][qt]);
        oacc[1][qt] = mfma(sel1, t, oacc[1][qt]);
      }
    }
#pragma unroll
    for (int qt = 0; qt < 3; ++qt) *(LDS_AS bf16x8*)(sm + L_O2 + (w * 3 + qt) * 1024 + l * 16) = cvt8(oacc[0][qt] * inv[qt], oacc[1][qt] * inv[qt]);
  }
  waitv<0>();
  BAR(); STAMP(18);
  const unsigned ov0 = (unsigned)((l >> 3) * 1024 + (((l & 7) ^ (g << 1)) << 4));
  WO_ISSUE(2, 1); WO_ISSUE(3, 4);

  f32x4 cacc[4][3];
  {
    bf16x8 of[8][3];
#pragma unroll
    for (int u = 0; u < 8; ++u)
#pragma unroll
      for (int qt = 0; qt < 3; ++qt) of[u][qt] = lds_rd128(sm + L_O2 + (((u + b) & 7) * 3 + qt) * 1024 + l * 16);
    const int fC = 2 * (g & 1) + (qq >> 1);
    ldsp wra = sm + w * 4096 + (4 * g + qq) * 128 + 8 * pp;
    bf16x8 wf[2][4];
#define CSLOT(u) (((u) & 3) == 0 ? 3 : (((u) & 3) == 1 ? 0 : (((u) & 3) == 2 ? 1 : 4)))
#define C_READ(u)                                                                                                   \
  do {                                                                                                              \
    _Pragma("unroll") for (int t_ = 0; t_ < 4; ++t_) {                                                              \
      ldsp p_ = wra + CSLOT(u) * L_SLOT + ((t_ ^ fC) << 5);                                                         \
      wf[(u) & 1][t_] = lds_tr2(p_, p_ + 2048);                                                                     \
    }                                                                                                               \
  } while (0)
    C_READ(0);
#pragma unroll
    for (int u = 0; u < 8; ++u) {
      if (u >= 1 && u + 3 < 8) WO_ISSUE(u + 3, CSLOT(u + 3));
      if (u + 1 < 8) {
        const int last = (u + 3 < 8) ? (u + 3) : 7;
        const int rem = last - (u + 1);
        if (rem == 2) waitv<8>(); else if (rem == 1) waitv<4>(); else waitv<0>();
        C_READ(u + 1);
      }
      SCHED();
#pragma unroll
      for (int t = 0; t < 4; ++t)
#pragma unroll
        for (int qt = 0; qt < 3; ++qt) cacc[t][qt] = mfma(wf[u & 1][t], of[u][qt], u == 0 ? f32x4{0.f, 0.f, 0.f, 0.f} : cacc[t][qt]);
      SCHED();
    }
  }
  STAMP(19);
#define ST_AUX 0
  {
    auto rs = __builtin_amdgcn_make_buffer_rsrc((void*)(part + ((size_t)(b * 16 + kvh * 2 + hp) * 8 + w) * 6 * 64 * 8), 0, 6 * 1024, 0x00020000);
#pragma unroll
    for (int a = 0; a < 2; ++a)
#pragma unroll
      for (int qt = 0; qt < 3; ++qt)
        __builtin_amdgcn_raw_buffer_store_b128(__builtin_bit_cast(u32x4, cvt8(cacc[2 * a][qt], cacc[2 * a + 1][qt])), rs, l * 16 + (a * 3 + qt) * 1024, 0, ST_AUX);
  }
}

__global__ __launch_bounds__(192) void gqa_reduce(const __bf16* __restrict__ part, __bf16* __restrict__ out) {
  const int idx = blockIdx.x * 192 + threadIdx.x;
  const int b = idx / 3072, rem = idx - b * 3072;
  const bf16x8* p = (const bf16x8*)part + (size_t)b * 16 * 3072 + rem;
  bf16x8 v[16];
#pragma unroll
  for (int s = 0; s < 16; ++s) v[s] = p[(size_t)s * 3072];
  f32x4 a0 = {0.f, 0.f, 0.f, 0.f}, a1 = {0.f, 0.f, 0.f, 0.f};
#pragma unroll
  for (int s = 0; s < 16; ++s)
#pragma unroll
    for (int e = 0; e < 4; ++e) { a0[e] += (float)v[s][e]; a1[e] += (float)v[s][4 + e]; }
  const int lane = rem & 63, tq = rem >> 6, qt = tq % 3, wa = tq / 3;
  const int q = 16 * qt + (lane & 15), n = 32 * wa + 4 * (lane >> 4);
  __bf16* o = out + ((size_t)b * SQ + q) * DM + n;
  *(bf16x4*)o = cvt4(a0);
  *(bf16x4*)(o + 16) = cvt4(a1);
}

extern "C" void kernel_launch(void* const* d_in, const int* in_sizes, int n_in, void* d_out, int out_size, void* d_ws, size_t ws_size,
                              hipStream_t stream) {
  const __bf16* x = (const __bf16*)d_in[0];
  const __bf16* kc = (const __bf16*)d_in[1];
  const __bf16* vc = (const __bf16*)d_in[2];
  const __bf16* wq = (const __bf16*)d_in[3];
  const __bf16* wo = (const __bf16*)d_in[4];
  __bf16* part = (__bf16*)d_ws;
  (void)hipFuncSetAttribute((const void*)gqa_fused, hipFuncAttributeMaxDynamicSharedMemorySize, LDS_BYTES);
  hipLaunchKernelGGL(gqa_fused, dim3(256), dim3(512), LDS_BYTES, stream, x, kc, vc, wq, wo, part);
  hipLaunchKernelGGL(gqa_reduce, dim3(256), dim3(192), 0, stream, (const __bf16*)part, (__bf16*)d_out);
}
```

```cpp
#include <hip/hip_runtime.h>
#include <hip/hip_bf16.h>
#include <stdint.h>

typedef __bf16 bf16x8 __attribute__((ext_vector_type(8)));
typedef __bf16 bf16x4 __attribute__((ext_vector_type(4)));
typedef float f32x4 __attribute__((ext_vector_type(4)));
typedef float f32x2 __attribute__((ext_vector_type(2)));
typedef int i32x4 __attribute__((ext_vector_type(4)));
typedef unsigned u32x4 __attribute__((ext_vector_type(4)));
#define LDS_AS __attribute__((address_space(3)))
typedef LDS_AS char* ldsp;

constexpr int B_ = 16, SQ = 48, DM = 512, HQ = 32, HKV = 8, SK = 512, DH = 128;
constexpr float SCL2E = 0.08838834764831845f * 1.4426950408889634f;

constexpr int L_SLOT = 32768;
constexpr int L_RING = 32768;
constexpr int L_X = 114688;
constexpr int L_Q = 131072;
constexpr int L_ST1 = 155648;
constexpr int L_ST2 = 32768 + 24576;
constexpr int L_O2 = 2 * 32768;
constexpr int LDS_BYTES = 163840;
constexpr int NRING = 5;
#define RESC_T 16.0f

#define PRETOUCH 1
#define BAR() asm volatile("s_waitcnt lgkmcnt(0)\n\ts_barrier" ::: "memory")
#define SCHED() __builtin_amdgcn_sched_barrier(0)
#define STAMP(i)
template <int N> __device__ __forceinline__ void waitv() { asm volatile("s_waitcnt vmcnt(%0)" ::"i"(N) : "memory"); }

__device__ __forceinline__ void dma4(unsigned lds, unsigned v0, unsigned v1, unsigned v2, unsigned v3, i32x4 srd, unsigned soff) {
  asm volatile("s_mov_b32 m0, %0\n\ts_nop 0\n\t"
               "buffer_load_dwordx4 %1, %5, %6 offen lds\n\t"
               "buffer_load_dwordx4 %2, %5, %6 offen offset:1024 lds\n\t"
               "buffer_load_dwordx4 %3, %5, %6 offen offset:2048 lds\n\t"
               "buffer_load_dwordx4 %4, %5, %6 offen offset:3072 lds"
               ::"s"(lds), "v"(v0), "v"(v1), "v"(v2), "v"(v3), "s"(srd), "s"(soff) : "memory");
}
__device__ __forceinline__ void dma4nt(unsigned lds, unsigned v0, unsigned v1, unsigned v2, unsigned v3, i32x4 srd, unsigned soff) {
  asm volatile("s_mov_b32 m0, %0\n\ts_nop 0\n\t"
               "buffer_load_dwordx4 %1, %5, %6 offen nt lds\n\t"
               "buffer_load_dwordx4 %2, %5, %6 offen offset:1024 nt lds\n\t"
               "buffer_load_dwordx4 %3, %5, %6 offen offset:2048 nt lds\n\t"
               "buffer_load_dwordx4 %4, %5, %6 offen offset:3072 nt lds"
               ::"s"(lds), "v"(v0), "v"(v1), "v"(v2), "v"(v3), "s"(srd), "s"(soff) : "memory");
}
__device__ __forceinline__ i32x4 make_srd(const void* p, unsigned bytes) {
  unsigned long long a = (unsigned long long)p;
  i32x4 s;
  s[0] = (int)(unsigned)a; s[1] = (int)((unsigned)(a >> 32) & 0xffffu); s[2] = (int)bytes; s[3] = 0x00020000;
  return s;
}
__device__ __forceinline__ bf16x8 lds_rd128(ldsp p) { return *(LDS_AS bf16x8*)p; }
__device__ __forceinline__ bf16x8 lds_tr2(ldsp p0, ldsp p1) {
  bf16x4 a = __builtin_amdgcn_ds_read_tr16_b64_v4bf16((LDS_AS bf16x4*)p0);
  bf16x4 b = __builtin_amdgcn_ds_read_tr16_b64_v4bf16((LDS_AS bf16x4*)p1);
  return __builtin_shufflevector(a, b, 0, 1, 2, 3, 4, 5, 6, 7);
}
__device__ __forceinline__ f32x4 mfma(bf16x8 a, bf16x8 b, f32x4 c) { return __builtin_amdgcn_mfma_f32_16x16x32_bf16(a, b, c, 0, 0, 0); }
__device__ __forceinline__ bf16x4 cvt4(f32x4 v) {
  bf16x4 r; r[0] = (__bf16)v[0]; r[1] = (__bf16)v[1]; r[2] = (__bf16)v[2]; r[3] = (__bf16)v[3]; return r;
}
__device__ __forceinline__ bf16x8 cvt8(f32x4 a, f32x4 b) { return __builtin_shufflevector(cvt4(a), cvt4(b), 0, 1, 2, 3, 4, 5, 6, 7); }
__device__ __forceinline__ float max3(float a, float b, float c) { return __builtin_elementwise_maximum(__builtin_elementwise_maximum(a, b), c); }
__device__ __forceinline__ float max8(f32x4 a, f32x4 b) {
  return max3(max3(max3(a[0], a[1], a[2]), a[3], b[0]), max3(b[1], b[2], b[3]), b[3]);
}
__device__ __forceinline__ float xmax_g(float v) {
  auto r = __builtin_amdgcn_permlane16_swap(__float_as_uint(v), __float_as_uint(v), false, false);
  v = fmaxf(__uint_as_float(r[0]), __uint_as_float(r[1]));
  auto r2 = __builtin_amdgcn_permlane32_swap(__float_as_uint(v), __float_as_uint(v), false, false);
  return fmaxf(__uint_as_float(r2[0]), __uint_as_float(r2[1]));
}
__device__ __forceinline__ int xoff(int wd, int r, int qt) { return (r == 0 ? L_O2 : (r == 1 ? L_SLOT : 4 * L_SLOT)) + (wd * 3 + qt) * 1024; }

__global__ __launch_bounds__(512, 2) void gqa_fused(const __bf16* __restrict__ x, const __bf16* __restrict__ kc, const __bf16* __restrict__ vc,
                                                    const __bf16* __restrict__ wq, const __bf16* __restrict__ wo, __bf16* __restrict__ part) {
  extern __shared__ __attribute__((aligned(16))) char smem_raw[];
  ldsp sm = (ldsp)smem_raw;
  const unsigned smbase = (unsigned)(uintptr_t)sm;
  const int w = __builtin_amdgcn_readfirstlane(threadIdx.x >> 6);
  const int bid = blockIdx.x, kvh = bid & 7, jj = bid >> 3, b = jj >> 1, hp = jj & 1;
  STAMP(0);
  const int n0 = kvh * 512 + hp * 256;
  const i32x4 srd_k = make_srd(kc + (size_t)(b * HKV + kvh) * SK * DH, SK * DH * 2);
  const i32x4 srd_v = make_srd(vc + (size_t)(b * HKV + kvh) * SK * DH, SK * DH * 2);
  const i32x4 srd_o = make_srd(wo + (size_t)n0 * DM, 256 * DM * 2);
  const int h = w >> 2, kq = w & 3;
  f32x4 oacc[8][3];
  f32x4 lacc[3];
  float mref[3];
  {
    const int l = threadIdx.x & 63, c = l & 15, g = l >> 4, qq = c >> 2, pp = l & 3;
    const unsigned kv0 = (unsigned)(g * 256 + ((c ^ g) << 4));
    const unsigned vv0 = (unsigned)(g * 256 + ((c ^ (2 * g)) << 4));
    if (w < 4) {
      f32x4 qacc[4][3];
      const i32x4 srd_x = make_srd(x + (size_t)b * SQ * DM, SQ * DM * 2);
      const i32x4 srd_w = make_srd(wq, DM * HQ * DH * 2);
#pragma unroll
      for (int i = 0; i < 3; ++i) {
        const int r = w * 12 + 4 * i;
        dma4(smbase + L_X + r * 1024, (unsigned)((l ^ (r & 15)) * 16), (unsigned)((l ^ ((r + 1) & 15)) * 16), (unsigned)((l ^ ((r + 2) & 15)) * 16),
             (unsigned)((l ^ ((r + 3) & 15)) * 16), srd_x, (unsigned)(r * 1024));
      }
      const unsigned ring = smbase + L_RING + w * (NRING * 4096);
      const unsigned wv0 = (unsigned)((l >> 3) * 8192 + (((l & 7) ^ (((l >> 4) & 1) << 1)) * 16));
      const unsigned wv1 = (wv0 ^ 64u) + (65536u - 1024u), wv2 = wv0 + 2u * (65536u - 1024u), wv3 = (wv0 ^ 64u) + 3u * (65536u - 1024u);
      const unsigned scol = (unsigned)((n0 + 64 * w) * 2);
#define WQ_ISSUE(s) dma4(ring + ((s) % NRING) * 4096, wv0, wv1, wv2, wv3, srd_w, scol + (unsigned)((((s) + b) & 15) * (32 * 8192)))
      WQ_ISSUE(0); WQ_ISSUE(1); WQ_ISSUE(2); WQ_ISSUE(3);
      waitv<16>();
      STAMP(1); BAR(); STAMP(2);
      const int fA = ((qq >> 1) & 1) | ((g & 1) << 1);
      ldsp wqa = sm + L_RING + w * (NRING * 4096) + (8 * g + qq) * 128 + 8 * pp;
      ldsp xa = sm + L_X + c * 1024;
      bf16x8 fw[2][4], fx[2][3];
#define A_READ(s, buf)                                                                                          \
  do {                                                                                                          \
    _Pragma("unroll") for (int t_ = 0; t_ < 4; ++t_) {                                                          \
      ldsp p_ = wqa + ((s) % NRING) * 4096 + ((t_ ^ fA) << 5);                                                  \
      fw[buf][t_] = lds_tr2(p_, p_ + 512);                                                                      \
    }                                                                                                           \
    _Pragma("unroll") for (int q_ = 0; q_ < 3; ++q_)                                                            \
        fx[buf][q_] = lds_rd128(xa + q_ * 16384 + ((((4 * (((s) + b) & 15)) | g) ^ c) << 4));                   \
  } while (0)
      waitv<12>();
      A_READ(0, 0);
#pragma unroll
      for (int s = 0; s < 16; ++s) {
        if (s + 4 < 16) WQ_ISSUE(s + 4);
        if (s == 12) {
          dma4nt(smbase + (32 * w) * 256, kv0, kv0 ^ 64u, kv0 ^ 128u, kv0 ^ 192u, srd_k, (unsigned)(128 * w * 256));
          dma4nt(smbase + (32 * w + 16) * 256, kv0, kv0 ^ 64u, kv0 ^ 128u, kv0 ^ 192u, srd_k, (unsigned)((128 * w + 16) * 256));
        }
        if (s + 1 < 16) {
          const int rem = ((s + 4 < 16) ? (s + 4) : 15) - (s + 1);
          if (s < 12) waitv<12>(); else if (rem == 2) waitv<16>(); else if (rem == 1) waitv<12>(); else waitv<8>();
          A_READ(s + 1, (s + 1) & 1);
        }
        SCHED();
#pragma unroll
        for (int t = 0; t < 4; ++t)
#pragma unroll
          for (int qt = 0; qt < 3; ++qt) qacc[t][qt] = mfma(fw[s & 1][t], fx[s & 1][qt], s == 0 ? f32x4{0.f, 0.f, 0.f, 0.f} : qacc[t][qt]);
        SCHED();
      }
      STAMP(3); BAR(); STAMP(4);
#pragma unroll
      for (int t = 0; t < 4; ++t)
#pragma unroll
        for (int qt = 0; qt < 3; ++qt) {
          const int c16 = 8 * (w & 1) + 2 * t + (g >> 1);
          *(LDS_AS bf16x4*)(sm + L_Q + (48 * (w >> 1) + 16 * qt + c) * 256 + ((c16 ^ c) << 4) + 8 * (g & 1)) = cvt4(qacc[t][qt] * SCL2E);
        }
    } else {
      unsigned t0 = 0;
      if (w == 4) {
        const __bf16* p = (l < 32 ? kc : vc) + (size_t)(b * HKV + kvh) * SK * DH + (l & 31) * 2048;
        t0 = *(const unsigned*)p;
      } else if (w == 5) {
        t0 = *(const unsigned*)(wo + (size_t)n0 * DM + l * 2048);
      }
      STAMP(1); BAR(); STAMP(2);
      STAMP(3); BAR(); STAMP(4);
      asm volatile("" ::"v"(t0));
    }
    const unsigned kvrow = (unsigned)(128 * kq * 256);
#define K_ISSUE(j, slot)                                                                                                                  \
  do { if (h == 0) {                                                                                                                      \
    dma4nt(smbase + (slot) * L_SLOT + kq * 8192, kv0, kv0 ^ 64u, kv0 ^ 128u, kv0 ^ 192u, srd_k, kvrow + (unsigned)((j) * 8192));          \
    dma4nt(smbase + (slot) * L_SLOT + kq * 8192 + 4096, kv0, kv0 ^ 64u, kv0 ^ 128u, kv0 ^ 192u, srd_k, kvrow + (unsigned)((j) * 8192 + 4096)); \
  } } while (0)
#define V_ISSUE(j, slot)                                                                                                                  \
  do { if (h == 0) {                                                                                                                      \
    dma4nt(smbase + (slot) * L_SLOT + kq * 8192, vv0, vv0 ^ 128u, vv0, vv0 ^ 128u, srd_v, kvrow + (unsigned)((j) * 8192));                \
    dma4nt(smbase + (slot) * L_SLOT + kq * 8192 + 4096, vv0, vv0 ^ 128u, vv0, vv0 ^ 128u, srd_v, kvrow + (unsigned)((j) * 8192 + 4096));  \
  } } while (0)
    const unsigned ov0 = (unsigned)((l >> 3) * 1024 + (((l & 7) ^ (g << 1)) << 4));
#define WO_ISSUE_W(u, slot, ww) dma4(smbase + (slot) * L_SLOT + (ww) * 4096, ov0, ov0 + 7168u, ov0 + 14336u, ov0 + 21504u, srd_o, (unsigned)((ww) * 128 + (((u) + b) & 7) * 32768))
#define WO_ISSUE(u, slot) WO_ISSUE_W(u, slot, w)
#define WO_ISSUE2(u, slot) do { if (h == 0) { WO_ISSUE_W(u, slot, w); WO_ISSUE_W(u, slot, w + 4); } } while (0)
    K_ISSUE(1, 1);
    waitv<8>();
    STAMP(5); BAR(); STAMP(6);

    bf16x8 qf[4][3];
#pragma unroll
    for (int s = 0; s < 4; ++s)
#pragma unroll
      for (int qt = 0; qt < 3; ++qt) qf[s][qt] = lds_rd128(sm + L_Q + (48 * h + 16 * qt + c) * 256 + ((((4 * s) | g) ^ c) << 4));
    V_ISSUE(0, 2);
    ldsp ka = sm + (32 * kq + c) * 256;
    const int fV = 4 * (g & 1) + qq;
    unsigned va[4];
#pragma unroll
    for (int r = 0; r < 4; ++r) va[r] = smbase + (32 * kq + 4 * g + qq) * 256 + 8 * pp + (((2 * ((r + kq) & 3)) ^ fV) << 5);
    bf16x8 ones;
#pragma unroll
    for (int e = 0; e < 8; ++e) ones[e] = (__bf16)1.0f;
    f32x4 negm[3];
    constexpr int kslot[4] = {0, 1, 3, 4}, vslot[4] = {2, 0, 1, 2};
#define SYNC(j)                                                               \
  do {                                                                        \
    if ((j) < 3) waitv<8>(); else waitv<16>();                                \
    if ((j) == 1 && w == 3) waitv<0>();                                       \
    BAR(); STAMP(7 + (j));                                                    \
    if ((j) == 0) { V_ISSUE(1, 0); }                                          \
    if ((j) == 1) { if (w == 3) K_ISSUE(3, 4); V_ISSUE(2, 1); }               \
    if ((j) == 2) { WO_ISSUE2(0, 3); }                                        \
  } while (0)
#pragma unroll
    for (int j = 0; j < 4; ++j) {
      f32x4 sacc[2][3];
      {
        unsigned kso = (unsigned)(kslot[j] * L_SLOT);
        asm volatile("" : "+s"(kso));
        bf16x8 kf[2][4];
#pragma unroll
        for (int s = 0; s < 4; ++s)
#pragma unroll
          for (int kt = 0; kt < 2; ++kt) kf[kt][s] = lds_rd128(ka + kso + kt * 4096 + ((((4 * s) | g) ^ c) << 4));
        SCHED();
#pragma unroll
        for (int s = 0; s < 4; ++s)
#pragma unroll
          for (int kt = 0; kt < 2; ++kt)
#pragma unroll
            for (int qt = 0; qt < 3; ++qt)
              sacc[kt][qt] = mfma(kf[kt][s], qf[s][qt], s == 0 ? (j == 0 ? f32x4{0.f, 0.f, 0.f, 0.f} : negm[qt]) : sacc[kt][qt]);
        SCHED();
        if (j == 0) K_ISSUE(2, 3);
      }
      bool resc = false;
      float al[3];
      if (j == 0) {
#pragma unroll
        for (int qt = 0; qt < 3; ++qt) {
          const float m = xmax_g(max8(sacc[0][qt], sacc[1][qt]));
          if (g == 0) *(LDS_AS float*)(sm + L_ST1 + ((w * 3 + qt) * 16 + c) * 4) = m;
        }
        SYNC(0);
#pragma unroll
        for (int qt = 0; qt < 3; ++qt) {
          float m = *(LDS_AS float*)(sm + L_ST1 + ((h * 12 + qt) * 16 + c) * 4);
#pragma unroll
          for (int k = 1; k < 4; ++k) m = fmaxf(m, *(LDS_AS float*)(sm + L_ST1 + ((h * 12 + 3 * k + qt) * 16 + c) * 4));
          mref[qt] = m;
          negm[qt] = f32x4{-m, -m, -m, -m};
          sacc[0][qt] += negm[qt];
          sacc[1][qt] += negm[qt];
        }
      } else {
        if (h == 0) SYNC(j);
        float t = max3(sacc[0][0][0], sacc[0][0][1], sacc[0][0][2]);
        t = max3(t, sacc[0][0][3], sacc[1][0][0]); t = max3(t, sacc[1][0][1], sacc[1][0][2]);
        t = max3(t, sacc[1][0][3], sacc[0][1][0]); t = max3(t, sacc[0][1][1], sacc[0][1][2]);
        t = max3(t, sacc[0][1][3], sacc[1][1][0]); t = max3(t, sacc[1][1][1], sacc[1][1][2]);
        t = max3(t, sacc[1][1][3], sacc[0][2][0]); t = max3(t, sacc[0][2][1], sacc[0][2][2]);
        t = max3(t, sacc[0][2][3], sacc[1][2][0]); t = max3(t, sacc[1][2][1], sacc[1][2][2]);
        t = max3(t, sacc[1][2][3], t);
        resc = __any(!(t <= RESC_T));
        if (__builtin_expect(resc, 0)) {
#pragma unroll
          for (int qt = 0; qt < 3; ++qt) {
            const float d = fmaxf(xmax_g(max8(sacc[0][qt], sacc[1][qt])), 0.f);
            al[qt] = __builtin_amdgcn_exp2f(-d);
            mref[qt] += d;
            negm[qt] -= d;
            sacc[0][qt] -= d;
            sacc[1][qt] -= d;
          }
        }
      }
      bf16x8 pf[3];
#pragma unroll
      for (int qt = 0; qt < 3; ++qt) {
#pragma unroll
        for (int kt = 0; kt < 2; ++kt)
#pragma unroll
          for (int r = 0; r < 4; ++r) sacc[kt][qt][r] = __builtin_amdgcn_exp2f(sacc[kt][qt][r]);
        pf[qt] = cvt8(sacc[0][qt], sacc[1][qt]);
        asm volatile("" : "+v"(pf[qt]));
      }
      SCHED();
      if (j > 0) {
        if (h == 1) SYNC(j);
        if (__builtin_expect(resc, 0)) {
#pragma unroll
          for (int qt = 0; qt < 3; ++qt) {
            lacc[qt] *= al[qt];
#pragma unroll
            for (int i = 0; i < 8; ++i) oacc[i][qt] *= al[qt];
          }
        }
      }
      {
        unsigned vso = (unsigned)(vslot[j] * L_SLOT);
        asm volatile("" : "+s"(vso));
        bf16x8 vf[8];
#pragma unroll
        for (int i = 0; i < 8; ++i) {
          ldsp p = (ldsp)((va[i >> 1] ^ (unsigned)((i & 1) << 5)) + vso);
          vf[i] = lds_tr2(p, p + 4096);
        }
        SCHED();
#pragma unroll
        for (int qt = 0; qt < 3; ++qt) lacc[qt] = mfma(ones, pf[qt], j == 0 ? f32x4{0.f, 0.f, 0.f, 0.f} : lacc[qt]);
#pragma unroll
        for (int i = 0; i < 8; ++i)
#pragma unroll
          for (int qt = 0; qt < 3; ++qt) oacc[i][qt] = mfma(vf[i], pf[qt], j == 0 ? f32x4{0.f, 0.f, 0.f, 0.f} : oacc[i][qt]);
        SCHED();
        if (j == 0 && w != 3) K_ISSUE(3, 4);
        if (j == 1) V_ISSUE(3, 2);
        if (j == 2) WO_ISSUE2(1, 0);
      }
    }
    if (g == 0) {
#pragma unroll
      for (int qt = 0; qt < 3; ++qt) *(LDS_AS f32x2*)(sm + L_ST2 + ((w * 3 + qt) * 16 + c) * 8) = f32x2{mref[qt], lacc[qt][0]};
    }
    BAR(); STAMP(11);
  }
  int tz = threadIdx.x;
  asm volatile("" : "+v"(tz));
  const int l = tz & 63, c = l & 15, g = l >> 4, qq = c >> 2, pp = l & 3;
  {
    float inv[3], sc[3];
    bool differ = false;
#pragma unroll
    for (int qt = 0; qt < 3; ++qt) {
      f32x2 ml[4];
#pragma unroll
      for (int k = 0; k < 4; ++k) ml[k] = *(LDS_AS f32x2*)(sm + L_ST2 + ((h * 12 + 3 * k + qt) * 16 + c) * 8);
      const float m = fmaxf(fmaxf(ml[0][0], ml[1][0]), fmaxf(ml[2][0], ml[3][0]));
      float lt = 0.f;
#pragma unroll
      for (int k = 0; k < 4; ++k) lt += ml[k][1] * __builtin_amdgcn_exp2f(ml[k][0] - m);
      inv[qt] = __builtin_amdgcn_rcpf(lt);
      sc[qt] = __builtin_amdgcn_exp2f(mref[qt] - m);
      differ = differ || (mref[qt] != m);
    }
    if (__builtin_expect(__any(differ), 0)) {
#pragma unroll
      for (int qt = 0; qt < 3; ++qt)
#pragma unroll
        for (int i = 0; i < 8; ++i) oacc[i][qt] *= sc[qt];
    }
#pragma unroll
    for (int r = 1; r < 4; ++r) {
      const int wd = h * 4 + ((kq + r) & 3);
#pragma unroll
      for (int qt = 0; qt < 3; ++qt) *(LDS_AS bf16x8*)(sm + xoff(wd, r - 1, qt) + l * 16) = cvt8(oacc[2 * r][qt], oacc[2 * r + 1][qt]);
    }
    bf16x8 sel0, sel1;
#pragma unroll
    for (int e = 0; e < 4; ++e) {
      const __bf16 v = (4 * g + e == c) ? (__bf16)1.0f : (__bf16)0.0f;
      sel0[e] = v; sel0[4 + e] = (__bf16)0.0f; sel1[e] = (__bf16)0.0f; sel1[4 + e] = v;
    }
    BAR(); STAMP(17);
#pragma unroll
    for (int qt = 0; qt < 3; ++qt) {
#pragma unroll
      for (int r = 0; r < 3; ++r) {
        bf16x8 t = lds_rd128(sm + xoff(w, r, qt) + l * 16);
        oacc[0][qt] = mfma(sel0, t, oacc[0][qt]);
        oacc[1][qt] = mfma(sel1, t, oacc[1][qt]);
      }
    }
#pragma unroll
    for (int qt = 0; qt < 3; ++qt) *(LDS_AS bf16x8*)(sm + L_O2 + (w * 3 + qt) * 1024 + l * 16) = cvt8(oacc[0][qt] * inv[qt], oacc[1][qt] * inv[qt]);
  }
  waitv<0>();
  BAR(); STAMP(18);
  const unsigned ov0 = (unsigned)((l >> 3) * 1024 + (((l & 7) ^ (g << 1)) << 4));
  WO_ISSUE(2, 1); WO_ISSUE(3, 4);

  f32x4 cacc[4][3];
  {
    bf16x8 of[8][3];
#pragma unroll
    for (int u = 0; u < 8; ++u)
#pragma unroll
      for (int qt = 0; qt < 3; ++qt) of[u][qt] = lds_rd128(sm + L_O2 + (((u + b) & 7) * 3 + qt) * 1024 + l * 16);
    const int fC = 2 * (g & 1) + (qq >> 1);
    ldsp wra = sm + w * 4096 + (4 * g + qq) * 128 + 8 * pp;
    bf16x8 wf[2][4];
#define CSLOT(u) (((u) & 3) == 0 ? 3 : (((u) & 3) == 1 ? 0 : (((u) & 3) == 2 ? 1 : 4)))
#define C_READ(u)                                                                                                   \
  do {                                                                                                              \
    _Pragma("unroll") for (int t_ = 0; t_ < 4; ++t_) {                                                              \
      ldsp p_ = wra + CSLOT(u) * L_SLOT + ((t_ ^ fC) << 5);                                                         \
      wf[(u) & 1][t_] = lds_tr2(p_, p_ + 2048);                                                                     \
    }                                                                                                               \
  } while (0)
    C_READ(0);
#pragma unroll
    for (int u = 0; u < 8; ++u) {
      if (u >= 1 && u + 3 < 8) WO_ISSUE(u + 3, CSLOT(u + 3));
      if (u + 1 < 8) {
        const int last = (u + 3 < 8) ? (u + 3) : 7;
        const int rem = last - (u + 1);
        if (rem == 2) waitv<8>(); else if (rem == 1) waitv<4>(); else waitv<0>();
        C_READ(u + 1);
      }
      SCHED();
#pragma unroll
      for (int t = 0; t < 4; ++t)
#pragma unroll
        for (int qt = 0; qt < 3; ++qt) cacc[t][qt] = mfma(wf[u & 1][t], of[u][qt], u == 0 ? f32x4{0.f, 0.f, 0.f, 0.f} : cacc[t][qt]);
      SCHED();
    }
  }
  STAMP(19);
#define ST_AUX 16
  {
    auto rs = __builtin_amdgcn_make_buffer_rsrc((void*)(part + ((size_t)(b * 16 + kvh * 2 + hp) * 8 + w) * 6 * 64 * 8), 0, 6 * 1024, 0x00020000);
#pragma unroll
    for (int a = 0; a < 2; ++a)
#pragma unroll
      for (int qt = 0; qt < 3; ++qt)
        __builtin_amdgcn_raw_buffer_store_b128(__builtin_bit_cast(u32x4, cvt8(cacc[2 * a][qt], cacc[2 * a + 1][qt])), rs, l * 16 + (a * 3 + qt) * 1024, 0, ST_AUX);
  }
}

__global__ __launch_bounds__(192) void gqa_reduce(const __bf16* __restrict__ part, __bf16* __restrict__ out) {
  const int idx = blockIdx.x * 192 + threadIdx.x;
  const int b = idx / 3072, rem = idx - b * 3072;
  const bf16x8* p = (const bf16x8*)part + (size_t)b * 16 * 3072 + rem;
  bf16x8 v[16];
#pragma unroll
  for (int s = 0; s < 16; ++s) v[s] = p[(size_t)s * 3072];
  f32x4 a0 = {0.f, 0.f, 0.f, 0.f}, a1 = {0.f, 0.f, 0.f, 0.f};
#pragma unroll
  for (int s = 0; s < 16; ++s)
#pragma unroll
    for (int e = 0; e < 4; ++e) { a0[e] += (float)v[s][e]; a1[e] += (float)v[s][4 + e]; }
  const int lane = rem & 63, tq = rem >> 6, qt = tq % 3, wa = tq / 3;
  const int q = 16 * qt + (lane & 15), n = 32 * wa + 4 * (lane >> 4);
  __bf16* o = out + ((size_t)b * SQ + q) * DM + n;
  *(bf16x4*)o = cvt4(a0);
  *(bf16x4*)(o + 16) = cvt4(a1);
}

extern "C" void kernel_launch(void* const* d_in, const int* in_sizes, int n_in, void* d_out, int out_size, void* d_ws, size_t ws_size,
                              hipStream_t stream) {
  const __bf16* x = (const __bf16*)d_in[0];
  const __bf16* kc = (const __bf16*)d_in[1];
  const __bf16* vc = (const __bf16*)d_in[2];
  const __bf16* wq = (const __bf16*)d_in[3];
  const __bf16* wo = (const __bf16*)d_in[4];
  __bf16* part = (__bf16*)d_ws;
  (void)hipFuncSetAttribute((const void*)gqa_fused, hipFuncAttributeMaxDynamicSharedMemorySize, LDS_BYTES);
  hipLaunchKernelGGL(gqa_fused, dim3(256), dim3(512), LDS_BYTES, stream, x, kc, vc, wq, wo, part);
  hipLaunchKernelGGL(gqa_reduce, dim3(256), dim3(192), 0, stream, (const __bf16*)part, (__bf16*)d_out);
}
```

```cpp
#include <hip/hip_runtime.h>
#include <hip/hip_bf16.h>
#include <stdint.h>

typedef __bf16 bf16x8 __attribute__((ext_vector_type(8)));
typedef __bf16 bf16x4 __attribute__((ext_vector_type(4)));
typedef float f32x4 __attribute__((ext_vector_type(4)));
typedef float f32x2 __attribute__((ext_vector_type(2)));
typedef int i32x4 __attribute__((ext_vector_type(4)));
typedef unsigned u32x4 __attribute__((ext_vector_type(4)));
#define LDS_AS __attribute__((address_space(3)))
typedef LDS_AS char* ldsp;

constexpr int B_ = 16, SQ = 48, DM = 512, HQ = 32, HKV = 8, SK = 512, DH = 128;
constexpr float SCL2E = 0.08838834764831845f * 1.4426950408889634f;

constexpr int L_SLOT = 32768;
constexpr int L_RING = 32768;
constexpr int L_X = 114688;
constexpr int L_Q = 131072;
constexpr int L_ST1 = 155648;
constexpr int L_ST2 = 32768 + 24576;
constexpr int L_O2 = 2 * 32768;
constexpr int LDS_BYTES = 163840;
constexpr int WS_EPOCH = 0, WS_FLAGS = 4096, WS_PART = 65536;
constexpr int NRING = 5;
#define RESC_T 16.0f

#define BAR() asm volatile("s_waitcnt lgkmcnt(0)\n\ts_barrier" ::: "memory")
#define SCHED() __builtin_amdgcn_sched_barrier(0)
#define STAMP(i)
template <int N> __device__ __forceinline__ void waitv() { asm volatile("s_waitcnt vmcnt(%0)" ::"i"(N) : "memory"); }

__device__ __forceinline__ void dma4(unsigned lds, unsigned v0, unsigned v1, unsigned v2, unsigned v3, i32x4 srd, unsigned soff) {
  asm volatile("s_mov_b32 m0, %0\n\ts_nop 0\n\t"
               "buffer_load_dwordx4 %1, %5, %6 offen lds\n\t"
               "buffer_load_dwordx4 %2, %5, %6 offen offset:1024 lds\n\t"
               "buffer_load_dwordx4 %3, %5, %6 offen offset:2048 lds\n\t"
               "buffer_load_dwordx4 %4, %5, %6 offen offset:3072 lds"
               ::"s"(lds), "v"(v0), "v"(v1), "v"(v2), "v"(v3), "s"(srd), "s"(soff) : "memory");
}
__device__ __forceinline__ void dma4nt(unsigned lds, unsigned v0, unsigned v1, unsigned v2, unsigned v3, i32x4 srd, unsigned soff) {
  asm volatile("s_mov_b32 m0, %0\n\ts_nop 0\n\t"
               "buffer_load_dwordx4 %1, %5, %6 offen nt lds\n\t"
               "buffer_load_dwordx4 %2, %5, %6 offen offset:1024 nt lds\n\t"
               "buffer_load_dwordx4 %3, %5, %6 offen offset:2048 nt lds\n\t"
               "buffer_load_dwordx4 %4, %5, %6 offen offset:3072 nt lds"
               ::"s"(lds), "v"(v0), "v"(v1), "v"(v2), "v"(v3), "s"(srd), "s"(soff) : "memory");
}
__device__ __forceinline__ i32x4 make_srd(const void* p, unsigned bytes) {
  unsigned long long a = (unsigned long long)p;
  i32x4 s;
  s[0] = (int)(unsigned)a; s[1] = (int)((unsigned)(a >> 32) & 0xffffu); s[2] = (int)bytes; s[3] = 0x00020000;
  return s;
}
__device__ __forceinline__ bf16x8 lds_rd128(ldsp p) { return *(LDS_AS bf16x8*)p; }
__device__ __forceinline__ bf16x8 lds_tr2(ldsp p0, ldsp p1) {
  bf16x4 a = __builtin_amdgcn_ds_read_tr16_b64_v4bf16((LDS_AS bf16x4*)p0);
  bf16x4 b = __builtin_amdgcn_ds_read_tr16_b64_v4bf16((LDS_AS bf16x4*)p1);
  return __builtin_shufflevector(a, b, 0, 1, 2, 3, 4, 5, 6, 7);
}
__device__ __forceinline__ f32x4 mfma(bf16x8 a, bf16x8 b, f32x4 c) { return __builtin_amdgcn_mfma_f32_16x16x32_bf16(a, b, c, 0, 0, 0); }
__device__ __forceinline__ bf16x4 cvt4(f32x4 v) {
  bf16x4 r; r[0] = (__bf16)v[0]; r[1] = (__bf16)v[1]; r[2] = (__bf16)v[2]; r[3] = (__bf16)v[3]; return r;
}
__device__ __forceinline__ bf16x8 cvt8(f32x4 a, f32x4 b) { return __builtin_shufflevector(cvt4(a), cvt4(b), 0, 1, 2, 3, 4, 5, 6, 7); }
__device__ __forceinline__ float max3(float a, float b, float c) { return __builtin_elementwise_maximum(__builtin_elementwise_maximum(a, b), c); }
__device__ __forceinline__ float max8(f32x4 a, f32x4 b) {
  return max3(max3(max3(a[0], a[1], a[2]), a[3], b[0]), max3(b[1], b[2], b[3]), b[3]);
}
__device__ __forceinline__ float xmax_g(float v) {
  auto r = __builtin_amdgcn_permlane16_swap(__float_as_uint(v), __float_as_uint(v), false, false);
  v = fmaxf(__uint_as_float(r[0]), __uint_as_float(r[1]));
  auto r2 = __builtin_amdgcn_permlane32_swap(__float_as_uint(v), __float_as_uint(v), false, false);
  return fmaxf(__uint_as_float(r2[0]), __uint_as_float(r2[1]));
}
__device__ __forceinline__ int xoff(int wd, int r, int qt) { return (r == 0 ? L_O2 : (r == 1 ? L_SLOT : 4 * L_SLOT)) + (wd * 3 + qt) * 1024; }

__global__ __launch_bounds__(512, 2) void gqa_fused(const __bf16* __restrict__ x, const __bf16* __restrict__ kc, const __bf16* __restrict__ vc,
                                                    const __bf16* __restrict__ wq, const __bf16* __restrict__ wo, char* ws, __bf16* __restrict__ out) {
  extern __shared__ __attribute__((aligned(16))) char smem_raw[];
  ldsp sm = (ldsp)smem_raw;
  const unsigned smbase = (unsigned)(uintptr_t)sm;
  const int w = __builtin_amdgcn_readfirstlane(threadIdx.x >> 6);
  const int bid = blockIdx.x, kvh = bid & 7, jj = bid >> 3, b = jj >> 1, hp = jj & 1;
  STAMP(0);
  const int n0 = kvh * 512 + hp * 256;
  const i32x4 srd_k = make_srd(kc + (size_t)(b * HKV + kvh) * SK * DH, SK * DH * 2);
  const i32x4 srd_v = make_srd(vc + (size_t)(b * HKV + kvh) * SK * DH, SK * DH * 2);
  const i32x4 srd_o = make_srd(wo + (size_t)n0 * DM, 256 * DM * 2);
  const int h = w >> 2, kq = w & 3;
  typedef __attribute__((address_space(1))) unsigned long long gu64;
  gu64* epoch = (gu64*)(ws + WS_EPOCH + b * 256);
  gu64* flags = (gu64*)(ws + WS_FLAGS + b * 16 * 128);
  const unsigned long long tag = __hip_atomic_load(epoch, __ATOMIC_RELAXED, __HIP_MEMORY_SCOPE_AGENT) + 1ull;
  f32x4 oacc[8][3];
  f32x4 lacc[3];
  float mref[3];
  {
    const int l = threadIdx.x & 63, c = l & 15, g = l >> 4, qq = c >> 2, pp = l & 3;
    const unsigned kv0 = (unsigned)(g * 256 + ((c ^ g) << 4));
    const unsigned vv0 = (unsigned)(g * 256 + ((c ^ (2 * g)) << 4));
    if (w < 4) {
      f32x4 qacc[4][3];
      const i32x4 srd_x = make_srd(x + (size_t)b * SQ * DM, SQ * DM * 2);
      const i32x4 srd_w = make_srd(wq, DM * HQ * DH * 2);
#pragma unroll
      for (int i = 0; i < 3; ++i) {
        const int r = w * 12 + 4 * i;
        dma4(smbase + L_X + r * 1024, (unsigned)((l ^ (r & 15)) * 16), (unsigned)((l ^ ((r + 1) & 15)) * 16), (unsigned)((l ^ ((r + 2) & 15)) * 16),
             (unsigned)((l ^ ((r + 3) & 15)) * 16), srd_x, (unsigned)(r * 1024));
      }
      const unsigned ring = smbase + L_RING + w * (NRING * 4096);
      const unsigned wv0 = (unsigned)((l >> 3) * 8192 + (((l & 7) ^ (((l >> 4) & 1) << 1)) * 16));
      const unsigned wv1 = (wv0 ^ 64u) + (65536u - 1024u), wv2 = wv0 + 2u * (65536u - 1024u), wv3 = (wv0 ^ 64u) + 3u * (65536u - 1024u);
      const unsigned scol = (unsigned)((n0 + 64 * w) * 2);
#define WQ_ISSUE(s) dma4(ring + ((s) % NRING) * 4096, wv0, wv1, wv2, wv3, srd_w, scol + (unsigned)((((s) + b) & 15) * (32 * 8192)))
      WQ_ISSUE(0); WQ_ISSUE(1); WQ_ISSUE(2); WQ_ISSUE(3);
      waitv<16>();
      STAMP(1); BAR(); STAMP(2);
      const int fA = ((qq >> 1) & 1) | ((g & 1) << 1);
      ldsp wqa = sm + L_RING + w * (NRING * 4096) + (8 * g + qq) * 128 + 8 * pp;
      ldsp xa = sm + L_X + c * 1024;
      bf16x8 fw[2][4], fx[2][3];
#define A_READ(s, buf)                                                                                          \
  do {                                                                                                          \
    _Pragma("unroll") for (int t_ = 0; t_ < 4; ++t_) {                                                          \
      ldsp p_ = wqa + ((s) % NRING) * 4096 + ((t_ ^ fA) << 5);                                                  \
      fw[buf][t_] = lds_tr2(p_, p_ + 512);                                                                      \
    }                                                                                                           \
    _Pragma("unroll") for (int q_ = 0; q_ < 3; ++q_)                                                            \
        fx[buf][q_] = lds_rd128(xa + q_ * 16384 + ((((4 * (((s) + b) & 15)) | g) ^ c) << 4));                   \
  } while (0)
      waitv<12>();
      A_READ(0, 0);
#pragma unroll
      for (int s = 0; s < 16; ++s) {
        if (s + 4 < 16) WQ_ISSUE(s + 4);
        if (s == 12) {
          dma4nt(smbase + (32 * w) * 256, kv0, kv0 ^ 64u, kv0 ^ 128u, kv0 ^ 192u, srd_k, (unsigned)(128 * w * 256));
          dma4nt(smbase + (32 * w + 16) * 256, kv0, kv0 ^ 64u, kv0 ^ 128u, kv0 ^ 192u, srd_k, (unsigned)((128 * w + 16) * 256));
        }
        if (s + 1 < 16) {
          const int rem = ((s + 4 < 16) ? (s + 4) : 15) - (s + 1);
          if (s < 12) waitv<12>(); else if (rem == 2) waitv<16>(); else if (rem == 1) waitv<12>(); else waitv<8>();
          A_READ(s + 1, (s + 1) & 1);
        }
        SCHED();
#pragma unroll
        for (int t = 0; t < 4; ++t)
#pragma unroll
          for (int qt = 0; qt < 3; ++qt) qacc[t][qt] = mfma(fw[s & 1][t], fx[s & 1][qt], s == 0 ? f32x4{0.f, 0.f, 0.f, 0.f} : qacc[t][qt]);
        SCHED();
      }
      STAMP(3); BAR(); STAMP(4);
#pragma unroll
      for (int t = 0; t < 4; ++t)
#pragma unroll
        for (int qt = 0; qt < 3; ++qt) {
          const int c16 = 8 * (w & 1) + 2 * t + (g >> 1);
          *(LDS_AS bf16x4*)(sm + L_Q + (48 * (w >> 1) + 16 * qt + c) * 256 + ((c16 ^ c) << 4) + 8 * (g & 1)) = cvt4(qacc[t][qt] * SCL2E);
        }
    } else {
      STAMP(1); BAR(); STAMP(2);
      STAMP(3); BAR(); STAMP(4);
    }
    const unsigned kvrow = (unsigned)(128 * kq * 256);
#define K_ISSUE(j, slot)                                                                                                                  \
  do { if (h == 0) {                                                                                                                      \
    dma4nt(smbase + (slot) * L_SLOT + kq * 8192, kv0, kv0 ^ 64u, kv0 ^ 128u, kv0 ^ 192u, srd_k, kvrow + (unsigned)((j) * 8192));          \
    dma4nt(smbase + (slot) * L_SLOT + kq * 8192 + 4096, kv0, kv0 ^ 64u, kv0 ^ 128u, kv0 ^ 192u, srd_k, kvrow + (unsigned)((j) * 8192 + 4096)); \
  } } while (0)
#define V_ISSUE(j, slot)                                                                                                                  \
  do { if (h == 0) {                                                                                                                      \
    dma4nt(smbase + (slot) * L_SLOT + kq * 8192, vv0, vv0 ^ 128u, vv0, vv0 ^ 128u, srd_v, kvrow + (unsigned)((j) * 8192));                \
    dma4nt(smbase + (slot) * L_SLOT + kq * 8192 + 4096, vv0, vv0 ^ 128u, vv0, vv0 ^ 128u, srd_v, kvrow + (unsigned)((j) * 8192 + 4096));  \
  } } while (0)
    const unsigned ov0 = (unsigned)((l >> 3) * 1024 + (((l & 7) ^ (g << 1)) << 4));
#define WO_ISSUE_W(u, slot, ww) dma4(smbase + (slot) * L_SLOT + (ww) * 4096, ov0, ov0 + 7168u, ov0 + 14336u, ov0 + 21504u, srd_o, (unsigned)((ww) * 128 + (((u) + b) & 7) * 32768))
#define WO_ISSUE(u, slot) WO_ISSUE_W(u, slot, w)
#define WO_ISSUE2(u, slot) do { if (h == 0) { WO_ISSUE_W(u, slot, w); WO_ISSUE_W(u, slot, w + 4); } } while (0)
    K_ISSUE(1, 1);
    waitv<8>();
    STAMP(5); BAR(); STAMP(6);

    bf16x8 qf[4][3];
#pragma unroll
    for (int s = 0; s < 4; ++s)
#pragma unroll
      for (int qt = 0; qt < 3; ++qt) qf[s][qt] = lds_rd128(sm + L_Q + (48 * h + 16 * qt + c) * 256 + ((((4 * s) | g) ^ c) << 4));
    V_ISSUE(0, 2);
    ldsp ka = sm + (32 * kq + c) * 256;
    const int fV = 4 * (g & 1) + qq;
    unsigned va[4];
#pragma unroll
    for (int r = 0; r < 4; ++r) va[r] = smbase + (32 * kq + 4 * g + qq) * 256 + 8 * pp + (((2 * ((r + kq) & 3)) ^ fV) << 5);
    bf16x8 ones;
#pragma unroll
    for (int e = 0; e < 8; ++e) ones[e] = (__bf16)1.0f;
    f32x4 negm[3];
    constexpr int kslot[4] = {0, 1, 3, 4}, vslot[4] = {2, 0, 1, 2};
#define SYNC(j)                                                               \
  do {                                                                        \
    if ((j) < 3) waitv<8>(); else waitv<16>();                                \
    if ((j) == 1 && w == 3) waitv<0>();                                       \
    BAR(); STAMP(7 + (j));                                                    \
    if ((j) == 0) { V_ISSUE(1, 0); }                                          \
    if ((j) == 1) { if (w == 3) K_ISSUE(3, 4); V_ISSUE(2, 1); }               \
    if ((j) == 2) { WO_ISSUE2(0, 3); }                                        \
  } while (0)
#pragma unroll
    for (int j = 0; j < 4; ++j) {
      f32x4 sacc[2][3];
      {
        unsigned kso = (unsigned)(kslot[j] * L_SLOT);
        asm volatile("" : "+s"(kso));
        bf16x8 kf[2][4];
#pragma unroll
        for (int s = 0; s < 4; ++s)
#pragma unroll
          for (int kt = 0; kt < 2; ++kt) kf[kt][s] = lds_rd128(ka + kso + kt * 4096 + ((((4 * s) | g) ^ c) << 4));
        SCHED();
#pragma unroll
        for (int s = 0; s < 4; ++s)
#pragma unroll
          for (int kt = 0; kt < 2; ++kt)
#pragma unroll
            for (int qt = 0; qt < 3; ++qt)
              sacc[kt][qt] = mfma(kf[kt][s], qf[s][qt], s == 0 ? (j == 0 ? f32x4{0.f, 0.f, 0.f, 0.f} : negm[qt]) : sacc[kt][qt]);
        SCHED();
        if (j == 0) K_ISSUE(2, 3);
      }
      bool resc = false;
      float al[3];
      if (j == 0) {
#pragma unroll
        for (int qt = 0; qt < 3; ++qt) {
          const float m = xmax_g(max8(sacc[0][qt], sacc[1][qt]));
          if (g == 0) *(LDS_AS float*)(sm + L_ST1 + ((w * 3 + qt) * 16 + c) * 4) = m;
        }
        SYNC(0);
#pragma unroll
        for (int qt = 0; qt < 3; ++qt) {
          float m = *(LDS_AS float*)(sm + L_ST1 + ((h * 12 + qt) * 16 + c) * 4);
#pragma unroll
          for (int k = 1; k < 4; ++k) m = fmaxf(m, *(LDS_AS float*)(sm + L_ST1 + ((h * 12 + 3 * k + qt) * 16 + c) * 4));
          mref[qt] = m;
          negm[qt] = f32x4{-m, -m, -m, -m};
          sacc[0][qt] += negm[qt];
          sacc[1][qt] += negm[qt];
        }
      } else {
        if (h == 0) SYNC(j);
        float t = max3(sacc[0][0][0], sacc[0][0][1], sacc[0][0][2]);
        t = max3(t, sacc[0][0][3], sacc[1][0][0]); t = max3(t, sacc[1][0][1], sacc[1][0][2]);
        t = max3(t, sacc[1][0][3], sacc[0][1][0]); t = max3(t, sacc[0][1][1], sacc[0][1][2]);
        t = max3(t, sacc[0][1][3], sacc[1][1][0]); t = max3(t, sacc[1][1][1], sacc[1][1][2]);
        t = max3(t, sacc[1][1][3], sacc[0][2][0]); t = max3(t, sacc[0][2][1], sacc[0][2][2]);
        t = max3(t, sacc[0][2][3], sacc[1][2][0]); t = max3(t, sacc[1][2][1], sacc[1][2][2]);
        t = max3(t, sacc[1][2][3], t);
        resc = __any(!(t <= RESC_T));
        if (__builtin_expect(resc, 0)) {
#pragma unroll
          for (int qt = 0; qt < 3; ++qt) {
            const float d = fmaxf(xmax_g(max8(sacc[0][qt], sacc[1][qt])), 0.f);
            al[qt] = __builtin_amdgcn_exp2f(-d);
            mref[qt] += d;
            negm[qt] -= d;
            sacc[0][qt] -= d;
            sacc[1][qt] -= d;
          }
        }
      }
      bf16x8 pf[3];
#pragma unroll
      for (int qt = 0; qt < 3; ++qt) {
#pragma unroll
        for (int kt = 0; kt < 2; ++kt)
#pragma unroll
          for (int r = 0; r < 4; ++r) sacc[kt][qt][r] = __builtin_amdgcn_exp2f(sacc[kt][qt][r]);
        pf[qt] = cvt8(sacc[0][qt], sacc[1][qt]);
        asm volatile("" : "+v"(pf[qt]));
      }
      SCHED();
      if (j > 0) {
        if (h == 1) SYNC(j);
        if (__builtin_expect(resc, 0)) {
#pragma unroll
          for (int qt = 0; qt < 3; ++qt) {
            lacc[qt] *= al[qt];
#pragma unroll
            for (int i = 0; i < 8; ++i) oacc[i][qt] *= al[qt];
          }
        }
      }
      {
        unsigned vso = (unsigned)(vslot[j] * L_SLOT);
        asm volatile("" : "+s"(vso));
        bf16x8 vf[8];
#pragma unroll
        for (int i = 0; i < 8; ++i) {
          ldsp p = (ldsp)((va[i >> 1] ^ (unsigned)((i & 1) << 5)) + vso);
          vf[i] = lds_tr2(p, p + 4096);
        }
        SCHED();
#pragma unroll
        for (int qt = 0; qt < 3; ++qt) lacc[qt] = mfma(ones, pf[qt], j == 0 ? f32x4{0.f, 0.f, 0.f, 0.f} : lacc[qt]);
#pragma unroll
        for (int i = 0; i < 8; ++i)
#pragma unroll
          for (int qt = 0; qt < 3; ++qt) oacc[i][qt] = mfma(vf[i], pf[qt], j == 0 ? f32x4{0.f, 0.f, 0.f, 0.f} : oacc[i][qt]);
        SCHED();
        if (j == 0 && w != 3) K_ISSUE(3, 4);
        if (j == 1) V_ISSUE(3, 2);
        if (j == 2) WO_ISSUE2(1, 0);
      }
    }
    if (g == 0) {
#pragma unroll
      for (int qt = 0; qt < 3; ++qt) *(LDS_AS f32x2*)(sm + L_ST2 + ((w * 3 + qt) * 16 + c) * 8) = f32x2{mref[qt], lacc[qt][0]};
    }
    BAR(); STAMP(11);
  }
  int tz = threadIdx.x;
  asm volatile("" : "+v"(tz));
  const int l = tz & 63, c = l & 15, g = l >> 4, qq = c >> 2, pp = l & 3;
  {
    float inv[3], sc[3];
    bool differ = false;
#pragma unroll
    for (int qt = 0; qt < 3; ++qt) {
      f32x2 ml[4];
#pragma unroll
      for (int k = 0; k < 4; ++k) ml[k] = *(LDS_AS f32x2*)(sm + L_ST2 + ((h * 12 + 3 * k + qt) * 16 + c) * 8);
      const float m = fmaxf(fmaxf(ml[0][0], ml[1][0]), fmaxf(ml[2][0], ml[3][0]));
      float lt = 0.f;
#pragma unroll
      for (int k = 0; k < 4; ++k) lt += ml[k][1] * __builtin_amdgcn_exp2f(ml[k][0] - m);
      inv[qt] = __builtin_amdgcn_rcpf(lt);
      sc[qt] = __builtin_amdgcn_exp2f(mref[qt] - m);
      differ = differ || (mref[qt] != m);
    }
    if (__builtin_expect(__any(differ), 0)) {
#pragma unroll
      for (int qt = 0; qt < 3; ++qt)
#pragma unroll
        for (int i = 0; i < 8; ++i) oacc[i][qt] *= sc[qt];
    }
#pragma unroll
    for (int r = 1; r < 4; ++r) {
      const int wd = h * 4 + ((kq + r) & 3);
#pragma unroll
      for (int qt = 0; qt < 3; ++qt) *(LDS_AS bf16x8*)(sm + xoff(wd, r - 1, qt) + l * 16) = cvt8(oacc[2 * r][qt], oacc[2 * r + 1][qt]);
    }
    bf16x8 sel0, sel1;
#pragma unroll
    for (int e = 0; e < 4; ++e) {
      const __bf16 v = (4 * g + e == c) ? (__bf16)1.0f : (__bf16)0.0f;
      sel0[e] = v; sel0[4 + e] = (__bf16)0.0f; sel1[e] = (__bf16)0.0f; sel1[4 + e] = v;
    }
    BAR(); STAMP(17);
#pragma unroll
    for (int qt = 0; qt < 3; ++qt) {
#pragma unroll
      for (int r = 0; r < 3; ++r) {
        bf16x8 t = lds_rd128(sm + xoff(w, r, qt) + l * 16);
        oacc[0][qt] = mfma(sel0, t, oacc[0][qt]);
        oacc[1][qt] = mfma(sel1, t, oacc[1][qt]);
      }
    }
#pragma unroll
    for (int qt = 0; qt < 3; ++qt) *(LDS_AS bf16x8*)(sm + L_O2 + (w * 3 + qt) * 1024 + l * 16) = cvt8(oacc[0][qt] * inv[qt], oacc[1][qt] * inv[qt]);
  }
  waitv<0>();
  BAR(); STAMP(18);
  const unsigned ov0 = (unsigned)((l >> 3) * 1024 + (((l & 7) ^ (g << 1)) << 4));
  WO_ISSUE(2, 1); WO_ISSUE(3, 4);

  f32x4 cacc[4][3];
  {
    bf16x8 of[8][3];
#pragma unroll
    for (int u = 0; u < 8; ++u)
#pragma unroll
      for (int qt = 0; qt < 3; ++qt) of[u][qt] = lds_rd128(sm + L_O2 + (((u + b) & 7) * 3 + qt) * 1024 + l * 16);
    const int fC = 2 * (g & 1) + (qq >> 1);
    ldsp wra = sm + w * 4096 + (4 * g + qq) * 128 + 8 * pp;
    bf16x8 wf[2][4];
#define CSLOT(u) (((u) & 3) == 0 ? 3 : (((u) & 3) == 1 ? 0 : (((u) & 3) == 2 ? 1 : 4)))
#define C_READ(u)                                                                                                   \
  do {                                                                                                              \
    _Pragma("unroll") for (int t_ = 0; t_ < 4; ++t_) {                                                              \
      ldsp p_ = wra + CSLOT(u) * L_SLOT + ((t_ ^ fC) << 5);                                                         \
      wf[(u) & 1][t_] = lds_tr2(p_, p_ + 2048);                                                                     \
    }                                                                                                               \
  } while (0)
    C_READ(0);
#pragma unroll
    for (int u = 0; u < 8; ++u) {
      if (u >= 1 && u + 3 < 8) WO_ISSUE(u + 3, CSLOT(u + 3));
      if (u + 1 < 8) {
        const int last = (u + 3 < 8) ? (u + 3) : 7;
        const int rem = last - (u + 1);
        if (rem == 2) waitv<8>(); else if (rem == 1) waitv<4>(); else waitv<0>();
        C_READ(u + 1);
      }
      SCHED();
#pragma unroll
      for (int t = 0; t < 4; ++t)
#pragma unroll
        for (int qt = 0; qt < 3; ++qt) cacc[t][qt] = mfma(wf[u & 1][t], of[u][qt], u == 0 ? f32x4{0.f, 0.f, 0.f, 0.f} : cacc[t][qt]);
      SCHED();
    }
  }
  STAMP(19);
  const int s16 = kvh * 2 + hp;
  {
    auto rs = __builtin_amdgcn_make_buffer_rsrc((void*)(ws + WS_PART + ((size_t)(b * 16 + s16) * 8 + w) * 6144), 0, 6144, 0x00020000);
#pragma unroll
    for (int a = 0; a < 2; ++a)
#pragma unroll
      for (int qt = 0; qt < 3; ++qt)
        __builtin_amdgcn_raw_buffer_store_b128(__builtin_bit_cast(u32x4, cvt8(cacc[2 * a][qt], cacc[2 * a + 1][qt])), rs, l * 16 + (a * 3 + qt) * 1024, 0, 16);
  }
  asm volatile("s_waitcnt vmcnt(0)" ::: "memory");
  BAR();
  if (w == 0) {
    if (l == 0) __hip_atomic_store(flags + s16 * 16, tag, __ATOMIC_RELAXED, __HIP_MEMORY_SCOPE_AGENT);
    for (unsigned spins = 0; spins < (1u << 22); ++spins) {
      const unsigned long long f = __hip_atomic_load(flags + (l & 15) * 16, __ATOMIC_RELAXED, __HIP_MEMORY_SCOPE_AGENT);
      if (__all(f == tag)) break;
      __builtin_amdgcn_s_sleep(2);
    }
    if (s16 == 0 && l == 0) __hip_atomic_store(epoch, tag, __ATOMIC_RELAXED, __HIP_MEMORY_SCOPE_AGENT);
  }
  BAR();
  STAMP(21);
  if (w < 3) {
    const int rem = s16 * 192 + w * 64 + l;
    auto rs = __builtin_amdgcn_make_buffer_rsrc((void*)(ws + WS_PART + (size_t)b * 16 * 49152), 0, 16 * 49152, 0x00020000);
    u32x4 v[16];
#pragma unroll
    for (int sl = 0; sl < 16; ++sl) v[sl] = __builtin_amdgcn_raw_buffer_load_b128(rs, rem * 16, sl * 49152, 16);
    f32x4 a0 = {0.f, 0.f, 0.f, 0.f}, a1 = {0.f, 0.f, 0.f, 0.f};
#pragma unroll
    for (int sl = 0; sl < 16; ++sl) {
#pragma unroll
      for (int e = 0; e < 2; ++e) {
        a0[2 * e] += __uint_as_float(v[sl][e] << 16); a0[2 * e + 1] += __uint_as_float(v[sl][e] & 0xffff0000u);
        a1[2 * e] += __uint_as_float(v[sl][2 + e] << 16); a1[2 * e + 1] += __uint_as_float(v[sl][2 + e] & 0xffff0000u);
      }
    }
    const int tq = rem >> 6, qt = tq % 3, wa = tq / 3;
    const int q = 16 * qt + c, n = 32 * wa + 4 * g;
    __bf16* o = out + ((size_t)b * SQ + q) * DM + n;
    *(bf16x4*)o = cvt4(a0);
    *(bf16x4*)(o + 16) = cvt4(a1);
  }
}

extern "C" void kernel_launch(void* const* d_in, const int* in_sizes, int n_in, void* d_out, int out_size, void* d_ws, size_t ws_size,
                              hipStream_t stream) {
  const __bf16* x = (const __bf16*)d_in[0];
  const __bf16* kc = (const __bf16*)d_in[1];
  const __bf16* vc = (const __bf16*)d_in[2];
  const __bf16* wq = (const __bf16*)d_in[3];
  const __bf16* wo = (const __bf16*)d_in[4];
  (void)hipFuncSetAttribute((const void*)gqa_fused, hipFuncAttributeMaxDynamicSharedMemorySize, LDS_BYTES);
  hipLaunchKernelGGL(gqa_fused, dim3(256), dim3(512), LDS_BYTES, stream, x, kc, vc, wq, wo, (char*)d_ws, (__bf16*)d_out);
}
```

```cpp
#include <hip/hip_runtime.h>
#include <hip/hip_bf16.h>
#include <stdint.h>

typedef __bf16 bf16x8 __attribute__((ext_vector_type(8)));
typedef __bf16 bf16x4 __attribute__((ext_vector_type(4)));
typedef float f32x4 __attribute__((ext_vector_type(4)));
typedef float f32x2 __attribute__((ext_vector_type(2)));
typedef int i32x4 __attribute__((ext_vector_type(4)));
typedef unsigned u32x4 __attribute__((ext_vector_type(4)));
#define LDS_AS __attribute__((address_space(3)))
typedef LDS_AS char* ldsp;

constexpr int B_ = 16, SQ = 48, DM = 512, HQ = 32, HKV = 8, SK = 512, DH = 128;
constexpr float SCL2E = 0.08838834764831845f * 1.4426950408889634f;

constexpr int L_SLOT = 32768;
constexpr int L_RING = 32768;
constexpr int L_X = 114688;
constexpr int L_Q = 131072;
constexpr int L_ST1 = 155648;
constexpr int L_ST2 = 32768 + 24576;
constexpr int L_O2 = 2 * 32768;
constexpr int LDS_BYTES = 163840;
constexpr int WS_EPOCH = 0, WS_FLAGS = 4096, WS_PART = 4096 + 16 * 128 * 64;
constexpr int NRING = 5;
#define RESC_T 16.0f

#define BAR() asm volatile("s_waitcnt lgkmcnt(0)\n\ts_barrier" ::: "memory")
#define SCHED() __builtin_amdgcn_sched_barrier(0)
#define STAMP(i)
template <int N> __device__ __forceinline__ void waitv() { asm volatile("s_waitcnt vmcnt(%0)" ::"i"(N) : "memory"); }

__device__ __forceinline__ void dma4(unsigned lds, unsigned v0, unsigned v1, unsigned v2, unsigned v3, i32x4 srd, unsigned soff) {
  asm volatile("s_mov_b32 m0, %0\n\ts_nop 0\n\t"
               "buffer_load_dwordx4 %1, %5, %6 offen lds\n\t"
               "buffer_load_dwordx4 %2, %5, %6 offen offset:1024 lds\n\t"
               "buffer_load_dwordx4 %3, %5, %6 offen offset:2048 lds\n\t"
               "buffer_load_dwordx4 %4, %5, %6 offen offset:3072 lds"
               ::"s"(lds), "v"(v0), "v"(v1), "v"(v2), "v"(v3), "s"(srd), "s"(soff) : "memory");
}
__device__ __forceinline__ void dma4nt(unsigned lds, unsigned v0, unsigned v1, unsigned v2, unsigned v3, i32x4 srd, unsigned soff) {
  asm volatile("s_mov_b32 m0, %0\n\ts_nop 0\n\t"
               "buffer_load_dwordx4 %1, %5, %6 offen nt lds\n\t"
               "buffer_load_dwordx4 %2, %5, %6 offen offset:1024 nt lds\n\t"
               "buffer_load_dwordx4 %3, %5, %6 offen offset:2048 nt lds\n\t"
               "buffer_load_dwordx4 %4, %5, %6 offen offset:3072 nt lds"
               ::"s"(lds), "v"(v0), "v"(v1), "v"(v2), "v"(v3), "s"(srd), "s"(soff) : "memory");
}
__device__ __forceinline__ i32x4 make_srd(const void* p, unsigned bytes) {
  unsigned long long a = (unsigned long long)p;
  i32x4 s;
  s[0] = (int)(unsigned)a; s[1] = (int)((unsigned)(a >> 32) & 0xffffu); s[2] = (int)bytes; s[3] = 0x00020000;
  return s;
}
__device__ __forceinline__ bf16x8 lds_rd128(ldsp p) { return *(LDS_AS bf16x8*)p; }
__device__ __forceinline__ bf16x8 lds_tr2(ldsp p0, ldsp p1) {
  bf16x4 a = __builtin_amdgcn_ds_read_tr16_b64_v4bf16((LDS_AS bf16x4*)p0);
  bf16x4 b = __builtin_amdgcn_ds_read_tr16_b64_v4bf16((LDS_AS bf16x4*)p1);
  return __builtin_shufflevector(a, b, 0, 1, 2, 3, 4, 5, 6, 7);
}
__device__ __forceinline__ f32x4 mfma(bf16x8 a, bf16x8 b, f32x4 c) { return __builtin_amdgcn_mfma_f32_16x16x32_bf16(a, b, c, 0, 0, 0); }
__device__ __forceinline__ bf16x4 cvt4(f32x4 v) {
  bf16x4 r; r[0] = (__bf16)v[0]; r[1] = (__bf16)v[1]; r[2] = (__bf16)v[2]; r[3] = (__bf16)v[3]; return r;
}
__device__ __forceinline__ bf16x8 cvt8(f32x4 a, f32x4 b) { return __builtin_shufflevector(cvt4(a), cvt4(b), 0, 1, 2, 3, 4, 5, 6, 7); }
__device__ __forceinline__ float max3(float a, float b, float c) { return __builtin_elementwise_maximum(__builtin_elementwise_maximum(a, b), c); }
__device__ __forceinline__ float max8(f32x4 a, f32x4 b) {
  return max3(max3(max3(a[0], a[1], a[2]), a[3], b[0]), max3(b[1], b[2], b[3]), b[3]);
}
__device__ __forceinline__ float xmax_g(float v) {
  auto r = __builtin_amdgcn_permlane16_swap(__float_as_uint(v), __float_as_uint(v), false, false);
  v = fmaxf(__uint_as_float(r[0]), __uint_as_float(r[1]));
  auto r2 = __builtin_amdgcn_permlane32_swap(__float_as_uint(v), __float_as_uint(v), false, false);
  return fmaxf(__uint_as_float(r2[0]), __uint_as_float(r2[1]));
}
__device__ __forceinline__ int xoff(int wd, int r, int qt) { return (r == 0 ? L_O2 : (r == 1 ? L_SLOT : 4 * L_SLOT)) + (wd * 3 + qt) * 1024; }

__global__ __launch_bounds__(512, 2) void gqa_fused(const __bf16* __restrict__ x, const __bf16* __restrict__ kc, const __bf16* __restrict__ vc,
                                                    const __bf16* __restrict__ wq, const __bf16* __restrict__ wo, char* ws, __bf16* __restrict__ out) {
  extern __shared__ __attribute__((aligned(16))) char smem_raw[];
  ldsp sm = (ldsp)smem_raw;
  const unsigned smbase = (unsigned)(uintptr_t)sm;
  const int w = __builtin_amdgcn_readfirstlane(threadIdx.x >> 6);
  const int bid = blockIdx.x, kvh = bid & 7, jj = bid >> 3, b = jj >> 1, hp = jj & 1;
  STAMP(0);
  const int n0 = kvh * 512 + hp * 256;
  const i32x4 srd_k = make_srd(kc + (size_t)(b * HKV + kvh) * SK * DH, SK * DH * 2);
  const i32x4 srd_v = make_srd(vc + (size_t)(b * HKV + kvh) * SK * DH, SK * DH * 2);
  const i32x4 srd_o = make_srd(wo + (size_t)n0 * DM, 256 * DM * 2);
  const int h = w >> 2, kq = w & 3;
  typedef __attribute__((address_space(1))) unsigned long long gu64;
  gu64* epoch = (gu64*)(ws + WS_EPOCH + b * 256);
  gu64* flags = (gu64*)(ws + WS_FLAGS + b * 128 * 64);
  const unsigned long long tag = __hip_atomic_load(epoch, __ATOMIC_RELAXED, __HIP_MEMORY_SCOPE_AGENT) + 1ull;
  f32x4 oacc[8][3];
  f32x4 lacc[3];
  float mref[3];
  u32x4 wreg[3][4];
  {
    const int l = threadIdx.x & 63, c = l & 15, g = l >> 4, qq = c >> 2, pp = l & 3;
    const unsigned kv0 = (unsigned)(g * 256 + ((c ^ g) << 4));
    const unsigned vv0 = (unsigned)(g * 256 + ((c ^ (2 * g)) << 4));
    if (w < 4) {
      f32x4 qacc[4][3];
      const i32x4 srd_x = make_srd(x + (size_t)b * SQ * DM, SQ * DM * 2);
      const i32x4 srd_w = make_srd(wq, DM * HQ * DH * 2);
#pragma unroll
      for (int i = 0; i < 3; ++i) {
        const int r = w * 12 + 4 * i;
        dma4(smbase + L_X + r * 1024, (unsigned)((l ^ (r & 15)) * 16), (unsigned)((l ^ ((r + 1) & 15)) * 16), (unsigned)((l ^ ((r + 2) & 15)) * 16),
             (unsigned)((l ^ ((r + 3) & 15)) * 16), srd_x, (unsigned)(r * 1024));
      }
      const unsigned ring = smbase + L_RING + w * (NRING * 4096);
      const unsigned wv0 = (unsigned)((l >> 3) * 8192 + (((l & 7) ^ (((l >> 4) & 1) << 1)) * 16));
      const unsigned wv1 = (wv0 ^ 64u) + (65536u - 1024u), wv2 = wv0 + 2u * (65536u - 1024u), wv3 = (wv0 ^ 64u) + 3u * (65536u - 1024u);
      const unsigned scol = (unsigned)((n0 + 64 * w) * 2);
#define WQ_ISSUE(s) dma4(ring + ((s) % NRING) * 4096, wv0, wv1, wv2, wv3, srd_w, scol + (unsigned)((((s) + b) & 15) * (32 * 8192)))
      WQ_ISSUE(0); WQ_ISSUE(1); WQ_ISSUE(2); WQ_ISSUE(3);
      waitv<16>();
      STAMP(1); BAR(); STAMP(2);
      const int fA = ((qq >> 1) & 1) | ((g & 1) << 1);
      ldsp wqa = sm + L_RING + w * (NRING * 4096) + (8 * g + qq) * 128 + 8 * pp;
      ldsp xa = sm + L_X + c * 1024;
      bf16x8 fw[2][4], fx[2][3];
#define A_READ(s, buf)                                                                                          \
  do {                                                                                                          \
    _Pragma("unroll") for (int t_ = 0; t_ < 4; ++t_) {                                                          \
      ldsp p_ = wqa + ((s) % NRING) * 4096 + ((t_ ^ fA) << 5);                                                  \
      fw[buf][t_] = lds_tr2(p_, p_ + 512);                                                                      \
    }                                                                                                           \
    _Pragma("unroll") for (int q_ = 0; q_ < 3; ++q_)                                                            \
        fx[buf][q_] = lds_rd128(xa + q_ * 16384 + ((((4 * (((s) + b) & 15)) | g) ^ c) << 4));                   \
  } while (0)
      waitv<12>();
      A_READ(0, 0);
#pragma unroll
      for (int s = 0; s < 16; ++s) {
        if (s + 4 < 16) WQ_ISSUE(s + 4);
        if (s == 12) {
          dma4nt(smbase + (32 * w) * 256, kv0, kv0 ^ 64u, kv0 ^ 128u, kv0 ^ 192u, srd_k, (unsigned)(128 * w * 256));
          dma4nt(smbase + (32 * w + 16) * 256, kv0, kv0 ^ 64u, kv0 ^ 128u, kv0 ^ 192u, srd_k, (unsigned)((128 * w + 16) * 256));
        }
        if (s + 1 < 16) {
          const int rem = ((s + 4 < 16) ? (s + 4) : 15) - (s + 1);
          if (s < 12) waitv<12>(); else if (rem == 2) waitv<16>(); else if (rem == 1) waitv<12>(); else waitv<8>();
          A_READ(s + 1, (s + 1) & 1);
        }
        SCHED();
#pragma unroll
        for (int t = 0; t < 4; ++t)
#pragma unroll
          for (int qt = 0; qt < 3; ++qt) qacc[t][qt] = mfma(fw[s & 1][t], fx[s & 1][qt], s == 0 ? f32x4{0.f, 0.f, 0.f, 0.f} : qacc[t][qt]);
        SCHED();
      }
      STAMP(3); BAR(); STAMP(4);
#pragma unroll
      for (int t = 0; t < 4; ++t)
#pragma unroll
        for (int qt = 0; qt < 3; ++qt) {
          const int c16 = 8 * (w & 1) + 2 * t + (g >> 1);
          *(LDS_AS bf16x4*)(sm + L_Q + (48 * (w >> 1) + 16 * qt + c) * 256 + ((c16 ^ c) << 4) + 8 * (g & 1)) = cvt4(qacc[t][qt] * SCL2E);
        }
    } else {
      STAMP(1); BAR(); STAMP(2);
      STAMP(3); BAR(); STAMP(4);
    }
    const unsigned kvrow = (unsigned)(128 * kq * 256);
#define K_ISSUE(j, slot)                                                                                                                  \
  do { if (h == 0) {                                                                                                                      \
    dma4nt(smbase + (slot) * L_SLOT + kq * 8192, kv0, kv0 ^ 64u, kv0 ^ 128u, kv0 ^ 192u, srd_k, kvrow + (unsigned)((j) * 8192));          \
    dma4nt(smbase + (slot) * L_SLOT + kq * 8192 + 4096, kv0, kv0 ^ 64u, kv0 ^ 128u, kv0 ^ 192u, srd_k, kvrow + (unsigned)((j) * 8192 + 4096)); \
  } } while (0)
#define V_ISSUE(j, slot)                                                                                                                  \
  do { if (h == 0) {                                                                                                                      \
    dma4nt(smbase + (slot) * L_SLOT + kq * 8192, vv0, vv0 ^ 128u, vv0, vv0 ^ 128u, srd_v, kvrow + (unsigned)((j) * 8192));                \
    dma4nt(smbase + (slot) * L_SLOT + kq * 8192 + 4096, vv0, vv0 ^ 128u, vv0, vv0 ^ 128u, srd_v, kvrow + (unsigned)((j) * 8192 + 4096));  \
  } } while (0)
    const unsigned ov0 = (unsigned)((l >> 3) * 1024 + (((l & 7) ^ (g << 1)) << 4));
#define WO_ISSUE_W(u, slot, ww) dma4(smbase + (slot) * L_SLOT + (ww) * 4096, ov0, ov0 + 7168u, ov0 + 14336u, ov0 + 21504u, srd_o, (unsigned)((ww) * 128 + (((u) + b) & 7) * 32768))
#define WO_ISSUE(u, slot) WO_ISSUE_W(u, slot, w)
#define WO_ISSUE2(u, slot) do { if (h == 0) { WO_ISSUE_W(u, slot, w); WO_ISSUE_W(u, slot, w + 4); } } while (0)
    K_ISSUE(1, 1);
    waitv<8>();
    STAMP(5); BAR(); STAMP(6);

    bf16x8 qf[4][3];
#pragma unroll
    for (int s = 0; s < 4; ++s)
#pragma unroll
      for (int qt = 0; qt < 3; ++qt) qf[s][qt] = lds_rd128(sm + L_Q + (48 * h + 16 * qt + c) * 256 + ((((4 * s) | g) ^ c) << 4));
    V_ISSUE(0, 2);
    ldsp ka = sm + (32 * kq + c) * 256;
    const int fV = 4 * (g & 1) + qq;
    unsigned va[4];
#pragma unroll
    for (int r = 0; r < 4; ++r) va[r] = smbase + (32 * kq + 4 * g + qq) * 256 + 8 * pp + (((2 * ((r + kq) & 3)) ^ fV) << 5);
    bf16x8 ones;
#pragma unroll
    for (int e = 0; e < 8; ++e) ones[e] = (__bf16)1.0f;
    f32x4 negm[3];
    constexpr int kslot[4] = {0, 1, 3, 4}, vslot[4] = {2, 0, 1, 2};
#define SYNC(j)                                                               \
  do {                                                                        \
    if ((j) < 3) waitv<8>(); else waitv<16>();                                \
    if ((j) == 1 && w == 3) waitv<0>();                                       \
    BAR(); STAMP(7 + (j));                                                    \
    if ((j) == 0) { V_ISSUE(1, 0); }                                          \
    if ((j) == 1) { if (w == 3) K_ISSUE(3, 4); V_ISSUE(2, 1); }               \
    if ((j) == 2) { WO_ISSUE2(0, 3); }                                        \
  } while (0)
#pragma unroll
    for (int j = 0; j < 4; ++j) {
      f32x4 sacc[2][3];
      {
        unsigned kso = (unsigned)(kslot[j] * L_SLOT);
        asm volatile("" : "+s"(kso));
        bf16x8 kf[2][4];
#pragma unroll
        for (int s = 0; s < 4; ++s)
#pragma unroll
          for (int kt = 0; kt < 2; ++kt) kf[kt][s] = lds_rd128(ka + kso + kt * 4096 + ((((4 * s) | g) ^ c) << 4));
        SCHED();
#pragma unroll
        for (int s = 0; s < 4; ++s)
#pragma unroll
          for (int kt = 0; kt < 2; ++kt)
#pragma unroll
            for (int qt = 0; qt < 3; ++qt)
              sacc[kt][qt] = mfma(kf[kt][s], qf[s][qt], s == 0 ? (j == 0 ? f32x4{0.f, 0.f, 0.f, 0.f} : negm[qt]) : sacc[kt][qt]);
        SCHED();
        if (j == 0) K_ISSUE(2, 3);
      }
      bool resc = false;
      float al[3];
      if (j == 0) {
#pragma unroll
        for (int qt = 0; qt < 3; ++qt) {
          const float m = xmax_g(max8(sacc[0][qt], sacc[1][qt]));
          if (g == 0) *(LDS_AS float*)(sm + L_ST1 + ((w * 3 + qt) * 16 + c) * 4) = m;
        }
        SYNC(0);
#pragma unroll
        for (int qt = 0; qt < 3; ++qt) {
          float m = *(LDS_AS float*)(sm + L_ST1 + ((h * 12 + qt) * 16 + c) * 4);
#pragma unroll
          for (int k = 1; k < 4; ++k) m = fmaxf(m, *(LDS_AS float*)(sm + L_ST1 + ((h * 12 + 3 * k + qt) * 16 + c) * 4));
          mref[qt] = m;
          negm[qt] = f32x4{-m, -m, -m, -m};
          sacc[0][qt] += negm[qt];
          sacc[1][qt] += negm[qt];
        }
      } else {
        if (h == 0) SYNC(j);
        if (j == 3) {
          auto rso = __builtin_amdgcn_make_buffer_rsrc((void*)(wo + (size_t)n0 * DM), 0, 256 * DM * 2, 0x00020000);
#pragma unroll
          for (int u = 0; u < 3; ++u)
#pragma unroll
            for (int i = 0; i < 4; ++i)
              wreg[u][i] = __builtin_amdgcn_raw_buffer_load_b128(rso, (l >> 3) * 1024 + (l & 7) * 16, w * 128 + ((u + 2 + b) & 7) * 32768 + i * 8192, 0);
        }
        float t = max3(sacc[0][0][0], sacc[0][0][1], sacc[0][0][2]);
        t = max3(t, sacc[0][0][3], sacc[1][0][0]); t = max3(t, sacc[1][0][1], sacc[1][0][2]);
        t = max3(t, sacc[1][0][3], sacc[0][1][0]); t = max3(t, sacc[0][1][1], sacc[0][1][2]);
        t = max3(t, sacc[0][1][3], sacc[1][1][0]); t = max3(t, sacc[1][1][1], sacc[1][1][2]);
        t = max3(t, sacc[1][1][3], sacc[0][2][0]); t = max3(t, sacc[0][2][1], sacc[0][2][2]);
        t = max3(t, sacc[0][2][3], sacc[1][2][0]); t = max3(t, sacc[1][2][1], sacc[1][2][2]);
        t = max3(t, sacc[1][2][3], t);
        resc = __any(!(t <= RESC_T));
        if (__builtin_expect(resc, 0)) {
#pragma unroll
          for (int qt = 0; qt < 3; ++qt) {
            const float d = fmaxf(xmax_g(max8(sacc[0][qt], sacc[1][qt])), 0.f);
            al[qt] = __builtin_amdgcn_exp2f(-d);
            mref[qt] += d;
            negm[qt] -= d;
            sacc[0][qt] -= d;
            sacc[1][qt] -= d;
          }
        }
      }
      bf16x8 pf[3];
#pragma unroll
      for (int qt = 0; qt < 3; ++qt) {
#pragma unroll
        for (int kt = 0; kt < 2; ++kt)
#pragma unroll
          for (int r = 0; r < 4; ++r) sacc[kt][qt][r] = __builtin_amdgcn_exp2f(sacc[kt][qt][r]);
        pf[qt] = cvt8(sacc[0][qt], sacc[1][qt]);
        asm volatile("" : "+v"(pf[qt]));
      }
      SCHED();
      if (j > 0) {
        if (h == 1) SYNC(j);
        if (__builtin_expect(resc, 0)) {
#pragma unroll
          for (int qt = 0; qt < 3; ++qt) {
            lacc[qt] *= al[qt];
#pragma unroll
            for (int i = 0; i < 8; ++i) oacc[i][qt] *= al[qt];
          }
        }
      }
      {
        unsigned vso = (unsigned)(vslot[j] * L_SLOT);
        asm volatile("" : "+s"(vso));
        bf16x8 vf[8];
#pragma unroll
        for (int i = 0; i < 8; ++i) {
          ldsp p = (ldsp)((va[i >> 1] ^ (unsigned)((i & 1) << 5)) + vso);
          vf[i] = lds_tr2(p, p + 4096);
        }
        SCHED();
#pragma unroll
        for (int qt = 0; qt < 3; ++qt) lacc[qt] = mfma(ones, pf[qt], j == 0 ? f32x4{0.f, 0.f, 0.f, 0.f} : lacc[qt]);
#pragma unroll
        for (int i = 0; i < 8; ++i)
#pragma unroll
          for (int qt = 0; qt < 3; ++qt) oacc[i][qt] = mfma(vf[i], pf[qt], j == 0 ? f32x4{0.f, 0.f, 0.f, 0.f} : oacc[i][qt]);
        SCHED();
        if (j == 0 && w != 3) K_ISSUE(3, 4);
        if (j == 1) V_ISSUE(3, 2);
        if (j == 2) WO_ISSUE2(1, 0);
      }
    }
    if (g == 0) {
#pragma unroll
      for (int qt = 0; qt < 3; ++qt) *(LDS_AS f32x2*)(sm + L_ST2 + ((w * 3 + qt) * 16 + c) * 8) = f32x2{mref[qt], lacc[qt][0]};
    }
    BAR(); STAMP(11);
  }
  int tz = threadIdx.x;
  asm volatile("" : "+v"(tz));
  const int l = tz & 63, c = l & 15, g = l >> 4, qq = c >> 2, pp = l & 3;
  {
    float inv[3], sc[3];
    bool differ = false;
#pragma unroll
    for (int qt = 0; qt < 3; ++qt) {
      f32x2 ml[4];
#pragma unroll
      for (int k = 0; k < 4; ++k) ml[k] = *(LDS_AS f32x2*)(sm + L_ST2 + ((h * 12 + 3 * k + qt) * 16 + c) * 8);
      const float m = fmaxf(fmaxf(ml[0][0], ml[1][0]), fmaxf(ml[2][0], ml[3][0]));
      float lt = 0.f;
#pragma unroll
      for (int k = 0; k < 4; ++k) lt += ml[k][1] * __builtin_amdgcn_exp2f(ml[k][0] - m);
      inv[qt] = __builtin_amdgcn_rcpf(lt);
      sc[qt] = __builtin_amdgcn_exp2f(mref[qt] - m);
      differ = differ || (mref[qt] != m);
    }
    if (__builtin_expect(__any(differ), 0)) {
#pragma unroll
      for (int qt = 0; qt < 3; ++qt)
#pragma unroll
        for (int i = 0; i < 8; ++i) oacc[i][qt] *= sc[qt];
    }
#pragma unroll
    for (int r = 1; r < 4; ++r) {
      const int wd = h * 4 + ((kq + r) & 3);
#pragma unroll
      for (int qt = 0; qt < 3; ++qt) *(LDS_AS bf16x8*)(sm + xoff(wd, r - 1, qt) + l * 16) = cvt8(oacc[2 * r][qt], oacc[2 * r + 1][qt]);
    }
    bf16x8 sel0, sel1;
#pragma unroll
    for (int e = 0; e < 4; ++e) {
      const __bf16 v = (4 * g + e == c) ? (__bf16)1.0f : (__bf16)0.0f;
      sel0[e] = v; sel0[4 + e] = (__bf16)0.0f; sel1[e] = (__bf16)0.0f; sel1[4 + e] = v;
    }
    BAR(); STAMP(17);
#pragma unroll
    for (int qt = 0; qt < 3; ++qt) {
#pragma unroll
      for (int r = 0; r < 3; ++r) {
        bf16x8 t = lds_rd128(sm + xoff(w, r, qt) + l * 16);
        oacc[0][qt] = mfma(sel0, t, oacc[0][qt]);
        oacc[1][qt] = mfma(sel1, t, oacc[1][qt]);
      }
    }
#pragma unroll
    for (int qt = 0; qt < 3; ++qt) *(LDS_AS bf16x8*)(sm + L_O2 + (w * 3 + qt) * 1024 + l * 16) = cvt8(oacc[0][qt] * inv[qt], oacc[1][qt] * inv[qt]);
  }
  waitv<12>();
  BAR(); STAMP(18);
  const unsigned ov0 = (unsigned)((l >> 3) * 1024 + (((l & 7) ^ (g << 1)) << 4));
  WO_ISSUE(5, 1); WO_ISSUE(6, 4);

  f32x4 cacc[4][3];
  {
    bf16x8 of[8][3];
#pragma unroll
    for (int u = 0; u < 8; ++u)
#pragma unroll
      for (int qt = 0; qt < 3; ++qt) of[u][qt] = lds_rd128(sm + L_O2 + (((u + b) & 7) * 3 + qt) * 1024 + l * 16);
    const int fC = 2 * (g & 1) + (qq >> 1);
    ldsp wra = sm + w * 4096 + (4 * g + qq) * 128 + 8 * pp;
    bf16x8 wf[2][4];
#define CSLOT(u) ((u) == 5 ? 1 : ((u) == 6 ? 4 : (((u) & 1) ? 0 : 3)))
#define C_READ(u)                                                                                                   \
  do {                                                                                                              \
    _Pragma("unroll") for (int t_ = 0; t_ < 4; ++t_) {                                                              \
      ldsp p_ = wra + CSLOT(u) * L_SLOT + ((t_ ^ fC) << 5);                                                         \
      wf[(u) & 1][t_] = lds_tr2(p_, p_ + 2048);                                                                     \
    }                                                                                                               \
  } while (0)
    ldsp wst = sm + w * 4096 + (l >> 3) * 128 + (((l & 7) ^ (g << 1)) << 4);
    C_READ(0);
#pragma unroll
    for (int u = 0; u < 8; ++u) {
      if (u < 3) {
#pragma unroll
        for (int i = 0; i < 4; ++i) *(LDS_AS u32x4*)(wst + CSLOT(u + 2) * L_SLOT + i * 1024) = wreg[u][i];
      }
      if (u == 4) WO_ISSUE(7, 0);
      if (u + 1 < 8) {
        if (u + 1 == 5) waitv<8>(); else if (u + 1 == 6) waitv<4>(); else if (u + 1 == 7) waitv<0>();
        C_READ(u + 1);
      }
      SCHED();
#pragma unroll
      for (int t = 0; t < 4; ++t)
#pragma unroll
        for (int qt = 0; qt < 3; ++qt) cacc[t][qt] = mfma(wf[u & 1][t], of[u][qt], u == 0 ? f32x4{0.f, 0.f, 0.f, 0.f} : cacc[t][qt]);
      SCHED();
    }
  }
  STAMP(19);
  const int s16 = kvh * 2 + hp;
  {
    auto rs = __builtin_amdgcn_make_buffer_rsrc((void*)(ws + WS_PART + ((size_t)(b * 16 + s16) * 8 + w) * 6144), 0, 6144, 0x00020000);
#pragma unroll
    for (int a = 0; a < 2; ++a)
#pragma unroll
      for (int qt = 0; qt < 3; ++qt)
        __builtin_amdgcn_raw_buffer_store_b128(__builtin_bit_cast(u32x4, cvt8(cacc[2 * a][qt], cacc[2 * a + 1][qt])), rs, l * 16 + (a * 3 + qt) * 1024, 0, 16);
  }
  asm volatile("s_waitcnt vmcnt(0)" ::: "memory");
  if (l == 0) __hip_atomic_store(flags + (w * 16 + s16) * 8, tag, __ATOMIC_RELAXED, __HIP_MEMORY_SCOPE_AGENT);
  if (w < 3) {
    for (unsigned spins = 0; spins < (1u << 22); ++spins) {
      const unsigned long long f = __hip_atomic_load(flags + ((s16 >> 1) * 16 + (l & 15)) * 8, __ATOMIC_RELAXED, __HIP_MEMORY_SCOPE_AGENT);
      if (__all(f == tag)) break;
      __builtin_amdgcn_s_sleep(1);
    }
    __builtin_amdgcn_fence(__ATOMIC_ACQUIRE, "wavefront");
    if (s16 == 0 && w == 0 && l == 0) __hip_atomic_store(epoch, tag, __ATOMIC_RELAXED, __HIP_MEMORY_SCOPE_AGENT);
    STAMP(21);
    const int rem = s16 * 192 + w * 64 + l;
    auto rs = __builtin_amdgcn_make_buffer_rsrc((void*)(ws + WS_PART + (size_t)b * 16 * 49152), 0, 16 * 49152, 0x00020000);
    u32x4 v[16];
#pragma unroll
    for (int sl = 0; sl < 16; ++sl) v[sl] = __builtin_amdgcn_raw_buffer_load_b128(rs, rem * 16, sl * 49152, 16);
    f32x4 a0 = {0.f, 0.f, 0.f, 0.f}, a1 = {0.f, 0.f, 0.f, 0.f};
#pragma unroll
    for (int sl = 0; sl < 16; ++sl) {
#pragma unroll
      for (int e = 0; e < 2; ++e) {
        a0[2 * e] += __uint_as_float(v[sl][e] << 16); a0[2 * e + 1] += __uint_as_float(v[sl][e] & 0xffff0000u);
        a1[2 * e] += __uint_as_float(v[sl][2 + e] << 16); a1[2 * e + 1] += __uint_as_float(v[sl][2 + e] & 0xffff0000u);
      }
    }
    const int tq = rem >> 6, qt = tq % 3, wa = tq / 3;
    const int q = 16 * qt + c, n = 32 * wa + 4 * g;
    __bf16* o = out + ((size_t)b * SQ + q) * DM + n;
    *(bf16x4*)o = cvt4(a0);
    *(bf16x4*)(o + 16) = cvt4(a1);
  }
}

extern "C" void kernel_launch(void* const* d_in, const int* in_sizes, int n_in, void* d_out, int out_size, void* d_ws, size_t ws_size,
                              hipStream_t stream) {
  const __bf16* x = (const __bf16*)d_in[0];
  const __bf16* kc = (const __bf16*)d_in[1];
  const __bf16* vc = (const __bf16*)d_in[2];
  const __bf16* wq = (const __bf16*)d_in[3];
  const __bf16* wo = (const __bf16*)d_in[4];
  (void)hipFuncSetAttribute((const void*)gqa_fused, hipFuncAttributeMaxDynamicSharedMemorySize, LDS_BYTES);
  hipLaunchKernelGGL(gqa_fused, dim3(256), dim3(512), LDS_BYTES, stream, x, kc, vc, wq, wo, (char*)d_ws, (__bf16*)d_out);
}
```

```cpp
#include <hip/hip_runtime.h>
#include <hip/hip_bf16.h>
#include <stdint.h>

typedef __bf16 bf16x8 __attribute__((ext_vector_type(8)));
typedef __bf16 bf16x4 __attribute__((ext_vector_type(4)));
typedef float f32x4 __attribute__((ext_vector_type(4)));
typedef float f32x2 __attribute__((ext_vector_type(2)));
typedef int i32x4 __attribute__((ext_vector_type(4)));
typedef unsigned u32x4 __attribute__((ext_vector_type(4)));
#define LDS_AS __attribute__((address_space(3)))
typedef LDS_AS char* ldsp;

constexpr int B_ = 16, SQ = 48, DM = 512, HQ = 32, HKV = 8, SK = 512, DH = 128;
constexpr float SCL2E = 0.08838834764831845f * 1.4426950408889634f;

constexpr int L_SLOT = 32768;
constexpr int L_RING = 32768;
constexpr int L_X = 114688;
constexpr int L_Q = 131072;
constexpr int L_ST1 = 155648;
constexpr int L_ST2 = 32768 + 24576;
constexpr int L_O2 = 2 * 32768;
constexpr int LDS_BYTES = 163840;
constexpr int WS_EPOCH = 0, WS_FLAGS = 4096, WS_PART = 4096 + 16 * 128 * 64;
constexpr int NRING = 5;
#define RESC_T 16.0f

#define BAR() asm volatile("s_waitcnt lgkmcnt(0)\n\ts_barrier" ::: "memory")
#define SCHED() __builtin_amdgcn_sched_barrier(0)
#define STAMP(i)
template <int N> __device__ __forceinline__ void waitv() { asm volatile("s_waitcnt vmcnt(%0)" ::"i"(N) : "memory"); }

__device__ __forceinline__ void dma4(unsigned lds, unsigned v0, unsigned v1, unsigned v2, unsigned v3, i32x4 srd, unsigned soff) {
  asm volatile("s_mov_b32 m0, %0\n\ts_nop 0\n\t"
               "buffer_load_dwordx4 %1, %5, %6 offen lds\n\t"
               "buffer_load_dwordx4 %2, %5, %6 offen offset:1024 lds\n\t"
               "buffer_load_dwordx4 %3, %5, %6 offen offset:2048 lds\n\t"
               "buffer_load_dwordx4 %4, %5, %6 offen offset:3072 lds"
               ::"s"(lds), "v"(v0), "v"(v1), "v"(v2), "v"(v3), "s"(srd), "s"(soff) : "memory");
}
__device__ __forceinline__ void dma4nt(unsigned lds, unsigned v0, unsigned v1, unsigned v2, unsigned v3, i32x4 srd, unsigned soff) {
  asm volatile("s_mov_b32 m0, %0\n\ts_nop 0\n\t"
               "buffer_load_dwordx4 %1, %5, %6 offen nt lds\n\t"
               "buffer_load_dwordx4 %2, %5, %6 offen offset:1024 nt lds\n\t"
               "buffer_load_dwordx4 %3, %5, %6 offen offset:2048 nt lds\n\t"
               "buffer_load_dwordx4 %4, %5, %6 offen offset:3072 nt lds"
               ::"s"(lds), "v"(v0), "v"(v1), "v"(v2), "v"(v3), "s"(srd), "s"(soff) : "memory");
}
__device__ __forceinline__ i32x4 make_srd(const void* p, unsigned bytes) {
  unsigned long long a = (unsigned long long)p;
  i32x4 s;
  s[0] = (int)(unsigned)a; s[1] = (int)((unsigned)(a >> 32) & 0xffffu); s[2] = (int)bytes; s[3] = 0x00020000;
  return s;
}
__device__ __forceinline__ bf16x8 lds_rd128(ldsp p) { return *(LDS_AS bf16x8*)p; }
__device__ __forceinline__ bf16x8 lds_tr2(ldsp p0, ldsp p1) {
  bf16x4 a = __builtin_amdgcn_ds_read_tr16_b64_v4bf16((LDS_AS bf16x4*)p0);
  bf16x4 b = __builtin_amdgcn_ds_read_tr16_b64_v4bf16((LDS_AS bf16x4*)p1);
  return __builtin_shufflevector(a, b, 0, 1, 2, 3, 4, 5, 6, 7);
}
__device__ __forceinline__ f32x4 mfma(bf16x8 a, bf16x8 b, f32x4 c) { return __builtin_amdgcn_mfma_f32_16x16x32_bf16(a, b, c, 0, 0, 0); }
__device__ __forceinline__ bf16x4 cvt4(f32x4 v) {
  bf16x4 r; r[0] = (__bf16)v[0]; r[1] = (__bf16)v[1]; r[2] = (__bf16)v[2]; r[3] = (__bf16)v[3]; return r;
}
__device__ __forceinline__ bf16x8 cvt8(f32x4 a, f32x4 b) { return __builtin_shufflevector(cvt4(a), cvt4(b), 0, 1, 2, 3, 4, 5, 6, 7); }
__device__ __forceinline__ float max3(float a, float b, float c) { return __builtin_elementwise_maximum(__builtin_elementwise_maximum(a, b), c); }
__device__ __forceinline__ float max8(f32x4 a, f32x4 b) {
  return max3(max3(max3(a[0], a[1], a[2]), a[3], b[0]), max3(b[1], b[2], b[3]), b[3]);
}
__device__ __forceinline__ float xmax_g(float v) {
  auto r = __builtin_amdgcn_permlane16_swap(__float_as_uint(v), __float_as_uint(v), false, false);
  v = fmaxf(__uint_as_float(r[0]), __uint_as_float(r[1]));
  auto r2 = __builtin_amdgcn_permlane32_swap(__float_as_uint(v), __float_as_uint(v), false, false);
  return fmaxf(__uint_as_float(r2[0]), __uint_as_float(r2[1]));
}
__device__ __forceinline__ int xoff(int wd, int r, int qt) { return (r == 0 ? L_O2 : (r == 1 ? L_SLOT : 4 * L_SLOT)) + (wd * 3 + qt) * 1024; }

__global__ __launch_bounds__(512, 2) void gqa_fused(const __bf16* __restrict__ x, const __bf16* __restrict__ kc, const __bf16* __restrict__ vc,
                                                    const __bf16* __restrict__ wq, const __bf16* __restrict__ wo, char* ws, __bf16* __restrict__ out) {
  extern __shared__ __attribute__((aligned(16))) char smem_raw[];
  ldsp sm = (ldsp)smem_raw;
  const unsigned smbase = (unsigned)(uintptr_t)sm;
  const int w = __builtin_amdgcn_readfirstlane(threadIdx.x >> 6);
  const int bid = blockIdx.x, kvh = bid & 7, jj = bid >> 3, b = jj >> 1, hp = jj & 1;
  STAMP(0);
  const int n0 = kvh * 512 + hp * 256;
  const i32x4 srd_k = make_srd(kc + (size_t)(b * HKV + kvh) * SK * DH, SK * DH * 2);
  const i32x4 srd_v = make_srd(vc + (size_t)(b * HKV + kvh) * SK * DH, SK * DH * 2);
  const i32x4 srd_o = make_srd(wo + (size_t)n0 * DM, 256 * DM * 2);
  const int h = w >> 2, kq = w & 3;
  typedef __attribute__((address_space(1))) unsigned long long gu64;
  gu64* epoch = (gu64*)(ws + WS_EPOCH + b * 256);
  gu64* flags = (gu64*)(ws + WS_FLAGS + b * 128 * 64);
  const unsigned long long tag = __hip_atomic_load(epoch, __ATOMIC_RELAXED, __HIP_MEMORY_SCOPE_AGENT) + 1ull;
  f32x4 oacc[8][3];
  f32x4 lacc[3];
  float mref[3];
  u32x4 wreg[3][4];
  {
    const int l = threadIdx.x & 63, c = l & 15, g = l >> 4, qq = c >> 2, pp = l & 3;
    const unsigned kv0 = (unsigned)(g * 256 + ((c ^ g) << 4));
    const unsigned vv0 = (unsigned)(g * 256 + ((c ^ (2 * g)) << 4));
    if (w < 4) {
      f32x4 qacc[4][3];
      const i32x4 srd_x = make_srd(x + (size_t)b * SQ * DM, SQ * DM * 2);
      const i32x4 srd_w = make_srd(wq, DM * HQ * DH * 2);
#pragma unroll
      for (int i = 0; i < 3; ++i) {
        const int r = w * 12 + 4 * i;
        dma4(smbase + L_X + r * 1024, (unsigned)((l ^ (r & 15)) * 16), (unsigned)((l ^ ((r + 1) & 15)) * 16), (unsigned)((l ^ ((r + 2) & 15)) * 16),
             (unsigned)((l ^ ((r + 3) & 15)) * 16), srd_x, (unsigned)(r * 1024));
      }
      const unsigned ring = smbase + L_RING + w * (NRING * 4096);
      const unsigned wv0 = (unsigned)((l >> 3) * 8192 + (((l & 7) ^ (((l >> 4) & 1) << 1)) * 16));
      const unsigned wv1 = (wv0 ^ 64u) + (65536u - 1024u), wv2 = wv0 + 2u * (65536u - 1024u), wv3 = (wv0 ^ 64u) + 3u * (65536u - 1024u);
      const unsigned scol = (unsigned)((n0 + 64 * w) * 2);
#define WQ_ISSUE(s) dma4(ring + ((s) % NRING) * 4096, wv0, wv1, wv2, wv3, srd_w, scol + (unsigned)((((s) + b) & 15) * (32 * 8192)))
      WQ_ISSUE(0); WQ_ISSUE(1); WQ_ISSUE(2); WQ_ISSUE(3);
      waitv<16>();
      STAMP(1); BAR(); STAMP(2);
      const int fA = ((qq >> 1) & 1) | ((g & 1) << 1);
      ldsp wqa = sm + L_RING + w * (NRING * 4096) + (8 * g + qq) * 128 + 8 * pp;
      ldsp xa = sm + L_X + c * 1024;
      bf16x8 fw[2][4], fx[2][3];
#define A_READ(s, buf)                                                                                          \
  do {                                                                                                          \
    _Pragma("unroll") for (int t_ = 0; t_ < 4; ++t_) {                                                          \
      ldsp p_ = wqa + ((s) % NRING) * 4096 + ((t_ ^ fA) << 5);                                                  \
      fw[buf][t_] = lds_tr2(p_, p_ + 512);                                                                      \
    }                                                                                                           \
    _Pragma("unroll") for (int q_ = 0; q_ < 3; ++q_)                                                            \
        fx[buf][q_] = lds_rd128(xa + q_ * 16384 + ((((4 * (((s) + b) & 15)) | g) ^ c) << 4));                   \
  } while (0)
      waitv<12>();
      A_READ(0, 0);
#pragma unroll
      for (int s = 0; s < 16; ++s) {
        if (s + 4 < 16) WQ_ISSUE(s + 4);
        if (s == 12) {
          dma4nt(smbase + (32 * w) * 256, kv0, kv0 ^ 64u, kv0 ^ 128u, kv0 ^ 192u, srd_k, (unsigned)(128 * w * 256));
          dma4nt(smbase + (32 * w + 16) * 256, kv0, kv0 ^ 64u, kv0 ^ 128u, kv0 ^ 192u, srd_k, (unsigned)((128 * w + 16) * 256));
        }
        if (s + 1 < 16) {
          const int rem = ((s + 4 < 16) ? (s + 4) : 15) - (s + 1);
          if (s < 12) waitv<12>(); else if (rem == 2) waitv<16>(); else if (rem == 1) waitv<12>(); else waitv<8>();
          A_READ(s + 1, (s + 1) & 1);
        }
        SCHED();
#pragma unroll
        for (int t = 0; t < 4; ++t)
#pragma unroll
          for (int qt = 0; qt < 3; ++qt) qacc[t][qt] = mfma(fw[s & 1][t], fx[s & 1][qt], s == 0 ? f32x4{0.f, 0.f, 0.f, 0.f} : qacc[t][qt]);
        SCHED();
      }
      STAMP(3); BAR(); STAMP(4);
#pragma unroll
      for (int t = 0; t < 4; ++t)
#pragma unroll
        for (int qt = 0; qt < 3; ++qt) {
          const int c16 = 8 * (w & 1) + 2 * t + (g >> 1);
          *(LDS_AS bf16x4*)(sm + L_Q + (48 * (w >> 1) + 16 * qt + c) * 256 + ((c16 ^ c) << 4) + 8 * (g & 1)) = cvt4(qacc[t][qt] * SCL2E);
        }
    } else {
      STAMP(1); BAR(); STAMP(2);
      STAMP(3); BAR(); STAMP(4);
    }
    const unsigned kvrow = (unsigned)(128 * kq * 256);
#define K_ISSUE(j, slot)                                                                                                                  \
  do { if (h == 0) {                                                                                                                      \
    dma4nt(smbase + (slot) * L_SLOT + kq * 8192, kv0, kv0 ^ 64u, kv0 ^ 128u, kv0 ^ 192u, srd_k, kvrow + (unsigned)((j) * 8192));          \
    dma4nt(smbase + (slot) * L_SLOT + kq * 8192 + 4096, kv0, kv0 ^ 64u, kv0 ^ 128u, kv0 ^ 192u, srd_k, kvrow + (unsigned)((j) * 8192 + 4096)); \
  } } while (0)
#define V_ISSUE(j, slot)                                                                                                                  \
  do { if (h == 0) {                                                                                                                      \
    dma4nt(smbase + (slot) * L_SLOT + kq * 8192, vv0, vv0 ^ 128u, vv0, vv0 ^ 128u, srd_v, kvrow + (unsigned)((j) * 8192));                \
    dma4nt(smbase + (slot) * L_SLOT + kq * 8192 + 4096, vv0, vv0 ^ 128u, vv0, vv0 ^ 128u, srd_v, kvrow + (unsigned)((j) * 8192 + 4096));  \
  } } while (0)
    const unsigned ov0 = (unsigned)((l >> 3) * 1024 + (((l & 7) ^ (g << 1)) << 4));
#define WO_ISSUE_W(u, slot, ww) dma4(smbase + (slot) * L_SLOT + (ww) * 4096, ov0, ov0 + 7168u, ov0 + 14336u, ov0 + 21504u, srd_o, (unsigned)((ww) * 128 + (((u) + b) & 7) * 32768))
#define WO_ISSUE(u, slot) WO_ISSUE_W(u, slot, w)
#define WO_ISSUE2(u, slot) do { if (h == 0) { WO_ISSUE_W(u, slot, w); WO_ISSUE_W(u, slot, w + 4); } } while (0)
    K_ISSUE(1, 1);
    waitv<8>();
    STAMP(5); BAR(); STAMP(6);

    bf16x8 qf[4][3];
#pragma unroll
    for (int s = 0; s < 4; ++s)
#pragma unroll
      for (int qt = 0; qt < 3; ++qt) qf[s][qt] = lds_rd128(sm + L_Q + (48 * h + 16 * qt + c) * 256 + ((((4 * s) | g) ^ c) << 4));
    V_ISSUE(0, 2);
    ldsp ka = sm + (32 * kq + c) * 256;
    const int fV = 4 * (g & 1) + qq;
    unsigned va[4];
#pragma unroll
    for (int r = 0; r < 4; ++r) va[r] = smbase + (32 * kq + 4 * g + qq) * 256 + 8 * pp + (((2 * ((r + kq) & 3)) ^ fV) << 5);
    bf16x8 ones;
#pragma unroll
    for (int e = 0; e < 8; ++e) ones[e] = (__bf16)1.0f;
    f32x4 negm[3];
    constexpr int kslot[4] = {0, 1, 3, 4}, vslot[4] = {2, 0, 1, 2};
#define SYNC(j)                                                               \
  do {                                                                        \
    if ((j) < 3) waitv<8>(); else waitv<16>();                                \
    if ((j) == 1 && w == 3) waitv<0>();                                       \
    BAR(); STAMP(7 + (j));                                                    \
    if ((j) == 0) { V_ISSUE(1, 0); }                                          \
    if ((j) == 1) { if (w == 3) K_ISSUE(3, 4); V_ISSUE(2, 1); }               \
    if ((j) == 2) { WO_ISSUE2(0, 3); }                                        \
  } while (0)
#pragma unroll
    for (int j = 0; j < 4; ++j) {
      f32x4 sacc[2][3];
      {
        unsigned kso = (unsigned)(kslot[j] * L_SLOT);
        asm volatile("" : "+s"(kso));
        bf16x8 kf[2][4];
#pragma unroll
        for (int s = 0; s < 4; ++s)
#pragma unroll
          for (int kt = 0; kt < 2; ++kt) kf[kt][s] = lds_rd128(ka + kso + kt * 4096 + ((((4 * s) | g) ^ c) << 4));
        SCHED();
#pragma unroll
        for (int s = 0; s < 4; ++s)
#pragma unroll
          for (int kt = 0; kt < 2; ++kt)
#pragma unroll
            for (int qt = 0; qt < 3; ++qt)
              sacc[kt][qt] = mfma(kf[kt][s], qf[s][qt], s == 0 ? (j == 0 ? f32x4{0.f, 0.f, 0.f, 0.f} : negm[qt]) : sacc[kt][qt]);
        SCHED();
        if (j == 0) K_ISSUE(2, 3);
      }
      bool resc = false;
      float al[3];
      if (j == 0) {
#pragma unroll
        for (int qt = 0; qt < 3; ++qt) {
          const float m = xmax_g(max8(sacc[0][qt], sacc[1][qt]));
          if (g == 0) *(LDS_AS float*)(sm + L_ST1 + ((w * 3 + qt) * 16 + c) * 4) = m;
        }
        SYNC(0);
#pragma unroll
        for (int qt = 0; qt < 3; ++qt) {
          float m = *(LDS_AS float*)(sm + L_ST1 + ((h * 12 + qt) * 16 + c) * 4);
#pragma unroll
          for (int k = 1; k < 4; ++k) m = fmaxf(m, *(LDS_AS float*)(sm + L_ST1 + ((h * 12 + 3 * k + qt) * 16 + c) * 4));
          mref[qt] = m;
          negm[qt] = f32x4{-m, -m, -m, -m};
          sacc[0][qt] += negm[qt];
          sacc[1][qt] += negm[qt];
        }
      } else {
        if (h == 0) SYNC(j);
        if (j == 3) {
          auto rso = __builtin_amdgcn_make_buffer_rsrc((void*)(wo + (size_t)n0 * DM), 0, 256 * DM * 2, 0x00020000);
#pragma unroll
          for (int u = 0; u < 3; ++u)
#pragma unroll
            for (int i = 0; i < 4; ++i)
              wreg[u][i] = __builtin_amdgcn_raw_buffer_load_b128(rso, (l >> 3) * 1024 + (l & 7) * 16, w * 128 + ((u + 2 + b) & 7) * 32768 + i * 8192, 0);
        }
        float t = max3(sacc[0][0][0], sacc[0][0][1], sacc[0][0][2]);
        t = max3(t, sacc[0][0][3], sacc[1][0][0]); t = max3(t, sacc[1][0][1], sacc[1][0][2]);
        t = max3(t, sacc[1][0][3], sacc[0][1][0]); t = max3(t, sacc[0][1][1], sacc[0][1][2]);
        t = max3(t, sacc[0][1][3], sacc[1][1][0]); t = max3(t, sacc[1][1][1], sacc[1][1][2]);
        t = max3(t, sacc[1][1][3], sacc[0][2][0]); t = max3(t, sacc[0][2][1], sacc[0][2][2]);
        t = max3(t, sacc[0][2][3], sacc[1][2][0]); t = max3(t, sacc[1][2][1], sacc[1][2][2]);
        t = max3(t, sacc[1][2][3], t);
        resc = __any(!(t <= RESC_T));
        if (__builtin_expect(resc, 0)) {
#pragma unroll
          for (int qt = 0; qt < 3; ++qt) {
            const float d = fmaxf(xmax_g(max8(sacc[0][qt], sacc[1][qt])), 0.f);
            al[qt] = __builtin_amdgcn_exp2f(-d);
            mref[qt] += d;
            negm[qt] -= d;
            sacc[0][qt] -= d;
            sacc[1][qt] -= d;
          }
        }
      }
      bf16x8 pf[3];
#pragma unroll
      for (int qt = 0; qt < 3; ++qt) {
#pragma unroll
        for (int kt = 0; kt < 2; ++kt)
#pragma unroll
          for (int r = 0; r < 4; ++r) sacc[kt][qt][r] = __builtin_amdgcn_exp2f(sacc[kt][qt][r]);
        pf[qt] = cvt8(sacc[0][qt], sacc[1][qt]);
        asm volatile("" : "+v"(pf[qt]));
      }
      SCHED();
      if (j > 0) {
        if (h == 1) SYNC(j);
        if (__builtin_expect(resc, 0)) {
#pragma unroll
          for (int qt = 0; qt < 3; ++qt) {
            lacc[qt] *= al[qt];
#pragma unroll
            for (int i = 0; i < 8; ++i) oacc[i][qt] *= al[qt];
          }
        }
      }
      {
        unsigned vso = (unsigned)(vslot[j] * L_SLOT);
        asm volatile("" : "+s"(vso));
        bf16x8 vf[8];
#pragma unroll
        for (int i = 0; i < 8; ++i) {
          ldsp p = (ldsp)((va[i >> 1] ^ (unsigned)((i & 1) << 5)) + vso);
          vf[i] = lds_tr2(p, p + 4096);
        }
        SCHED();
#pragma unroll
        for (int qt = 0; qt < 3; ++qt) lacc[qt] = mfma(ones, pf[qt], j == 0 ? f32x4{0.f, 0.f, 0.f, 0.f} : lacc[qt]);
#pragma unroll
        for (int i = 0; i < 8; ++i)
#pragma unroll
          for (int qt = 0; qt < 3; ++qt) oacc[i][qt] = mfma(vf[i], pf[qt], j == 0 ? f32x4{0.f, 0.f, 0.f, 0.f} : oacc[i][qt]);
        SCHED();
        if (j == 0 && w != 3) K_ISSUE(3, 4);
        if (j == 1) V_ISSUE(3, 2);
        if (j == 2) WO_ISSUE2(1, 0);
      }
    }
    if (g == 0) {
#pragma unroll
      for (int qt = 0; qt < 3; ++qt) *(LDS_AS f32x2*)(sm + L_ST2 + ((w * 3 + qt) * 16 + c) * 8) = f32x2{mref[qt], lacc[qt][0]};
    }
    BAR(); STAMP(11);
  }
  int tz = threadIdx.x;
  asm volatile("" : "+v"(tz));
  const int l = tz & 63, c = l & 15, g = l >> 4, qq = c >> 2, pp = l & 3;
  {
    float inv[3], sc[3];
    bool differ = false;
#pragma unroll
    for (int qt = 0; qt < 3; ++qt) {
      f32x2 ml[4];
#pragma unroll
      for (int k = 0; k < 4; ++k) ml[k] = *(LDS_AS f32x2*)(sm + L_ST2 + ((h * 12 + 3 * k + qt) * 16 + c) * 8);
      const float m = fmaxf(fmaxf(ml[0][0], ml[1][0]), fmaxf(ml[2][0], ml[3][0]));
      float lt = 0.f;
#pragma unroll
      for (int k = 0; k < 4; ++k) lt += ml[k][1] * __builtin_amdgcn_exp2f(ml[k][0] - m);
      inv[qt] = __builtin_amdgcn_rcpf(lt);
      sc[qt] = __builtin_amdgcn_exp2f(mref[qt] - m);
      differ = differ || (mref[qt] != m);
    }
    if (__builtin_expect(__any(differ), 0)) {
#pragma unroll
      for (int qt = 0; qt < 3; ++qt)
#pragma unroll
        for (int i = 0; i < 8; ++i) oacc[i][qt] *= sc[qt];
    }
#pragma unroll
    for (int r = 1; r < 4; ++r) {
      const int wd = h * 4 + ((kq + r) & 3);
#pragma unroll
      for (int qt = 0; qt < 3; ++qt) *(LDS_AS bf16x8*)(sm + xoff(wd, r - 1, qt) + l * 16) = cvt8(oacc[2 * r][qt], oacc[2 * r + 1][qt]);
    }
    bf16x8 sel0, sel1;
#pragma unroll
    for (int e = 0; e < 4; ++e) {
      const __bf16 v = (4 * g + e == c) ? (__bf16)1.0f : (__bf16)0.0f;
      sel0[e] = v; sel0[4 + e] = (__bf16)0.0f; sel1[e] = (__bf16)0.0f; sel1[4 + e] = v;
    }
    BAR(); STAMP(17);
#pragma unroll
    for (int qt = 0; qt < 3; ++qt) {
#pragma unroll
      for (int r = 0; r < 3; ++r) {
        bf16x8 t = lds_rd128(sm + xoff(w, r, qt) + l * 16);
        oacc[0][qt] = mfma(sel0, t, oacc[0][qt]);
        oacc[1][qt] = mfma(sel1, t, oacc[1][qt]);
      }
    }
#pragma unroll
    for (int qt = 0; qt < 3; ++qt) *(LDS_AS bf16x8*)(sm + L_O2 + (w * 3 + qt) * 1024 + l * 16) = cvt8(oacc[0][qt] * inv[qt], oacc[1][qt] * inv[qt]);
  }
#pragma unroll
  for (int u = 0; u < 3; ++u) asm volatile("" : "+v"(wreg[u][0]), "+v"(wreg[u][1]), "+v"(wreg[u][2]), "+v"(wreg[u][3]));
  waitv<0>();
  BAR(); STAMP(18);
  const unsigned ov0 = (unsigned)((l >> 3) * 1024 + (((l & 7) ^ (g << 1)) << 4));
  WO_ISSUE(5, 1); WO_ISSUE(6, 4);

  f32x4 cacc[4][3];
  {
    bf16x8 of[2][3];
    const int fC = 2 * (g & 1) + (qq >> 1);
    ldsp wra = sm + w * 4096 + (4 * g + qq) * 128 + 8 * pp;
    bf16x8 wf[2][4];
#define CSLOT(u) ((u) == 5 ? 1 : ((u) == 6 ? 4 : (((u) & 1) ? 0 : 3)))
#define C_READ(u)                                                                                                   \
  do {                                                                                                              \
    _Pragma("unroll") for (int t_ = 0; t_ < 4; ++t_) {                                                              \
      ldsp p_ = wra + CSLOT(u) * L_SLOT + ((t_ ^ fC) << 5);                                                         \
      wf[(u) & 1][t_] = lds_tr2(p_, p_ + 2048);                                                                     \
    }                                                                                                               \
    _Pragma("unroll") for (int q_ = 0; q_ < 3; ++q_)                                                                \
        of[(u) & 1][q_] = lds_rd128(sm + L_O2 + ((((u) + b) & 7) * 3 + q_) * 1024 + l * 16);                        \
  } while (0)
    ldsp wst = sm + w * 4096 + (l >> 3) * 128 + (((l & 7) ^ (g << 1)) << 4);
    C_READ(0);
#pragma unroll
    for (int u = 0; u < 8; ++u) {
      if (u < 3) {
#pragma unroll
        for (int i = 0; i < 4; ++i) *(LDS_AS u32x4*)(wst + CSLOT(u + 2) * L_SLOT + i * 1024) = wreg[u][i];
      }
      if (u == 4) WO_ISSUE(7, 0);
      if (u + 1 < 8) {
        if (u + 1 == 5) waitv<8>(); else if (u + 1 == 6) waitv<4>(); else if (u + 1 == 7) waitv<0>();
        C_READ(u + 1);
      }
      SCHED();
#pragma unroll
      for (int t = 0; t < 4; ++t)
#pragma unroll
        for (int qt = 0; qt < 3; ++qt) cacc[t][qt] = mfma(wf[u & 1][t], of[u & 1][qt], u == 0 ? f32x4{0.f, 0.f, 0.f, 0.f} : cacc[t][qt]);
      SCHED();
    }
  }
  STAMP(19);
  const int s16 = kvh * 2 + hp;
  {
    auto rs = __builtin_amdgcn_make_buffer_rsrc((void*)(ws + WS_PART + ((size_t)(b * 16 + s16) * 8 + w) * 6144), 0, 6144, 0x00020000);
#pragma unroll
    for (int a = 0; a < 2; ++a)
#pragma unroll
      for (int qt = 0; qt < 3; ++qt)
        __builtin_amdgcn_raw_buffer_store_b128(__builtin_bit_cast(u32x4, cvt8(cacc[2 * a][qt], cacc[2 * a + 1][qt])), rs, l * 16 + (a * 3 + qt) * 1024, 0, 16);
  }
  asm volatile("s_waitcnt vmcnt(0)" ::: "memory");
  if (l == 0) __hip_atomic_store(flags + (w * 16 + s16) * 8, tag, __ATOMIC_RELAXED, __HIP_MEMORY_SCOPE_AGENT);
  if (w < 3) {
    for (unsigned spins = 0; spins < (1u << 22); ++spins) {
      const unsigned long long f = __hip_atomic_load(flags + ((s16 >> 1) * 16 + (l & 15)) * 8, __ATOMIC_RELAXED, __HIP_MEMORY_SCOPE_AGENT);
      if (__all(f == tag)) break;
      __builtin_amdgcn_s_sleep(1);
    }
    __builtin_amdgcn_fence(__ATOMIC_ACQUIRE, "wavefront");
    if (s16 == 0 && w == 0 && l == 0) __hip_atomic_store(epoch, tag, __ATOMIC_RELAXED, __HIP_MEMORY_SCOPE_AGENT);
    STAMP(21);
    const int rem = s16 * 192 + w * 64 + l;
    auto rs = __builtin_amdgcn_make_buffer_rsrc((void*)(ws + WS_PART + (size_t)b * 16 * 49152), 0, 16 * 49152, 0x00020000);
    u32x4 v[16];
#pragma unroll
    for (int sl = 0; sl < 16; ++sl) v[sl] = __builtin_amdgcn_raw_buffer_load_b128(rs, rem * 16, sl * 49152, 16);
    f32x4 a0 = {0.f, 0.f, 0.f, 0.f}, a1 = {0.f, 0.f, 0.f, 0.f};
#pragma unroll
    for (int sl = 0; sl < 16; ++sl) {
#pragma unroll
      for (int e = 0; e < 2; ++e) {
        a0[2 * e] += __uint_as_float(v[sl][e] << 16); a0[2 * e + 1] += __uint_as_float(v[sl][e] & 0xffff0000u);
        a1[2 * e] += __uint_as_float(v[sl][2 + e] << 16); a1[2 * e + 1] += __uint_as_float(v[sl][2 + e] & 0xffff0000u);
      }
    }
    const int tq = rem >> 6, qt = tq % 3, wa = tq / 3;
    const int q = 16 * qt + c, n = 32 * wa + 4 * g;
    __bf16* o = out + ((size_t)b * SQ + q) * DM + n;
    *(bf16x4*)o = cvt4(a0);
    *(bf16x4*)(o + 16) = cvt4(a1);
  }
}

extern "C" void kernel_launch(void* const* d_in, const int* in_sizes, int n_in, void* d_out, int out_size, void* d_ws, size_t ws_size,
                              hipStream_t stream) {
  const __bf16* x = (const __bf16*)d_in[0];
  const __bf16* kc = (const __bf16*)d_in[1];
  const __bf16* vc = (const __bf16*)d_in[2];
  const __bf16* wq = (const __bf16*)d_in[3];
  const __bf16* wo = (const __bf16*)d_in[4];
  (void)hipFuncSetAttribute((const void*)gqa_fused, hipFuncAttributeMaxDynamicSharedMemorySize, LDS_BYTES);
  hipLaunchKernelGGL(gqa_fused, dim3(256), dim3(512), LDS_BYTES, stream, x, kc, vc, wq, wo, (char*)d_ws, (__bf16*)d_out);
}
```

```cpp
#include <hip/hip_runtime.h>
#include <hip/hip_bf16.h>
#include <stdint.h>

typedef __bf16 bf16x8 __attribute__((ext_vector_type(8)));
typedef __bf16 bf16x4 __attribute__((ext_vector_type(4)));
typedef float f32x4 __attribute__((ext_vector_type(4)));
typedef float f32x2 __attribute__((ext_vector_type(2)));
typedef int i32x4 __attribute__((ext_vector_type(4)));
typedef unsigned u32x4 __attribute__((ext_vector_type(4)));
#define LDS_AS __attribute__((address_space(3)))
typedef LDS_AS char* ldsp;

constexpr int B_ = 16, SQ = 48, DM = 512, HQ = 32, HKV = 8, SK = 512, DH = 128;
constexpr float SCL2E = 0.08838834764831845f * 1.4426950408889634f;

constexpr int L_SLOT = 32768;
constexpr int L_RING = 32768;
constexpr int L_X = 114688;
constexpr int L_Q = 131072;
constexpr int L_ST1 = 155648;
constexpr int L_ST2 = 32768 + 24576;
constexpr int L_O2 = 2 * 32768;
constexpr int LDS_BYTES = 163840;
constexpr int WS_EPOCH = 0, WS_FLAGS = 4096, WS_PART = 4096 + 16 * 128 * 64;
constexpr int NRING = 5;
#define RESC_T 16.0f

#define PRIO 0
#define BAR() asm volatile("s_waitcnt lgkmcnt(0)\n\ts_barrier" ::: "memory")
#define SCHED() __builtin_amdgcn_sched_barrier(0)
#define STAMP(i)
template <int N> __device__ __forceinline__ void waitv() { asm volatile("s_waitcnt vmcnt(%0)" ::"i"(N) : "memory"); }

__device__ __forceinline__ void dma4(unsigned lds, unsigned v0, unsigned v1, unsigned v2, unsigned v3, i32x4 srd, unsigned soff) {
  asm volatile("s_mov_b32 m0, %0\n\ts_nop 0\n\t"
               "buffer_load_dwordx4 %1, %5, %6 offen lds\n\t"
               "buffer_load_dwordx4 %2, %5, %6 offen offset:1024 lds\n\t"
               "buffer_load_dwordx4 %3, %5, %6 offen offset:2048 lds\n\t"
               "buffer_load_dwordx4 %4, %5, %6 offen offset:3072 lds"
               ::"s"(lds), "v"(v0), "v"(v1), "v"(v2), "v"(v3), "s"(srd), "s"(soff) : "memory");
}
__device__ __forceinline__ void dma4nt(unsigned lds, unsigned v0, unsigned v1, unsigned v2, unsigned v3, i32x4 srd, unsigned soff) {
  asm volatile("s_mov_b32 m0, %0\n\ts_nop 0\n\t"
               "buffer_load_dwordx4 %1, %5, %6 offen nt lds\n\t"
               "buffer_load_dwordx4 %2, %5, %6 offen offset:1024 nt lds\n\t"
               "buffer_load_dwordx4 %3, %5, %6 offen offset:2048 nt lds\n\t"
               "buffer_load_dwordx4 %4, %5, %6 offen offset:3072 nt lds"
               ::"s"(lds), "v"(v0), "v"(v1), "v"(v2), "v"(v3), "s"(srd), "s"(soff) : "memory");
}
__device__ __forceinline__ i32x4 make_srd(const void* p, unsigned bytes) {
  unsigned long long a = (unsigned long long)p;
  i32x4 s;
  s[0] = (int)(unsigned)a; s[1] = (int)((unsigned)(a >> 32) & 0xffffu); s[2] = (int)bytes; s[3] = 0x00020000;
  return s;
}
__device__ __forceinline__ bf16x8 lds_rd128(ldsp p) { return *(LDS_AS bf16x8*)p; }
__device__ __forceinline__ bf16x8 lds_tr2(ldsp p0, ldsp p1) {
  bf16x4 a = __builtin_amdgcn_ds_read_tr16_b64_v4bf16((LDS_AS bf16x4*)p0);
  bf16x4 b = __builtin_amdgcn_ds_read_tr16_b64_v4bf16((LDS_AS bf16x4*)p1);
  return __builtin_shufflevector(a, b, 0, 1, 2, 3, 4, 5, 6, 7);
}
__device__ __forceinline__ f32x4 mfma(bf16x8 a, bf16x8 b, f32x4 c) { return __builtin_amdgcn_mfma_f32_16x16x32_bf16(a, b, c, 0, 0, 0); }
__device__ __forceinline__ bf16x4 cvt4(f32x4 v) {
  bf16x4 r; r[0] = (__bf16)v[0]; r[1] = (__bf16)v[1]; r[2] = (__bf16)v[2]; r[3] = (__bf16)v[3]; return r;
}
__device__ __forceinline__ bf16x8 cvt8(f32x4 a, f32x4 b) { return __builtin_shufflevector(cvt4(a), cvt4(b), 0, 1, 2, 3, 4, 5, 6, 7); }
__device__ __forceinline__ float max3(float a, float b, float c) { return __builtin_elementwise_maximum(__builtin_elementwise_maximum(a, b), c); }
__device__ __forceinline__ float max8(f32x4 a, f32x4 b) {
  return max3(max3(max3(a[0], a[1], a[2]), a[3], b[0]), max3(b[1], b[2], b[3]), b[3]);
}
__device__ __forceinline__ float xmax_g(float v) {
  auto r = __builtin_amdgcn_permlane16_swap(__float_as_uint(v), __float_as_uint(v), false, false);
  v = fmaxf(__uint_as_float(r[0]), __uint_as_float(r[1]));
  auto r2 = __builtin_amdgcn_permlane32_swap(__float_as_uint(v), __float_as_uint(v), false, false);
  return fmaxf(__uint_as_float(r2[0]), __uint_as_float(r2[1]));
}
__device__ __forceinline__ int xoff(int wd, int r, int qt) { return (r == 0 ? L_O2 : (r == 1 ? L_SLOT : 4 * L_SLOT)) + (wd * 3 + qt) * 1024; }

__global__ __launch_bounds__(512, 2) void gqa_fused(const __bf16* __restrict__ x, const __bf16* __restrict__ kc, const __bf16* __restrict__ vc,
                                                    const __bf16* __restrict__ wq, const __bf16* __restrict__ wo, char* ws, __bf16* __restrict__ out) {
  extern __shared__ __attribute__((aligned(16))) char smem_raw[];
  ldsp sm = (ldsp)smem_raw;
  const unsigned smbase = (unsigned)(uintptr_t)sm;
  const int w = __builtin_amdgcn_readfirstlane(threadIdx.x >> 6);
  const int bid = blockIdx.x, kvh = bid & 7, jj = bid >> 3, b = jj >> 1, hp = jj & 1;
  STAMP(0);
  const int n0 = kvh * 512 + hp * 256;
  const i32x4 srd_k = make_srd(kc + (size_t)(b * HKV + kvh) * SK * DH, SK * DH * 2);
  const i32x4 srd_v = make_srd(vc + (size_t)(b * HKV + kvh) * SK * DH, SK * DH * 2);
  const i32x4 srd_o = make_srd(wo + (size_t)n0 * DM, 256 * DM * 2);
  const int h = w >> 2, kq = w & 3;
  typedef __attribute__((address_space(1))) unsigned long long gu64;
  gu64* epoch = (gu64*)(ws + WS_EPOCH + b * 256);
  gu64* flags = (gu64*)(ws + WS_FLAGS + b * 128 * 64);
  const unsigned long long tag = __hip_atomic_load(epoch, __ATOMIC_RELAXED, __HIP_MEMORY_SCOPE_AGENT) + 1ull;
  f32x4 oacc[8][3];
  f32x4 lacc[3];
  float mref[3];
  {
    const int l = threadIdx.x & 63, c = l & 15, g = l >> 4, qq = c >> 2, pp = l & 3;
    const unsigned kv0 = (unsigned)(g * 256 + ((c ^ g) << 4));
    const unsigned vv0 = (unsigned)(g * 256 + ((c ^ (2 * g)) << 4));
    if (w < 4) {
      f32x4 qacc[4][3];
      const i32x4 srd_x = make_srd(x + (size_t)b * SQ * DM, SQ * DM * 2);
      const i32x4 srd_w = make_srd(wq, DM * HQ * DH * 2);
#pragma unroll
      for (int i = 0; i < 3; ++i) {
        const int r = w * 12 + 4 * i;
        dma4(smbase + L_X + r * 1024, (unsigned)((l ^ (r & 15)) * 16), (unsigned)((l ^ ((r + 1) & 15)) * 16), (unsigned)((l ^ ((r + 2) & 15)) * 16),
             (unsigned)((l ^ ((r + 3) & 15)) * 16), srd_x, (unsigned)(r * 1024));
      }
      const unsigned ring = smbase + L_RING + w * (NRING * 4096);
      const unsigned wv0 = (unsigned)((l >> 3) * 8192 + (((l & 7) ^ (((l >> 4) & 1) << 1)) * 16));
      const unsigned wv1 = (wv0 ^ 64u) + (65536u - 1024u), wv2 = wv0 + 2u * (65536u - 1024u), wv3 = (wv0 ^ 64u) + 3u * (65536u - 1024u);
      const unsigned scol = (unsigned)((n0 + 64 * w) * 2);
#define WQ_ISSUE(s) dma4(ring + ((s) % NRING) * 4096, wv0, wv1, wv2, wv3, srd_w, scol + (unsigned)((((s) + b) & 15) * (32 * 8192)))
      WQ_ISSUE(0); WQ_ISSUE(1); WQ_ISSUE(2); WQ_ISSUE(3);
      waitv<16>();
      STAMP(1); BAR(); STAMP(2);
      const int fA = ((qq >> 1) & 1) | ((g & 1) << 1);
      ldsp wqa = sm + L_RING + w * (NRING * 4096) + (8 * g + qq) * 128 + 8 * pp;
      ldsp xa = sm + L_X + c * 1024;
      bf16x8 fw[2][4], fx[2][3];
#define A_READ(s, buf)                                                                                          \
  do {                                                                                                          \
    _Pragma("unroll") for (int t_ = 0; t_ < 4; ++t_) {                                                          \
      ldsp p_ = wqa + ((s) % NRING) * 4096 + ((t_ ^ fA) << 5);                                                  \
      fw[buf][t_] = lds_tr2(p_, p_ + 512);                                                                      \
    }                                                                                                           \
    _Pragma("unroll") for (int q_ = 0; q_ < 3; ++q_)                                                            \
        fx[buf][q_] = lds_rd128(xa + q_ * 16384 + ((((4 * (((s) + b) & 15)) | g) ^ c) << 4));                   \
  } while (0)
      waitv<12>();
      A_READ(0, 0);
#pragma unroll
      for (int s = 0; s < 16; ++s) {
        if (s + 4 < 16) WQ_ISSUE(s + 4);
        if (s == 12) {
          dma4nt(smbase + (32 * w) * 256, kv0, kv0 ^ 64u, kv0 ^ 128u, kv0 ^ 192u, srd_k, (unsigned)(128 * w * 256));
          dma4nt(smbase + (32 * w + 16) * 256, kv0, kv0 ^ 64u, kv0 ^ 128u, kv0 ^ 192u, srd_k, (unsigned)((128 * w + 16) * 256));
        }
        if (s + 1 < 16) {
          const int rem = ((s + 4 < 16) ? (s + 4) : 15) - (s + 1);
          if (s < 12) waitv<12>(); else if (rem == 2) waitv<16>(); else if (rem == 1) waitv<12>(); else waitv<8>();
          A_READ(s + 1, (s + 1) & 1);
        }
        SCHED();
#pragma unroll
        for (int t = 0; t < 4; ++t)
#pragma unroll
          for (int qt = 0; qt < 3; ++qt) qacc[t][qt] = mfma(fw[s & 1][t], fx[s & 1][qt], s == 0 ? f32x4{0.f, 0.f, 0.f, 0.f} : qacc[t][qt]);
        SCHED();
      }
      STAMP(3); BAR(); STAMP(4);
#pragma unroll
      for (int t = 0; t < 4; ++t)
#pragma unroll
        for (int qt = 0; qt < 3; ++qt) {
          const int c16 = 8 * (w & 1) + 2 * t + (g >> 1);
          *(LDS_AS bf16x4*)(sm + L_Q + (48 * (w >> 1) + 16 * qt + c) * 256 + ((c16 ^ c) << 4) + 8 * (g & 1)) = cvt4(qacc[t][qt] * SCL2E);
        }
    } else {
      STAMP(1); BAR(); STAMP(2);
      STAMP(3); BAR(); STAMP(4);
    }
    const unsigned kvrow = (unsigned)(128 * kq * 256);
#define K_ISSUE(j, slot)                                                                                                                  \
  do { if (h == 0) {                                                                                                                      \
    dma4nt(smbase + (slot) * L_SLOT + kq * 8192, kv0, kv0 ^ 64u, kv0 ^ 128u, kv0 ^ 192u, srd_k, kvrow + (unsigned)((j) * 8192));          \
    dma4nt(smbase + (slot) * L_SLOT + kq * 8192 + 4096, kv0, kv0 ^ 64u, kv0 ^ 128u, kv0 ^ 192u, srd_k, kvrow + (unsigned)((j) * 8192 + 4096)); \
  } } while (0)
#define V_ISSUE(j, slot)                                                                                                                  \
  do { if (h == 0) {                                                                                                                      \
    dma4nt(smbase + (slot) * L_SLOT + kq * 8192, vv0, vv0 ^ 128u, vv0, vv0 ^ 128u, srd_v, kvrow + (unsigned)((j) * 8192));                \
    dma4nt(smbase + (slot) * L_SLOT + kq * 8192 + 4096, vv0, vv0 ^ 128u, vv0, vv0 ^ 128u, srd_v, kvrow + (unsigned)((j) * 8192 + 4096));  \
  } } while (0)
    const unsigned ov0 = (unsigned)((l >> 3) * 1024 + (((l & 7) ^ (g << 1)) << 4));
#define WO_ISSUE_W(u, slot, ww) dma4(smbase + (slot) * L_SLOT + (ww) * 4096, ov0, ov0 + 7168u, ov0 + 14336u, ov0 + 21504u, srd_o, (unsigned)((ww) * 128 + (((u) + b) & 7) * 32768))
#define WO_ISSUE(u, slot) WO_ISSUE_W(u, slot, w)
#define WO_ISSUE2(u, slot) do { if (h == 0) { WO_ISSUE_W(u, slot, w); WO_ISSUE_W(u, slot, w + 4); } } while (0)
    K_ISSUE(1, 1);
    waitv<8>();
    STAMP(5); BAR(); STAMP(6);

    bf16x8 qf[4][3];
#pragma unroll
    for (int s = 0; s < 4; ++s)
#pragma unroll
      for (int qt = 0; qt < 3; ++qt) qf[s][qt] = lds_rd128(sm + L_Q + (48 * h + 16 * qt + c) * 256 + ((((4 * s) | g) ^ c) << 4));
    V_ISSUE(0, 2);
    ldsp ka = sm + (32 * kq + c) * 256;
    const int fV = 4 * (g & 1) + qq;
    unsigned va[4];
#pragma unroll
    for (int r = 0; r < 4; ++r) va[r] = smbase + (32 * kq + 4 * g + qq) * 256 + 8 * pp + (((2 * ((r + kq) & 3)) ^ fV) << 5);
    bf16x8 ones;
#pragma unroll
    for (int e = 0; e < 8; ++e) ones[e] = (__bf16)1.0f;
    f32x4 negm[3];
    constexpr int kslot[4] = {0, 1, 3, 4}, vslot[4] = {2, 0, 1, 2};
#define SYNC(j)                                                               \
  do {                                                                        \
    if ((j) < 3) waitv<8>(); else waitv<16>();                                \
    if ((j) == 1 && w == 3) waitv<0>();                                       \
    BAR(); STAMP(7 + (j));                                                    \
    if ((j) == 0) { V_ISSUE(1, 0); }                                          \
    if ((j) == 1) { if (w == 3) K_ISSUE(3, 4); V_ISSUE(2, 1); }               \
    if ((j) == 2) { WO_ISSUE2(0, 3); }                                        \
  } while (0)
#pragma unroll
    for (int j = 0; j < 4; ++j) {
      f32x4 sacc[2][3];
      {
        unsigned kso = (unsigned)(kslot[j] * L_SLOT);
        asm volatile("" : "+s"(kso));
        bf16x8 kf[2][4];
#pragma unroll
        for (int s = 0; s < 4; ++s)
#pragma unroll
          for (int kt = 0; kt < 2; ++kt) kf[kt][s] = lds_rd128(ka + kso + kt * 4096 + ((((4 * s) | g) ^ c) << 4));
        SCHED();
#pragma unroll
        for (int s = 0; s < 4; ++s)
#pragma unroll
          for (int kt = 0; kt < 2; ++kt)
#pragma unroll
            for (int qt = 0; qt < 3; ++qt)
              sacc[kt][qt] = mfma(kf[kt][s], qf[s][qt], s == 0 ? (j == 0 ? f32x4{0.f, 0.f, 0.f, 0.f} : negm[qt]) : sacc[kt][qt]);
        SCHED();
        if (j == 0) K_ISSUE(2, 3);
      }
      bool resc = false;
      float al[3];
      if (j == 0) {
#pragma unroll
        for (int qt = 0; qt < 3; ++qt) {
          const float m = xmax_g(max8(sacc[0][qt], sacc[1][qt]));
          if (g == 0) *(LDS_AS float*)(sm + L_ST1 + ((w * 3 + qt) * 16 + c) * 4) = m;
        }
        SYNC(0);
#pragma unroll
        for (int qt = 0; qt < 3; ++qt) {
          float m = *(LDS_AS float*)(sm + L_ST1 + ((h * 12 + qt) * 16 + c) * 4);
#pragma unroll
          for (int k = 1; k < 4; ++k) m = fmaxf(m, *(LDS_AS float*)(sm + L_ST1 + ((h * 12 + 3 * k + qt) * 16 + c) * 4));
          mref[qt] = m;
          negm[qt] = f32x4{-m, -m, -m, -m};
          sacc[0][qt] += negm[qt];
          sacc[1][qt] += negm[qt];
        }
      } else {
        if (h == 0) SYNC(j);
        float t = max3(sacc[0][0][0], sacc[0][0][1], sacc[0][0][2]);
        t = max3(t, sacc[0][0][3], sacc[1][0][0]); t = max3(t, sacc[1][0][1], sacc[1][0][2]);
        t = max3(t, sacc[1][0][3], sacc[0][1][0]); t = max3(t, sacc[0][1][1], sacc[0][1][2]);
        t = max3(t, sacc[0][1][3], sacc[1][1][0]); t = max3(t, sacc[1][1][1], sacc[1][1][2]);
        t = max3(t, sacc[1][1][3], sacc[0][2][0]); t = max3(t, sacc[0][2][1], sacc[0][2][2]);
        t = max3(t, sacc[0][2][3], sacc[1][2][0]); t = max3(t, sacc[1][2][1], sacc[1][2][2]);
        t = max3(t, sacc[1][2][3], t);
        resc = __any(!(t <= RESC_T));
        if (__builtin_expect(resc, 0)) {
#pragma unroll
          for (int qt = 0; qt < 3; ++qt) {
            const float d = fmaxf(xmax_g(max8(sacc[0][qt], sacc[1][qt])), 0.f);
            al[qt] = __builtin_amdgcn_exp2f(-d);
            mref[qt] += d;
            negm[qt] -= d;
            sacc[0][qt] -= d;
            sacc[1][qt] -= d;
          }
        }
      }
      bf16x8 pf[3];
#pragma unroll
      for (int qt = 0; qt < 3; ++qt) {
#pragma unroll
        for (int kt = 0; kt < 2; ++kt)
#pragma unroll
          for (int r = 0; r < 4; ++r) sacc[kt][qt][r] = __builtin_amdgcn_exp2f(sacc[kt][qt][r]);
        pf[qt] = cvt8(sacc[0][qt], sacc[1][qt]);
        asm volatile("" : "+v"(pf[qt]));
      }
      SCHED();
      if (j > 0) {
        if (h == 1) SYNC(j);
        if (__builtin_expect(resc, 0)) {
#pragma unroll
          for (int qt = 0; qt < 3; ++qt) {
            lacc[qt] *= al[qt];
#pragma unroll
            for (int i = 0; i < 8; ++i) oacc[i][qt] *= al[qt];
          }
        }
      }
      {
        unsigned vso = (unsigned)(vslot[j] * L_SLOT);
        asm volatile("" : "+s"(vso));
        bf16x8 vf[8];
#pragma unroll
        for (int i = 0; i < 8; ++i) {
          ldsp p = (ldsp)((va[i >> 1] ^ (unsigned)((i & 1) << 5)) + vso);
          vf[i] = lds_tr2(p, p + 4096);
        }
        SCHED();
#pragma unroll
        for (int qt = 0; qt < 3; ++qt) lacc[qt] = mfma(ones, pf[qt], j == 0 ? f32x4{0.f, 0.f, 0.f, 0.f} : lacc[qt]);
#pragma unroll
        for (int i = 0; i < 8; ++i)
#pragma unroll
          for (int qt = 0; qt < 3; ++qt) oacc[i][qt] = mfma(vf[i], pf[qt], j == 0 ? f32x4{0.f, 0.f, 0.f, 0.f} : oacc[i][qt]);
        SCHED();
        if (j == 0 && w != 3) K_ISSUE(3, 4);
        if (j == 1) V_ISSUE(3, 2);
        if (j == 2) WO_ISSUE2(1, 0);
      }
    }
    if (g == 0) {
#pragma unroll
      for (int qt = 0; qt < 3; ++qt) *(LDS_AS f32x2*)(sm + L_ST2 + ((w * 3 + qt) * 16 + c) * 8) = f32x2{mref[qt], lacc[qt][0]};
    }
    BAR(); STAMP(11);
  }
  int tz = threadIdx.x;
  asm volatile("" : "+v"(tz));
  const int l = tz & 63, c = l & 15, g = l >> 4, qq = c >> 2, pp = l & 3;
  {
    float inv[3], sc[3];
    bool differ = false;
#pragma unroll
    for (int qt = 0; qt < 3; ++qt) {
      f32x2 ml[4];
#pragma unroll
      for (int k = 0; k < 4; ++k) ml[k] = *(LDS_AS f32x2*)(sm + L_ST2 + ((h * 12 + 3 * k + qt) * 16 + c) * 8);
      const float m = fmaxf(fmaxf(ml[0][0], ml[1][0]), fmaxf(ml[2][0], ml[3][0]));
      float lt = 0.f;
#pragma unroll
      for (int k = 0; k < 4; ++k) lt += ml[k][1] * __builtin_amdgcn_exp2f(ml[k][0] - m);
      inv[qt] = __builtin_amdgcn_rcpf(lt);
      sc[qt] = __builtin_amdgcn_exp2f(mref[qt] - m);
      differ = differ || (mref[qt] != m);
    }
    if (__builtin_expect(__any(differ), 0)) {
#pragma unroll
      for (int qt = 0; qt < 3; ++qt)
#pragma unroll
        for (int i = 0; i < 8; ++i) oacc[i][qt] *= sc[qt];
    }
#pragma unroll
    for (int r = 1; r < 4; ++r) {
      const int wd = h * 4 + ((kq + r) & 3);
#pragma unroll
      for (int qt = 0; qt < 3; ++qt) *(LDS_AS bf16x8*)(sm + xoff(wd, r - 1, qt) + l * 16) = cvt8(oacc[2 * r][qt], oacc[2 * r + 1][qt]);
    }
    bf16x8 sel0, sel1;
#pragma unroll
    for (int e = 0; e < 4; ++e) {
      const __bf16 v = (4 * g + e == c) ? (__bf16)1.0f : (__bf16)0.0f;
      sel0[e] = v; sel0[4 + e] = (__bf16)0.0f; sel1[e] = (__bf16)0.0f; sel1[4 + e] = v;
    }
    BAR(); STAMP(17);
#pragma unroll
    for (int qt = 0; qt < 3; ++qt) {
#pragma unroll
      for (int r = 0; r < 3; ++r) {
        bf16x8 t = lds_rd128(sm + xoff(w, r, qt) + l * 16);
        oacc[0][qt] = mfma(sel0, t, oacc[0][qt]);
        oacc[1][qt] = mfma(sel1, t, oacc[1][qt]);
      }
    }
#pragma unroll
    for (int qt = 0; qt < 3; ++qt) *(LDS_AS bf16x8*)(sm + L_O2 + (w * 3 + qt) * 1024 + l * 16) = cvt8(oacc[0][qt] * inv[qt], oacc[1][qt] * inv[qt]);
  }
  waitv<0>();
  BAR(); STAMP(18);
  const unsigned ov0 = (unsigned)((l >> 3) * 1024 + (((l & 7) ^ (g << 1)) << 4));

  f32x4 cacc[4][3];
  {
    bf16x8 of[2][3];
    const int fC = 2 * (g & 1) + (qq >> 1);
    ldsp wra = sm + w * 4096 + (4 * g + qq) * 128 + 8 * pp;
    bf16x8 wf[2][4];
#define CSLOT(u) (((u) & 3) == 0 ? 3 : (((u) & 3) == 1 ? 0 : (((u) & 3) == 2 ? 1 : 4)))
#define C_READ(u)                                                                                                   \
  do {                                                                                                              \
    _Pragma("unroll") for (int t_ = 0; t_ < 4; ++t_) {                                                              \
      ldsp p_ = wra + CSLOT(u) * L_SLOT + ((t_ ^ fC) << 5);                                                         \
      wf[(u) & 1][t_] = lds_tr2(p_, p_ + 2048);                                                                     \
    }                                                                                                               \
    _Pragma("unroll") for (int q_ = 0; q_ < 3; ++q_)                                                                \
        of[(u) & 1][q_] = lds_rd128(sm + L_O2 + ((((u) + b) & 7) * 3 + q_) * 1024 + l * 16);                        \
  } while (0)
    WO_ISSUE(2, 1);
    C_READ(0);
#pragma unroll
    for (int u = 0; u < 8; ++u) {
      if (u + 3 < 8) WO_ISSUE(u + 3, CSLOT(u + 3));
      if (u + 1 < 8) {
        if (u + 1 >= 2) { const int last = (u + 3 < 8) ? (u + 3) : 7; const int rem = last - (u + 1);
          if (rem == 2) waitv<8>(); else if (rem == 1) waitv<4>(); else waitv<0>(); }
        C_READ(u + 1);
      }
      SCHED();
#pragma unroll
      for (int t = 0; t < 4; ++t)
#pragma unroll
        for (int qt = 0; qt < 3; ++qt) cacc[t][qt] = mfma(wf[u & 1][t], of[u & 1][qt], u == 0 ? f32x4{0.f, 0.f, 0.f, 0.f} : cacc[t][qt]);
      SCHED();
    }
  }
  STAMP(19);
  const int s16 = kvh * 2 + hp;
  {
    auto rs = __builtin_amdgcn_make_buffer_rsrc((void*)(ws + WS_PART + ((size_t)(b * 16 + s16) * 8 + w) * 6144), 0, 6144, 0x00020000);
#pragma unroll
    for (int a = 0; a < 2; ++a)
#pragma unroll
      for (int qt = 0; qt < 3; ++qt)
        __builtin_amdgcn_raw_buffer_store_b128(__builtin_bit_cast(u32x4, cvt8(cacc[2 * a][qt], cacc[2 * a + 1][qt])), rs, l * 16 + (a * 3 + qt) * 1024, 0, 16);
  }
  asm volatile("s_waitcnt vmcnt(0)" ::: "memory");
  if (l == 0) __hip_atomic_store(flags + (w * 16 + s16) * 8, tag, __ATOMIC_RELAXED, __HIP_MEMORY_SCOPE_AGENT);
  if (w < 3) {
    for (unsigned spins = 0; spins < (1u << 22); ++spins) {
      const unsigned long long f = __hip_atomic_load(flags + ((s16 >> 1) * 16 + (l & 15)) * 8, __ATOMIC_RELAXED, __HIP_MEMORY_SCOPE_AGENT);
      if (__all(f == tag)) break;
      __builtin_amdgcn_s_sleep(1);
    }
    __builtin_amdgcn_fence(__ATOMIC_ACQUIRE, "wavefront");
    if (s16 == 0 && w == 0 && l == 0) __hip_atomic_store(epoch, tag, __ATOMIC_RELAXED, __HIP_MEMORY_SCOPE_AGENT);
    STAMP(21);
    const int rem = s16 * 192 + w * 64 + l;
    auto rs = __builtin_amdgcn_make_buffer_rsrc((void*)(ws + WS_PART + (size_t)b * 16 * 49152), 0, 16 * 49152, 0x00020000);
    u32x4 v[16];
#pragma unroll
    for (int sl = 0; sl < 16; ++sl) v[sl] = __builtin_amdgcn_raw_buffer_load_b128(rs, rem * 16, sl * 49152, 16);
    f32x4 a0 = {0.f, 0.f, 0.f, 0.f}, a1 = {0.f, 0.f, 0.f, 0.f};
#pragma unroll
    for (int sl = 0; sl < 16; ++sl) {
#pragma unroll
      for (int e = 0; e < 2; ++e) {
        a0[2 * e] += __uint_as_float(v[sl][e] << 16); a0[2 * e + 1] += __uint_as_float(v[sl][e] & 0xffff0000u);
        a1[2 * e] += __uint_as_float(v[sl][2 + e] << 16); a1[2 * e + 1] += __uint_as_float(v[sl][2 + e] & 0xffff0000u);
      }
    }
    const int tq = rem >> 6, qt = tq % 3, wa = tq / 3;
    const int q = 16 * qt + c, n = 32 * wa + 4 * g;
    __bf16* o = out + ((size_t)b * SQ + q) * DM + n;
    *(bf16x4*)o = cvt4(a0);
    *(bf16x4*)(o + 16) = cvt4(a1);
  }
}

extern "C" void kernel_launch(void* const* d_in, const int* in_sizes, int n_in, void* d_out, int out_size, void* d_ws, size_t ws_size,
                              hipStream_t stream) {
  const __bf16* x = (const __bf16*)d_in[0];
  const __bf16* kc = (const __bf16*)d_in[1];
  const __bf16* vc = (const __bf16*)d_in[2];
  const __bf16* wq = (const __bf16*)d_in[3];
  const __bf16* wo = (const __bf16*)d_in[4];
  (void)hipFuncSetAttribute((const void*)gqa_fused, hipFuncAttributeMaxDynamicSharedMemorySize, LDS_BYTES);
  hipLaunchKernelGGL(gqa_fused, dim3(256), dim3(512), LDS_BYTES, stream, x, kc, vc, wq, wo, (char*)d_ws, (__bf16*)d_out);
}
```

```cpp
#include <hip/hip_runtime.h>
#include <hip/hip_bf16.h>
#include <stdint.h>

typedef __bf16 bf16x8 __attribute__((ext_vector_type(8)));
typedef __bf16 bf16x4 __attribute__((ext_vector_type(4)));
typedef float f32x4 __attribute__((ext_vector_type(4)));
typedef float f32x2 __attribute__((ext_vector_type(2)));
typedef int i32x4 __attribute__((ext_vector_type(4)));
typedef unsigned u32x4 __attribute__((ext_vector_type(4)));
#define LDS_AS __attribute__((address_space(3)))
typedef LDS_AS char* ldsp;

constexpr int B_ = 16, SQ = 48, DM = 512, HQ = 32, HKV = 8, SK = 512, DH = 128;
constexpr float SCL2E = 0.08838834764831845f * 1.4426950408889634f;

constexpr int L_SLOT = 32768;
constexpr int L_RING = 32768;
constexpr int L_X = 114688;
constexpr int L_Q = 131072;
constexpr int L_ST1 = 155648;
constexpr int L_ST2 = 32768 + 24576;
constexpr int L_O2 = 2 * 32768;
constexpr int LDS_BYTES = 163840;
constexpr int WS_EPOCH = 0, WS_FLAGS = 4096, WS_PART = 4096 + 16 * 128 * 64;
constexpr int NRING = 5;
#define RESC_T 16.0f

#define PRIO 1
#define BAR() asm volatile("s_waitcnt lgkmcnt(0)\n\ts_barrier" ::: "memory")
#define SCHED() __builtin_amdgcn_sched_barrier(0)
#define STAMP(i)
template <int N> __device__ __forceinline__ void waitv() { asm volatile("s_waitcnt vmcnt(%0)" ::"i"(N) : "memory"); }

__device__ __forceinline__ void dma4(unsigned lds, unsigned v0, unsigned v1, unsigned v2, unsigned v3, i32x4 srd, unsigned soff) {
  asm volatile("s_mov_b32 m0, %0\n\ts_nop 0\n\t"
               "buffer_load_dwordx4 %1, %5, %6 offen lds\n\t"
               "buffer_load_dwordx4 %2, %5, %6 offen offset:1024 lds\n\t"
               "buffer_load_dwordx4 %3, %5, %6 offen offset:2048 lds\n\t"
               "buffer_load_dwordx4 %4, %5, %6 offen offset:3072 lds"
               ::"s"(lds), "v"(v0), "v"(v1), "v"(v2), "v"(v3), "s"(srd), "s"(soff) : "memory");
}
__device__ __forceinline__ void dma4nt(unsigned lds, unsigned v0, unsigned v1, unsigned v2, unsigned v3, i32x4 srd, unsigned soff) {
  asm volatile("s_mov_b32 m0, %0\n\ts_nop 0\n\t"
               "buffer_load_dwordx4 %1, %5, %6 offen sc0 nt lds\n\t"
               "buffer_load_dwordx4 %2, %5, %6 offen offset:1024 sc0 nt lds\n\t"
               "buffer_load_dwordx4 %3, %5, %6 offen offset:2048 sc0 nt lds\n\t"
               "buffer_load_dwordx4 %4, %5, %6 offen offset:3072 sc0 nt lds"
               ::"s"(lds), "v"(v0), "v"(v1), "v"(v2), "v"(v3), "s"(srd), "s"(soff) : "memory");
}
__device__ __forceinline__ i32x4 make_srd(const void* p, unsigned bytes) {
  unsigned long long a = (unsigned long long)p;
  i32x4 s;
  s[0] = (int)(unsigned)a; s[1] = (int)((unsigned)(a >> 32) & 0xffffu); s[2] = (int)bytes; s[3] = 0x00020000;
  return s;
}
__device__ __forceinline__ bf16x8 lds_rd128(ldsp p) { return *(LDS_AS bf16x8*)p; }
__device__ __forceinline__ bf16x8 lds_tr2(ldsp p0, ldsp p1) {
  bf16x4 a = __builtin_amdgcn_ds_read_tr16_b64_v4bf16((LDS_AS bf16x4*)p0);
  bf16x4 b = __builtin_amdgcn_ds_read_tr16_b64_v4bf16((LDS_AS bf16x4*)p1);
  return __builtin_shufflevector(a, b, 0, 1, 2, 3, 4, 5, 6, 7);
}
__device__ __forceinline__ f32x4 mfma(bf16x8 a, bf16x8 b, f32x4 c) { return __builtin_amdgcn_mfma_f32_16x16x32_bf16(a, b, c, 0, 0, 0); }
__device__ __forceinline__ bf16x4 cvt4(f32x4 v) {
  bf16x4 r; r[0] = (__bf16)v[0]; r[1] = (__bf16)v[1]; r[2] = (__bf16)v[2]; r[3] = (__bf16)v[3]; return r;
}
__device__ __forceinline__ bf16x8 cvt8(f32x4 a, f32x4 b) { return __builtin_shufflevector(cvt4(a), cvt4(b), 0, 1, 2, 3, 4, 5, 6, 7); }
__device__ __forceinline__ float max3(float a, float b, float c) { return __builtin_elementwise_maximum(__builtin_elementwise_maximum(a, b), c); }
__device__ __forceinline__ float max8(f32x4 a, f32x4 b) {
  return max3(max3(max3(a[0], a[1], a[2]), a[3], b[0]), max3(b[1], b[2], b[3]), b[3]);
}
__device__ __forceinline__ float xmax_g(float v) {
  auto r = __builtin_amdgcn_permlane16_swap(__float_as_uint(v), __float_as_uint(v), false, false);
  v = fmaxf(__uint_as_float(r[0]), __uint_as_float(r[1]));
  auto r2 = __builtin_amdgcn_permlane32_swap(__float_as_uint(v), __float_as_uint(v), false, false);
  return fmaxf(__uint_as_float(r2[0]), __uint_as_float(r2[1]));
}
__device__ __forceinline__ int xoff(int wd, int r, int qt) { return (r == 0 ? L_O2 : (r == 1 ? L_SLOT : 4 * L_SLOT)) + (wd * 3 + qt) * 1024; }

__global__ __launch_bounds__(512, 2) void gqa_fused(const __bf16* __restrict__ x, const __bf16* __restrict__ kc, const __bf16* __restrict__ vc,
                                                    const __bf16* __restrict__ wq, const __bf16* __restrict__ wo, char* ws, __bf16* __restrict__ out) {
  extern __shared__ __attribute__((aligned(16))) char smem_raw[];
  ldsp sm = (ldsp)smem_raw;
  const unsigned smbase = (unsigned)(uintptr_t)sm;
  const int w = __builtin_amdgcn_readfirstlane(threadIdx.x >> 6);
  const int bid = blockIdx.x, kvh = bid & 7, jj = bid >> 3, b = jj >> 1, hp = jj & 1;
  STAMP(0);
  const int n0 = kvh * 512 + hp * 256;
  const i32x4 srd_k = make_srd(kc + (size_t)(b * HKV + kvh) * SK * DH, SK * DH * 2);
  const i32x4 srd_v = make_srd(vc + (size_t)(b * HKV + kvh) * SK * DH, SK * DH * 2);
  const i32x4 srd_o = make_srd(wo + (size_t)n0 * DM, 256 * DM * 2);
  const int h = w >> 2, kq = w & 3;
  if (w >= 4) __builtin_amdgcn_s_setprio(1);
  typedef __attribute__((address_space(1))) unsigned long long gu64;
  gu64* epoch = (gu64*)(ws + WS_EPOCH + b * 256);
  gu64* flags = (gu64*)(ws + WS_FLAGS + b * 128 * 64);
  const unsigned long long tag = __hip_atomic_load(epoch, __ATOMIC_RELAXED, __HIP_MEMORY_SCOPE_AGENT) + 1ull;
  f32x4 oacc[8][3];
  f32x4 lacc[3];
  float mref[3];
  {
    const int l = threadIdx.x & 63, c = l & 15, g = l >> 4, qq = c >> 2, pp = l & 3;
    const unsigned kv0 = (unsigned)(g * 256 + ((c ^ g) << 4));
    const unsigned vv0 = (unsigned)(g * 256 + ((c ^ (2 * g)) << 4));
    if (w < 4) {
      f32x4 qacc[4][3];
      const i32x4 srd_x = make_srd(x + (size_t)b * SQ * DM, SQ * DM * 2);
      const i32x4 srd_w = make_srd(wq, DM * HQ * DH * 2);
#pragma unroll
      for (int i = 0; i < 3; ++i) {
        const int r = w * 12 + 4 * i;
        dma4(smbase + L_X + r * 1024, (unsigned)((l ^ (r & 15)) * 16), (unsigned)((l ^ ((r + 1) & 15)) * 16), (unsigned)((l ^ ((r + 2) & 15)) * 16),
             (unsigned)((l ^ ((r + 3) & 15)) * 16), srd_x, (unsigned)(r * 1024));
      }
      const unsigned ring = smbase + L_RING + w * (NRING * 4096);
      const unsigned wv0 = (unsigned)((l >> 3) * 8192 + (((l & 7) ^ (((l >> 4) & 1) << 1)) * 16));
      const unsigned wv1 = (wv0 ^ 64u) + (65536u - 1024u), wv2 = wv0 + 2u * (65536u - 1024u), wv3 = (wv0 ^ 64u) + 3u * (65536u - 1024u);
      const unsigned scol = (unsigned)((n0 + 64 * w) * 2);
#define WQ_ISSUE(s) dma4(ring + ((s) % NRING) * 4096, wv0, wv1, wv2, wv3, srd_w, scol + (unsigned)((((s) + b) & 15) * (32 * 8192)))
      WQ_ISSUE(0); WQ_ISSUE(1); WQ_ISSUE(2); WQ_ISSUE(3);
      waitv<16>();
      STAMP(1); BAR(); STAMP(2);
      const int fA = ((qq >> 1) & 1) | ((g & 1) << 1);
      ldsp wqa = sm + L_RING + w * (NRING * 4096) + (8 * g + qq) * 128 + 8 * pp;
      ldsp xa = sm + L_X + c * 1024;
      bf16x8 fw[2][4], fx[2][3];
#define A_READ(s, buf)                                                                                          \
  do {                                                                                                          \
    _Pragma("unroll") for (int t_ = 0; t_ < 4; ++t_) {                                                          \
      ldsp p_ = wqa + ((s) % NRING) * 4096 + ((t_ ^ fA) << 5);                                                  \
      fw[buf][t_] = lds_tr2(p_, p_ + 512);                                                                      \
    }                                                                                                           \
    _Pragma("unroll") for (int q_ = 0; q_ < 3; ++q_)                                                            \
        fx[buf][q_] = lds_rd128(xa + q_ * 16384 + ((((4 * (((s) + b) & 15)) | g) ^ c) << 4));                   \
  } while (0)
      waitv<12>();
      A_READ(0, 0);
#pragma unroll
      for (int s = 0; s < 16; ++s) {
        if (s + 4 < 16) WQ_ISSUE(s + 4);
        if (s == 12) {
          dma4nt(smbase + (32 * w) * 256, kv0, kv0 ^ 64u, kv0 ^ 128u, kv0 ^ 192u, srd_k, (unsigned)(128 * w * 256));
          dma4nt(smbase + (32 * w + 16) * 256, kv0, kv0 ^ 64u, kv0 ^ 128u, kv0 ^ 192u, srd_k, (unsigned)((128 * w + 16) * 256));
        }
        if (s + 1 < 16) {
          const int rem = ((s + 4 < 16) ? (s + 4) : 15) - (s + 1);
          if (s < 12) waitv<12>(); else if (rem == 2) waitv<16>(); else if (rem == 1) waitv<12>(); else waitv<8>();
          A_READ(s + 1, (s + 1) & 1);
        }
        SCHED();
#pragma unroll
        for (int t = 0; t < 4; ++t)
#pragma unroll
          for (int qt = 0; qt < 3; ++qt) qacc[t][qt] = mfma(fw[s & 1][t], fx[s & 1][qt], s == 0 ? f32x4{0.f, 0.f, 0.f, 0.f} : qacc[t][qt]);
        SCHED();
      }
      STAMP(3); BAR(); STAMP(4);
#pragma unroll
      for (int t = 0; t < 4; ++t)
#pragma unroll
        for (int qt = 0; qt < 3; ++qt) {
          const int c16 = 8 * (w & 1) + 2 * t + (g >> 1);
          *(LDS_AS bf16x4*)(sm + L_Q + (48 * (w >> 1) + 16 * qt + c) * 256 + ((c16 ^ c) << 4) + 8 * (g & 1)) = cvt4(qacc[t][qt] * SCL2E);
        }
    } else {
      STAMP(1); BAR(); STAMP(2);
      STAMP(3); BAR(); STAMP(4);
    }
    const unsigned kvrow = (unsigned)(128 * kq * 256);
#define K_ISSUE(j, slot)                                                                                                                  \
  do { if (h == 0) {                                                                                                                      \
    dma4nt(smbase + (slot) * L_SLOT + kq * 8192, kv0, kv0 ^ 64u, kv0 ^ 128u, kv0 ^ 192u, srd_k, kvrow + (unsigned)((j) * 8192));          \
    dma4nt(smbase + (slot) * L_SLOT + kq * 8192 + 4096, kv0, kv0 ^ 64u, kv0 ^ 128u, kv0 ^ 192u, srd_k, kvrow + (unsigned)((j) * 8192 + 4096)); \
  } } while (0)
#define V_ISSUE(j, slot)                                                                                                                  \
  do { if (h == 0) {                                                                                                                      \
    dma4nt(smbase + (slot) * L_SLOT + kq * 8192, vv0, vv0 ^ 128u, vv0, vv0 ^ 128u, srd_v, kvrow + (unsigned)((j) * 8192));                \
    dma4nt(smbase + (slot) * L_SLOT + kq * 8192 + 4096, vv0, vv0 ^ 128u, vv0, vv0 ^ 128u, srd_v, kvrow + (unsigned)((j) * 8192 + 4096));  \
  } } while (0)
    const unsigned ov0 = (unsigned)((l >> 3) * 1024 + (((l & 7) ^ (g << 1)) << 4));
#define WO_ISSUE_W(u, slot, ww) dma4(smbase + (slot) * L_SLOT + (ww) * 4096, ov0, ov0 + 7168u, ov0 + 14336u, ov0 + 21504u, srd_o, (unsigned)((ww) * 128 + (((u) + b) & 7) * 32768))
#define WO_ISSUE(u, slot) WO_ISSUE_W(u, slot, w)
#define WO_ISSUE2(u, slot) do { if (h == 0) { WO_ISSUE_W(u, slot, w); WO_ISSUE_W(u, slot, w + 4); } } while (0)
    K_ISSUE(1, 1);
    waitv<8>();
    STAMP(5); BAR(); STAMP(6);

    bf16x8 qf[4][3];
#pragma unroll
    for (int s = 0; s < 4; ++s)
#pragma unroll
      for (int qt = 0; qt < 3; ++qt) qf[s][qt] = lds_rd128(sm + L_Q + (48 * h + 16 * qt + c) * 256 + ((((4 * s) | g) ^ c) << 4));
    V_ISSUE(0, 2);
    ldsp ka = sm + (32 * kq + c) * 256;
    const int fV = 4 * (g & 1) + qq;
    unsigned va[4];
#pragma unroll
    for (int r = 0; r < 4; ++r) va[r] = smbase + (32 * kq + 4 * g + qq) * 256 + 8 * pp + (((2 * ((r + kq) & 3)) ^ fV) << 5);
    bf16x8 ones;
#pragma unroll
    for (int e = 0; e < 8; ++e) ones[e] = (__bf16)1.0f;
    f32x4 negm[3];
    constexpr int kslot[4] = {0, 1, 3, 4}, vslot[4] = {2, 0, 1, 2};
#define SYNC(j)                                                               \
  do {                                                                        \
    if ((j) < 3) waitv<8>(); else waitv<16>();                                \
    if ((j) == 1 && w == 3) waitv<0>();                                       \
    BAR(); STAMP(7 + (j));                                                    \
    if ((j) == 0) { V_ISSUE(1, 0); }                                          \
    if ((j) == 1) { if (w == 3) K_ISSUE(3, 4); V_ISSUE(2, 1); }               \
    if ((j) == 2) { WO_ISSUE2(0, 3); }                                        \
  } while (0)
#pragma unroll
    for (int j = 0; j < 4; ++j) {
      f32x4 sacc[2][3];
      {
        unsigned kso = (unsigned)(kslot[j] * L_SLOT);
        asm volatile("" : "+s"(kso));
        bf16x8 kf[2][4];
#pragma unroll
        for (int s = 0; s < 4; ++s)
#pragma unroll
          for (int kt = 0; kt < 2; ++kt) kf[kt][s] = lds_rd128(ka + kso + kt * 4096 + ((((4 * s) | g) ^ c) << 4));
        SCHED();
#pragma unroll
        for (int s = 0; s < 4; ++s)
#pragma unroll
          for (int kt = 0; kt < 2; ++kt)
#pragma unroll
            for (int qt = 0; qt < 3; ++qt)
              sacc[kt][qt] = mfma(kf[kt][s], qf[s][qt], s == 0 ? (j == 0 ? f32x4{0.f, 0.f, 0.f, 0.f} : negm[qt]) : sacc[kt][qt]);
        SCHED();
        if (j == 0) K_ISSUE(2, 3);
      }
      bool resc = false;
      float al[3];
      if (j == 0) {
#pragma unroll
        for (int qt = 0; qt < 3; ++qt) {
          const float m = xmax_g(max8(sacc[0][qt], sacc[1][qt]));
          if (g == 0) *(LDS_AS float*)(sm + L_ST1 + ((w * 3 + qt) * 16 + c) * 4) = m;
        }
        SYNC(0);
#pragma unroll
        for (int qt = 0; qt < 3; ++qt) {
          float m = *(LDS_AS float*)(sm + L_ST1 + ((h * 12 + qt) * 16 + c) * 4);
#pragma unroll
          for (int k = 1; k < 4; ++k) m = fmaxf(m, *(LDS_AS float*)(sm + L_ST1 + ((h * 12 + 3 * k + qt) * 16 + c) * 4));
          mref[qt] = m;
          negm[qt] = f32x4{-m, -m, -m, -m};
          sacc[0][qt] += negm[qt];
          sacc[1][qt] += negm[qt];
        }
      } else {
        if (h == 0) SYNC(j);
        float t = max3(sacc[0][0][0], sacc[0][0][1], sacc[0][0][2]);
        t = max3(t, sacc[0][0][3], sacc[1][0][0]); t = max3(t, sacc[1][0][1], sacc[1][0][2]);
        t = max3(t, sacc[1][0][3], sacc[0][1][0]); t = max3(t, sacc[0][1][1], sacc[0][1][2]);
        t = max3(t, sacc[0][1][3], sacc[1][1][0]); t = max3(t, sacc[1][1][1], sacc[1][1][2]);
        t = max3(t, sacc[1][1][3], sacc[0][2][0]); t = max3(t, sacc[0][2][1], sacc[0][2][2]);
        t = max3(t, sacc[0][2][3], sacc[1][2][0]); t = max3(t, sacc[1][2][1], sacc[1][2][2]);
        t = max3(t, sacc[1][2][3], t);
        resc = __any(!(t <= RESC_T));
        if (__builtin_expect(resc, 0)) {
#pragma unroll
          for (int qt = 0; qt < 3; ++qt) {
            const float d = fmaxf(xmax_g(max8(sacc[0][qt], sacc[1][qt])), 0.f);
            al[qt] = __builtin_amdgcn_exp2f(-d);
            mref[qt] += d;
            negm[qt] -= d;
            sacc[0][qt] -= d;
            sacc[1][qt] -= d;
          }
        }
      }
      bf16x8 pf[3];
#pragma unroll
      for (int qt = 0; qt < 3; ++qt) {
#pragma unroll
        for (int kt = 0; kt < 2; ++kt)
#pragma unroll
          for (int r = 0; r < 4; ++r) sacc[kt][qt][r] = __builtin_amdgcn_exp2f(sacc[kt][qt][r]);
        pf[qt] = cvt8(sacc[0][qt], sacc[1][qt]);
        asm volatile("" : "+v"(pf[qt]));
      }
      SCHED();
      if (j > 0) {
        if (h == 1) SYNC(j);
        if (__builtin_expect(resc, 0)) {
#pragma unroll
          for (int qt = 0; qt < 3; ++qt) {
            lacc[qt] *= al[qt];
#pragma unroll
            for (int i = 0; i < 8; ++i) oacc[i][qt] *= al[qt];
          }
        }
      }
      {
        unsigned vso = (unsigned)(vslot[j] * L_SLOT);
        asm volatile("" : "+s"(vso));
        bf16x8 vf[8];
#pragma unroll
        for (int i = 0; i < 8; ++i) {
          ldsp p = (ldsp)((va[i >> 1] ^ (unsigned)((i & 1) << 5)) + vso);
          vf[i] = lds_tr2(p, p + 4096);
        }
        SCHED();
#pragma unroll
        for (int qt = 0; qt < 3; ++qt) lacc[qt] = mfma(ones, pf[qt], j == 0 ? f32x4{0.f, 0.f, 0.f, 0.f} : lacc[qt]);
#pragma unroll
        for (int i = 0; i < 8; ++i)
#pragma unroll
          for (int qt = 0; qt < 3; ++qt) oacc[i][qt] = mfma(vf[i], pf[qt], j == 0 ? f32x4{0.f, 0.f, 0.f, 0.f} : oacc[i][qt]);
        SCHED();
        if (j == 0 && w != 3) K_ISSUE(3, 4);
        if (j == 1) V_ISSUE(3, 2);
        if (j == 2) WO_ISSUE2(1, 0);
      }
    }
    if (g == 0) {
#pragma unroll
      for (int qt = 0; qt < 3; ++qt) *(LDS_AS f32x2*)(sm + L_ST2 + ((w * 3 + qt) * 16 + c) * 8) = f32x2{mref[qt], lacc[qt][0]};
    }
    BAR(); STAMP(11);
  }
  int tz = threadIdx.x;
  asm volatile("" : "+v"(tz));
  const int l = tz & 63, c = l & 15, g = l >> 4, qq = c >> 2, pp = l & 3;
  {
    float inv[3], sc[3];
    bool differ = false;
#pragma unroll
    for (int qt = 0; qt < 3; ++qt) {
      f32x2 ml[4];
#pragma unroll
      for (int k = 0; k < 4; ++k) ml[k] = *(LDS_AS f32x2*)(sm + L_ST2 + ((h * 12 + 3 * k + qt) * 16 + c) * 8);
      const float m = fmaxf(fmaxf(ml[0][0], ml[1][0]), fmaxf(ml[2][0], ml[3][0]));
      float lt = 0.f;
#pragma unroll
      for (int k = 0; k < 4; ++k) lt += ml[k][1] * __builtin_amdgcn_exp2f(ml[k][0] - m);
      inv[qt] = __builtin_amdgcn_rcpf(lt);
      sc[qt] = __builtin_amdgcn_exp2f(mref[qt] - m);
      differ = differ || (mref[qt] != m);
    }
    if (__builtin_expect(__any(differ), 0)) {
#pragma unroll
      for (int qt = 0; qt < 3; ++qt)
#pragma unroll
        for (int i = 0; i < 8; ++i) oacc[i][qt] *= sc[qt];
    }
#pragma unroll
    for (int r = 1; r < 4; ++r) {
      const int wd = h * 4 + ((kq + r) & 3);
#pragma unroll
      for (int qt = 0; qt < 3; ++qt) *(LDS_AS bf16x8*)(sm + xoff(wd, r - 1, qt) + l * 16) = cvt8(oacc[2 * r][qt], oacc[2 * r + 1][qt]);
    }
    bf16x8 sel0, sel1;
#pragma unroll
    for (int e = 0; e < 4; ++e) {
      const __bf16 v = (4 * g + e == c) ? (__bf16)1.0f : (__bf16)0.0f;
      sel0[e] = v; sel0[4 + e] = (__bf16)0.0f; sel1[e] = (__bf16)0.0f; sel1[4 + e] = v;
    }
    BAR(); STAMP(17);
#pragma unroll
    for (int qt = 0; qt < 3; ++qt) {
#pragma unroll
      for (int r = 0; r < 3; ++r) {
        bf16x8 t = lds_rd128(sm + xoff(w, r, qt) + l * 16);
        oacc[0][qt] = mfma(sel0, t, oacc[0][qt]);
        oacc[1][qt] = mfma(sel1, t, oacc[1][qt]);
      }
    }
#pragma unroll
    for (int qt = 0; qt < 3; ++qt) *(LDS_AS bf16x8*)(sm + L_O2 + (w * 3 + qt) * 1024 + l * 16) = cvt8(oacc[0][qt] * inv[qt], oacc[1][qt] * inv[qt]);
  }
  waitv<0>();
  BAR(); STAMP(18);
  const unsigned ov0 = (unsigned)((l >> 3) * 1024 + (((l & 7) ^ (g << 1)) << 4));

  f32x4 cacc[4][3];
  {
    bf16x8 of[2][3];
    const int fC = 2 * (g & 1) + (qq >> 1);
    ldsp wra = sm + w * 4096 + (4 * g + qq) * 128 + 8 * pp;
    bf16x8 wf[2][4];
#define CSLOT(u) (((u) & 3) == 0 ? 3 : (((u) & 3) == 1 ? 0 : (((u) & 3) == 2 ? 1 : 4)))
#define C_READ(u)                                                                                                   \
  do {                                                                                                              \
    _Pragma("unroll") for (int t_ = 0; t_ < 4; ++t_) {                                                              \
      ldsp p_ = wra + CSLOT(u) * L_SLOT + ((t_ ^ fC) << 5);                                                         \
      wf[(u) & 1][t_] = lds_tr2(p_, p_ + 2048);                                                                     \
    }                                                                                                               \
    _Pragma("unroll") for (int q_ = 0; q_ < 3; ++q_)                                                                \
        of[(u) & 1][q_] = lds_rd128(sm + L_O2 + ((((u) + b) & 7) * 3 + q_) * 1024 + l * 16);                        \
  } while (0)
    WO_ISSUE(2, 1);
    C_READ(0);
#pragma unroll
    for (int u = 0; u < 8; ++u) {
      if (u + 3 < 8) WO_ISSUE(u + 3, CSLOT(u + 3));
      if (u + 1 < 8) {
        if (u + 1 >= 2) { const int last = (u + 3 < 8) ? (u + 3) : 7; const int rem = last - (u + 1);
          if (rem == 2) waitv<8>(); else if (rem == 1) waitv<4>(); else waitv<0>(); }
        C_READ(u + 1);
      }
      SCHED();
#pragma unroll
      for (int t = 0; t < 4; ++t)
#pragma unroll
        for (int qt = 0; qt < 3; ++qt) cacc[t][qt] = mfma(wf[u & 1][t], of[u & 1][qt], u == 0 ? f32x4{0.f, 0.f, 0.f, 0.f} : cacc[t][qt]);
      SCHED();
    }
  }
  STAMP(19);
  const int s16 = kvh * 2 + hp;
  {
    auto rs = __builtin_amdgcn_make_buffer_rsrc((void*)(ws + WS_PART + ((size_t)(b * 16 + s16) * 8 + w) * 6144), 0, 6144, 0x00020000);
#pragma unroll
    for (int a = 0; a < 2; ++a)
#pragma unroll
      for (int qt = 0; qt < 3; ++qt)
        __builtin_amdgcn_raw_buffer_store_b128(__builtin_bit_cast(u32x4, cvt8(cacc[2 * a][qt], cacc[2 * a + 1][qt])), rs, l * 16 + (a * 3 + qt) * 1024, 0, 16);
  }
  asm volatile("s_waitcnt vmcnt(0)" ::: "memory");
  if (l == 0) __hip_atomic_store(flags + (w * 16 + s16) * 8, tag, __ATOMIC_RELAXED, __HIP_MEMORY_SCOPE_AGENT);
  if (w < 3) {
    for (unsigned spins = 0; spins < (1u << 22); ++spins) {
      const unsigned long long f = __hip_atomic_load(flags + ((s16 >> 1) * 16 + (l & 15)) * 8, __ATOMIC_RELAXED, __HIP_MEMORY_SCOPE_AGENT);
      if (__all(f == tag)) break;
      __builtin_amdgcn_s_sleep(1);
    }
    __builtin_amdgcn_fence(__ATOMIC_ACQUIRE, "wavefront");
    if (s16 == 0 && w == 0 && l == 0) __hip_atomic_store(epoch, tag, __ATOMIC_RELAXED, __HIP_MEMORY_SCOPE_AGENT);
    STAMP(21);
    const int rem = s16 * 192 + w * 64 + l;
    auto rs = __builtin_amdgcn_make_buffer_rsrc((void*)(ws + WS_PART + (size_t)b * 16 * 49152), 0, 16 * 49152, 0x00020000);
    u32x4 v[16];
#pragma unroll
    for (int sl = 0; sl < 16; ++sl) v[sl] = __builtin_amdgcn_raw_buffer_load_b128(rs, rem * 16, sl * 49152, 16);
    f32x4 a0 = {0.f, 0.f, 0.f, 0.f}, a1 = {0.f, 0.f, 0.f, 0.f};
#pragma unroll
    for (int sl = 0; sl < 16; ++sl) {
#pragma unroll
      for (int e = 0; e < 2; ++e) {
        a0[2 * e] += __uint_as_float(v[sl][e] << 16); a0[2 * e + 1] += __uint_as_float(v[sl][e] & 0xffff0000u);
        a1[2 * e] += __uint_as_float(v[sl][2 + e] << 16); a1[2 * e + 1] += __uint_as_float(v[sl][2 + e] & 0xffff0000u);
      }
    }
    const int tq = rem >> 6, qt = tq % 3, wa = tq / 3;
    const int q = 16 * qt + c, n = 32 * wa + 4 * g;
    __bf16* o = out + ((size_t)b * SQ + q) * DM + n;
    *(bf16x4*)o = cvt4(a0);
    *(bf16x4*)(o + 16) = cvt4(a1);
  }
}

extern "C" void kernel_launch(void* const* d_in, const int* in_sizes, int n_in, void* d_out, int out_size, void* d_ws, size_t ws_size,
                              hipStream_t stream) {
  const __bf16* x = (const __bf16*)d_in[0];
  const __bf16* kc = (const __bf16*)d_in[1];
  const __bf16* vc = (const __bf16*)d_in[2];
  const __bf16* wq = (const __bf16*)d_in[3];
  const __bf16* wo = (const __bf16*)d_in[4];
  (void)hipFuncSetAttribute((const void*)gqa_fused, hipFuncAttributeMaxDynamicSharedMemorySize, LDS_BYTES);
  hipLaunchKernelGGL(gqa_fused, dim3(256), dim3(512), LDS_BYTES, stream, x, kc, vc, wq, wo, (char*)d_ws, (__bf16*)d_out);
}
```

```cpp
#include <hip/hip_runtime.h>
#include <hip/hip_bf16.h>
#include <stdint.h>

typedef __bf16 bf16x8 __attribute__((ext_vector_type(8)));
typedef __bf16 bf16x4 __attribute__((ext_vector_type(4)));
typedef float f32x4 __attribute__((ext_vector_type(4)));
typedef float f32x2 __attribute__((ext_vector_type(2)));
typedef int i32x4 __attribute__((ext_vector_type(4)));
typedef unsigned u32x4 __attribute__((ext_vector_type(4)));
#define LDS_AS __attribute__((address_space(3)))
typedef LDS_AS char* ldsp;

constexpr int B_ = 16, SQ = 48, DM = 512, HQ = 32, HKV = 8, SK = 512, DH = 128;
constexpr float SCL2E = 0.08838834764831845f * 1.4426950408889634f;

constexpr int L_SLOT = 32768;
constexpr int L_RING = 32768;
constexpr int L_X = 114688;
constexpr int L_Q = 131072;
constexpr int L_ST1 = 155648;
constexpr int L_ST2 = 32768 + 24576;
constexpr int L_O2 = 2 * 32768;
constexpr int LDS_BYTES = 163840;
constexpr int WS_EPOCH = 0, WS_FLAGS = 4096, WS_PART = 4096 + 16 * 128 * 64;
constexpr int NRING = 5;
#define RESC_T 16.0f

#define PRIO 1
#define BAR() asm volatile("s_waitcnt lgkmcnt(0)\n\ts_barrier" ::: "memory")
#define SCHED() __builtin_amdgcn_sched_barrier(0)
#define STAMP(i)
template <int N> __device__ __forceinline__ void waitv() { asm volatile("s_waitcnt vmcnt(%0)" ::"i"(N) : "memory"); }

__device__ __forceinline__ void dma4(unsigned lds, unsigned v0, unsigned v1, unsigned v2, unsigned v3, i32x4 srd, unsigned soff) {
  asm volatile("s_mov_b32 m0, %0\n\ts_nop 0\n\t"
               "buffer_load_dwordx4 %1, %5, %6 offen lds\n\t"
               "buffer_load_dwordx4 %2, %5, %6 offen offset:1024 lds\n\t"
               "buffer_load_dwordx4 %3, %5, %6 offen offset:2048 lds\n\t"
               "buffer_load_dwordx4 %4, %5, %6 offen offset:3072 lds"
               ::"s"(lds), "v"(v0), "v"(v1), "v"(v2), "v"(v3), "s"(srd), "s"(soff) : "memory");
}
__device__ __forceinline__ void dma4nt(unsigned lds, unsigned v0, unsigned v1, unsigned v2, unsigned v3, i32x4 srd, unsigned soff) {
  asm volatile("s_mov_b32 m0, %0\n\ts_nop 0\n\t"
               "buffer_load_dwordx4 %1, %5, %6 offen nt lds\n\t"
               "buffer_load_dwordx4 %2, %5, %6 offen offset:1024 nt lds\n\t"
               "buffer_load_dwordx4 %3, %5, %6 offen offset:2048 nt lds\n\t"
               "buffer_load_dwordx4 %4, %5, %6 offen offset:3072 nt lds"
               ::"s"(lds), "v"(v0), "v"(v1), "v"(v2), "v"(v3), "s"(srd), "s"(soff) : "memory");
}
__device__ __forceinline__ i32x4 make_srd(const void* p, unsigned bytes) {
  unsigned long long a = (unsigned long long)p;
  i32x4 s;
  s[0] = (int)(unsigned)a; s[1] = (int)((unsigned)(a >> 32) & 0xffffu); s[2] = (int)bytes; s[3] = 0x00020000;
  return s;
}
__device__ __forceinline__ bf16x8 lds_rd128(ldsp p) { return *(LDS_AS bf16x8*)p; }
__device__ __forceinline__ bf16x8 lds_tr2(ldsp p0, ldsp p1) {
  bf16x4 a = __builtin_amdgcn_ds_read_tr16_b64_v4bf16((LDS_AS bf16x4*)p0);
  bf16x4 b = __builtin_amdgcn_ds_read_tr16_b64_v4bf16((LDS_AS bf16x4*)p1);
  return __builtin_shufflevector(a, b, 0, 1, 2, 3, 4, 5, 6, 7);
}
__device__ __forceinline__ f32x4 mfma(bf16x8 a, bf16x8 b, f32x4 c) { return __builtin_amdgcn_mfma_f32_16x16x32_bf16(a, b, c, 0, 0, 0); }
__device__ __forceinline__ bf16x4 cvt4(f32x4 v) {
  bf16x4 r; r[0] = (__bf16)v[0]; r[1] = (__bf16)v[1]; r[2] = (__bf16)v[2]; r[3] = (__bf16)v[3]; return r;
}
__device__ __forceinline__ bf16x8 cvt8(f32x4 a, f32x4 b) { return __builtin_shufflevector(cvt4(a), cvt4(b), 0, 1, 2, 3, 4, 5, 6, 7); }
__device__ __forceinline__ float max3(float a, float b, float c) { return __builtin_elementwise_maximum(__builtin_elementwise_maximum(a, b), c); }
__device__ __forceinline__ float max8(f32x4 a, f32x4 b) {
  return max3(max3(max3(a[0], a[1], a[2]), a[3], b[0]), max3(b[1], b[2], b[3]), b[3]);
}
__device__ __forceinline__ float xmax_g(float v) {
  auto r = __builtin_amdgcn_permlane16_swap(__float_as_uint(v), __float_as_uint(v), false, false);
  v = fmaxf(__uint_as_float(r[0]), __uint_as_float(r[1]));
  auto r2 = __builtin_amdgcn_permlane32_swap(__float_as_uint(v), __float_as_uint(v), false, false);
  return fmaxf(__uint_as_float(r2[0]), __uint_as_float(r2[1]));
}
__device__ __forceinline__ int xoff(int wd, int r, int qt) { return (r == 0 ? L_O2 : (r == 1 ? L_SLOT : 4 * L_SLOT)) + (wd * 3 + qt) * 1024; }

__global__ __launch_bounds__(512, 2) void gqa_fused(const __bf16* __restrict__ x, const __bf16* __restrict__ kc, const __bf16* __restrict__ vc,
                                                    const __bf16* __restrict__ wq, const __bf16* __restrict__ wo, char* ws, __bf16* __restrict__ out) {
  extern __shared__ __attribute__((aligned(16))) char smem_raw[];
  ldsp sm = (ldsp)smem_raw;
  const unsigned smbase = (unsigned)(uintptr_t)sm;
  const int w = __builtin_amdgcn_readfirstlane(threadIdx.x >> 6);
  const int bid = blockIdx.x, kvh = bid & 7, jj = bid >> 3, b = jj >> 1, hp = jj & 1;
  STAMP(0);
  const int n0 = kvh * 512 + hp * 256;
  const i32x4 srd_k = make_srd(kc + (size_t)(b * HKV + kvh) * SK * DH, SK * DH * 2);
  const i32x4 srd_v = make_srd(vc + (size_t)(b * HKV + kvh) * SK * DH, SK * DH * 2);
  const i32x4 srd_o = make_srd(wo + (size_t)n0 * DM, 256 * DM * 2);
  const int h = w >> 2, kq = w & 3;
  if (w >= 4) __builtin_amdgcn_s_setprio(1);
  typedef __attribute__((address_space(1))) unsigned long long gu64;
  gu64* epoch = (gu64*)(ws + WS_EPOCH + b * 256);
  gu64* flags = (gu64*)(ws + WS_FLAGS + b * 128 * 64);
  const unsigned long long tag = __hip_atomic_load(epoch, __ATOMIC_RELAXED, __HIP_MEMORY_SCOPE_AGENT) + 1ull;
  f32x4 oacc[8][3];
  f32x4 lacc[3];
  float mref[3];
  {
    const int l = threadIdx.x & 63, c = l & 15, g = l >> 4, qq = c >> 2, pp = l & 3;
    const unsigned kv0 = (unsigned)(g * 256 + ((c ^ g) << 4));
    const unsigned vv0 = (unsigned)(g * 256 + ((c ^ (2 * g)) << 4));
    if (w < 4) {
      f32x4 qacc[4][3];
      const i32x4 srd_x = make_srd(x + (size_t)b * SQ * DM, SQ * DM * 2);
      const i32x4 srd_w = make_srd(wq, DM * HQ * DH * 2);
#pragma unroll
      for (int i = 0; i < 3; ++i) {
        const int r = w * 12 + 4 * i;
        dma4(smbase + L_X + r * 1024, (unsigned)((l ^ (r & 15)) * 16), (unsigned)((l ^ ((r + 1) & 15)) * 16), (unsigned)((l ^ ((r + 2) & 15)) * 16),
             (unsigned)((l ^ ((r + 3) & 15)) * 16), srd_x, (unsigned)(r * 1024));
      }
      const unsigned ring = smbase + L_RING + w * (NRING * 4096);
      const unsigned wv0 = (unsigned)((l >> 3) * 8192 + (((l & 7) ^ (((l >> 4) & 1) << 1)) * 16));
      const unsigned wv1 = (wv0 ^ 64u) + (65536u - 1024u), wv2 = wv0 + 2u * (65536u - 1024u), wv3 = (wv0 ^ 64u) + 3u * (65536u - 1024u);
      const unsigned scol = (unsigned)((n0 + 64 * w) * 2);
#define WQ_ISSUE(s) dma4(ring + ((s) % NRING) * 4096, wv0, wv1, wv2, wv3, srd_w, scol + (unsigned)((((s) + b) & 15) * (32 * 8192)))
      WQ_ISSUE(0); WQ_ISSUE(1); WQ_ISSUE(2); WQ_ISSUE(3);
      waitv<16>();
      STAMP(1); BAR(); STAMP(2);
      const int fA = ((qq >> 1) & 1) | ((g & 1) << 1);
      ldsp wqa = sm + L_RING + w * (NRING * 4096) + (8 * g + qq) * 128 + 8 * pp;
      ldsp xa = sm + L_X + c * 1024;
      bf16x8 fw[2][4], fx[2][3];
#define A_READ(s, buf)                                                                                          \
  do {                                                                                                          \
    _Pragma("unroll") for (int t_ = 0; t_ < 4; ++t_) {                                                          \
      ldsp p_ = wqa + ((s) % NRING) * 4096 + ((t_ ^ fA) << 5);                                                  \
      fw[buf][t_] = lds_tr2(p_, p_ + 512);                                                                      \
    }                                                                                                           \
    _Pragma("unroll") for (int q_ = 0; q_ < 3; ++q_)                                                            \
        fx[buf][q_] = lds_rd128(xa + q_ * 16384 + ((((4 * (((s) + b) & 15)) | g) ^ c) << 4));                   \
  } while (0)
      waitv<12>();
      A_READ(0, 0);
#pragma unroll
      for (int s = 0; s < 16; ++s) {
        if (s + 4 < 16) WQ_ISSUE(s + 4);
        if (s == 12) {
          dma4nt(smbase + (32 * w) * 256, kv0, kv0 ^ 64u, kv0 ^ 128u, kv0 ^ 192u, srd_k, (unsigned)(128 * w * 256));
          dma4nt(smbase + (32 * w + 16) * 256, kv0, kv0 ^ 64u, kv0 ^ 128u, kv0 ^ 192u, srd_k, (unsigned)((128 * w + 16) * 256));
        }
        if (s + 1 < 16) {
          const int rem = ((s + 4 < 16) ? (s + 4) : 15) - (s + 1);
          if (s < 12) waitv<12>(); else if (rem == 2) waitv<16>(); else if (rem == 1) waitv<12>(); else waitv<8>();
          A_READ(s + 1, (s + 1) & 1);
        }
        SCHED();
#pragma unroll
        for (int t = 0; t < 4; ++t)
#pragma unroll
          for (int qt = 0; qt < 3; ++qt) qacc[t][qt] = mfma(fw[s & 1][t], fx[s & 1][qt], s == 0 ? f32x4{0.f, 0.f, 0.f, 0.f} : qacc[t][qt]);
        SCHED();
      }
      STAMP(3); BAR(); STAMP(4);
#pragma unroll
      for (int t = 0; t < 4; ++t)
#pragma unroll
        for (int qt = 0; qt < 3; ++qt) {
          const int c16 = 8 * (w & 1) + 2 * t + (g >> 1);
          *(LDS_AS bf16x4*)(sm + L_Q + (48 * (w >> 1) + 16 * qt + c) * 256 + ((c16 ^ c) << 4) + 8 * (g & 1)) = cvt4(qacc[t][qt] * SCL2E);
        }
    } else {
      STAMP(1); BAR(); STAMP(2);
      STAMP(3); BAR(); STAMP(4);
    }
    const unsigned kvrow = (unsigned)(128 * kq * 256);
#define K_ISSUE(j, slot)                                                                                                                  \
  do { if (h == 0) {                                                                                                                      \
    dma4nt(smbase + (slot) * L_SLOT + kq * 8192, kv0, kv0 ^ 64u, kv0 ^ 128u, kv0 ^ 192u, srd_k, kvrow + (unsigned)((j) * 8192));          \
    dma4nt(smbase + (slot) * L_SLOT + kq * 8192 + 4096, kv0, kv0 ^ 64u, kv0 ^ 128u, kv0 ^ 192u, srd_k, kvrow + (unsigned)((j) * 8192 + 4096)); \
  } } while (0)
#define V_ISSUE(j, slot)                                                                                                                  \
  do { if (h == 0) {                                                                                                                      \
    dma4nt(smbase + (slot) * L_SLOT + kq * 8192, vv0, vv0 ^ 128u, vv0, vv0 ^ 128u, srd_v, kvrow + (unsigned)((j) * 8192));                \
    dma4nt(smbase + (slot) * L_SLOT + kq * 8192 + 4096, vv0, vv0 ^ 128u, vv0, vv0 ^ 128u, srd_v, kvrow + (unsigned)((j) * 8192 + 4096));  \
  } } while (0)
    const unsigned ov0 = (unsigned)((l >> 3) * 1024 + (((l & 7) ^ (g << 1)) << 4));
#define WO_ISSUE_W(u, slot, ww) dma4(smbase + (slot) * L_SLOT + (ww) * 4096, ov0, ov0 + 7168u, ov0 + 14336u, ov0 + 21504u, srd_o, (unsigned)((ww) * 128 + (((u) + b) & 7) * 32768))
#define WO_ISSUE(u, slot) WO_ISSUE_W(u, slot, w)
#define WO_ISSUE2(u, slot) do { if (h == 0) { WO_ISSUE_W(u, slot, w); WO_ISSUE_W(u, slot, w + 4); } } while (0)
    K_ISSUE(1, 1);
    waitv<8>();
    STAMP(5); BAR(); STAMP(6);

    bf16x8 qf[4][3];
#pragma unroll
    for (int s = 0; s < 4; ++s)
#pragma unroll
      for (int qt = 0; qt < 3; ++qt) qf[s][qt] = lds_rd128(sm + L_Q + (48 * h + 16 * qt + c) * 256 + ((((4 * s) | g) ^ c) << 4));
    V_ISSUE(0, 2);
    ldsp ka = sm + (32 * kq + c) * 256;
    const int fV = 4 * (g & 1) + qq;
    unsigned va[4];
#pragma unroll
    for (int r = 0; r < 4; ++r) va[r] = smbase + (32 * kq + 4 * g + qq) * 256 + 8 * pp + (((2 * ((r + kq) & 3)) ^ fV) << 5);
    bf16x8 ones;
#pragma unroll
    for (int e = 0; e < 8; ++e) ones[e] = (__bf16)1.0f;
    f32x4 negm[3];
    constexpr int kslot[4] = {0, 1, 3, 4}, vslot[4] = {2, 0, 1, 2};
#define SYNC(j)                                                               \
  do {                                                                        \
    if ((j) < 3) waitv<8>(); else waitv<16>();                                \
    if ((j) == 1 && w == 3) waitv<0>();                                       \
    BAR(); STAMP(7 + (j));                                                    \
    if ((j) == 0) { V_ISSUE(1, 0); }                                          \
    if ((j) == 1) { if (w == 3) K_ISSUE(3, 4); V_ISSUE(2, 1); }               \
    if ((j) == 2) { WO_ISSUE2(0, 3); }                                        \
  } while (0)
#pragma unroll
    for (int j = 0; j < 4; ++j) {
      f32x4 sacc[2][3];
      {
        unsigned kso = (unsigned)(kslot[j] * L_SLOT);
        asm volatile("" : "+s"(kso));
        bf16x8 kf[2][4];
#pragma unroll
        for (int s = 0; s < 4; ++s)
#pragma unroll
          for (int kt = 0; kt < 2; ++kt) kf[kt][s] = lds_rd128(ka + kso + kt * 4096 + ((((4 * s) | g) ^ c) << 4));
        SCHED();
#pragma unroll
        for (int s = 0; s < 4; ++s)
#pragma unroll
          for (int kt = 0; kt < 2; ++kt)
#pragma unroll
            for (int qt = 0; qt < 3; ++qt)
              sacc[kt][qt] = mfma(kf[kt][s], qf[s][qt], s == 0 ? (j == 0 ? f32x4{0.f, 0.f, 0.f, 0.f} : negm[qt]) : sacc[kt][qt]);
        SCHED();
        if (j == 0) K_ISSUE(2, 3);
      }
      bool resc = false;
      float al[3];
      if (j == 0) {
#pragma unroll
        for (int qt = 0; qt < 3; ++qt) {
          const float m = xmax_g(max8(sacc[0][qt], sacc[1][qt]));
          if (g == 0) *(LDS_AS float*)(sm + L_ST1 + ((w * 3 + qt) * 16 + c) * 4) = m;
        }
        SYNC(0);
#pragma unroll
        for (int qt = 0; qt < 3; ++qt) {
          float m = *(LDS_AS float*)(sm + L_ST1 + ((h * 12 + qt) * 16 + c) * 4);
#pragma unroll
          for (int k = 1; k < 4; ++k) m = fmaxf(m, *(LDS_AS float*)(sm + L_ST1 + ((h * 12 + 3 * k + qt) * 16 + c) * 4));
          mref[qt] = m;
          negm[qt] = f32x4{-m, -m, -m, -m};
          sacc[0][qt] += negm[qt];
          sacc[1][qt] += negm[qt];
        }
      } else {
        if (h == 0) SYNC(j);
        float t = max3(sacc[0][0][0], sacc[0][0][1], sacc[0][0][2]);
        t = max3(t, sacc[0][0][3], sacc[1][0][0]); t = max3(t, sacc[1][0][1], sacc[1][0][2]);
        t = max3(t, sacc[1][0][3], sacc[0][1][0]); t = max3(t, sacc[0][1][1], sacc[0][1][2]);
        t = max3(t, sacc[0][1][3], sacc[1][1][0]); t = max3(t, sacc[1][1][1], sacc[1][1][2]);
        t = max3(t, sacc[1][1][3], sacc[0][2][0]); t = max3(t, sacc[0][2][1], sacc[0][2][2]);
        t = max3(t, sacc[0][2][3], sacc[1][2][0]); t = max3(t, sacc[1][2][1], sacc[1][2][2]);
        t = max3(t, sacc[1][2][3], t);
        resc = __any(!(t <= RESC_T));
        if (__builtin_expect(resc, 0)) {
#pragma unroll
          for (int qt = 0; qt < 3; ++qt) {
            const float d = fmaxf(xmax_g(max8(sacc[0][qt], sacc[1][qt])), 0.f);
            al[qt] = __builtin_amdgcn_exp2f(-d);
            mref[qt] += d;
            negm[qt] -= d;
            sacc[0][qt] -= d;
            sacc[1][qt] -= d;
          }
        }
      }
      bf16x8 pf[3];
#pragma unroll
      for (int qt = 0; qt < 3; ++qt) {
#pragma unroll
        for (int kt = 0; kt < 2; ++kt)
#pragma unroll
          for (int r = 0; r < 4; ++r) sacc[kt][qt][r] = __builtin_amdgcn_exp2f(sacc[kt][qt][r]);
        pf[qt] = cvt8(sacc[0][qt], sacc[1][qt]);
        asm volatile("" : "+v"(pf[qt]));
      }
      SCHED();
      if (j > 0) {
        if (h == 1) SYNC(j);
        if (__builtin_expect(resc, 0)) {
#pragma unroll
          for (int qt = 0; qt < 3; ++qt) {
            lacc[qt] *= al[qt];
#pragma unroll
            for (int i = 0; i < 8; ++i) oacc[i][qt] *= al[qt];
          }
        }
      }
      {
        unsigned vso = (unsigned)(vslot[j] * L_SLOT);
        asm volatile("" : "+s"(vso));
        bf16x8 vf[8];
#pragma unroll
        for (int i = 0; i < 8; ++i) {
          ldsp p = (ldsp)((va[i >> 1] ^ (unsigned)((i & 1) << 5)) + vso);
          vf[i] = lds_tr2(p, p + 4096);
        }
        SCHED();
#pragma unroll
        for (int qt = 0; qt < 3; ++qt) lacc[qt] = mfma(ones, pf[qt], j == 0 ? f32x4{0.f, 0.f, 0.f, 0.f} : lacc[qt]);
#pragma unroll
        for (int i = 0; i < 8; ++i)
#pragma unroll
          for (int qt = 0; qt < 3; ++qt) oacc[i][qt] = mfma(vf[i], pf[qt], j == 0 ? f32x4{0.f, 0.f, 0.f, 0.f} : oacc[i][qt]);
        SCHED();
        if (j == 0 && w != 3) K_ISSUE(3, 4);
        if (j == 1) V_ISSUE(3, 2);
        if (j == 2) WO_ISSUE2(1, 0);
      }
    }
    if (g == 0) {
#pragma unroll
      for (int qt = 0; qt < 3; ++qt) *(LDS_AS f32x2*)(sm + L_ST2 + ((w * 3 + qt) * 16 + c) * 8) = f32x2{mref[qt], lacc[qt][0]};
    }
    BAR(); STAMP(11);
  }
  int tz = threadIdx.x;
  asm volatile("" : "+v"(tz));
  const int l = tz & 63, c = l & 15, g = l >> 4, qq = c >> 2, pp = l & 3;
  {
    float inv[3], sc[3];
    bool differ = false;
#pragma unroll
    for (int qt = 0; qt < 3; ++qt) {
      f32x2 ml[4];
#pragma unroll
      for (int k = 0; k < 4; ++k) ml[k] = *(LDS_AS f32x2*)(sm + L_ST2 + ((h * 12 + 3 * k + qt) * 16 + c) * 8);
      const float m = fmaxf(fmaxf(ml[0][0], ml[1][0]), fmaxf(ml[2][0], ml[3][0]));
      float lt = 0.f;
#pragma unroll
      for (int k = 0; k < 4; ++k) lt += ml[k][1] * __builtin_amdgcn_exp2f(ml[k][0] - m);
      inv[qt] = __builtin_amdgcn_rcpf(lt);
      sc[qt] = __builtin_amdgcn_exp2f(mref[qt] - m);
      differ = differ || (mref[qt] != m);
    }
    if (__builtin_expect(__any(differ), 0)) {
#pragma unroll
      for (int qt = 0; qt < 3; ++qt)
#pragma unroll
        for (int i = 0; i < 8; ++i) oacc[i][qt] *= sc[qt];
    }
#pragma unroll
    for (int r = 1; r < 4; ++r) {
      const int wd = h * 4 + ((kq + r) & 3);
#pragma unroll
      for (int qt = 0; qt < 3; ++qt) *(LDS_AS bf16x8*)(sm + xoff(wd, r - 1, qt) + l * 16) = cvt8(oacc[2 * r][qt], oacc[2 * r + 1][qt]);
    }
    bf16x8 sel0, sel1;
#pragma unroll
    for (int e = 0; e < 4; ++e) {
      const __bf16 v = (4 * g + e == c) ? (__bf16)1.0f : (__bf16)0.0f;
      sel0[e] = v; sel0[4 + e] = (__bf16)0.0f; sel1[e] = (__bf16)0.0f; sel1[4 + e] = v;
    }
    BAR(); STAMP(17);
#pragma unroll
    for (int qt = 0; qt < 3; ++qt) {
#pragma unroll
      for (int r = 0; r < 3; ++r) {
        bf16x8 t = lds_rd128(sm + xoff(w, r, qt) + l * 16);
        oacc[0][qt] = mfma(sel0, t, oacc[0][qt]);
        oacc[1][qt] = mfma(sel1, t, oacc[1][qt]);
      }
    }
#pragma unroll
    for (int qt = 0; qt < 3; ++qt) *(LDS_AS bf16x8*)(sm + L_O2 + (w * 3 + qt) * 1024 + l * 16) = cvt8(oacc[0][qt] * inv[qt], oacc[1][qt] * inv[qt]);
  }
  waitv<0>();
  BAR(); STAMP(18);
  const unsigned ov0 = (unsigned)((l >> 3) * 1024 + (((l & 7) ^ (g << 1)) << 4));

  f32x4 cacc[4][3];
  {
    bf16x8 of[2][3];
    const int fC = 2 * (g & 1) + (qq >> 1);
    ldsp wra = sm + w * 4096 + (4 * g + qq) * 128 + 8 * pp;
    bf16x8 wf[2][4];
#define CSLOT(u) (((u) & 3) == 0 ? 3 : (((u) & 3) == 1 ? 0 : (((u) & 3) == 2 ? 1 : 4)))
#define C_READ(u)                                                                                                   \
  do {                                                                                                              \
    _Pragma("unroll") for (int t_ = 0; t_ < 4; ++t_) {                                                              \
      ldsp p_ = wra + CSLOT(u) * L_SLOT + ((t_ ^ fC) << 5);                                                         \
      wf[(u) & 1][t_] = lds_tr2(p_, p_ + 2048);                                                                     \
    }                                                                                                               \
    _Pragma("unroll") for (int q_ = 0; q_ < 3; ++q_)                                                                \
        of[(u) & 1][q_] = lds_rd128(sm + L_O2 + ((((u) + b) & 7) * 3 + q_) * 1024 + l * 16);                        \
  } while (0)
    if (h == 0) {
      asm volatile("; copy A");
    WO_ISSUE(2, 1);
    C_READ(0);
#pragma unroll
    for (int u = 0; u < 8; ++u) {
      if (u + 3 < 8) WO_ISSUE(u + 3, CSLOT(u + 3));
      if (u + 1 < 8) {
        if (u + 1 >= 2) { const int last = (u + 3 < 8) ? (u + 3) : 7; const int rem = last - (u + 1);
          if (rem == 2) waitv<8>(); else if (rem == 1) waitv<4>(); else waitv<0>(); }
        C_READ(u + 1);
      }
      SCHED();
#pragma unroll
      for (int t = 0; t < 4; ++t)
#pragma unroll
        for (int qt = 0; qt < 3; ++qt) cacc[t][qt] = mfma(wf[u & 1][t], of[u & 1][qt], u == 0 ? f32x4{0.f, 0.f, 0.f, 0.f} : cacc[t][qt]);
      SCHED();
    }
    } else {
      asm volatile("s_nop 0 ; copy B");
    WO_ISSUE(2, 1);
    C_READ(0);
#pragma unroll
    for (int u = 0; u < 8; ++u) {
      if (u + 3 < 8) WO_ISSUE(u + 3, CSLOT(u + 3));
      if (u + 1 < 8) {
        if (u + 1 >= 2) { const int last = (u + 3 < 8) ? (u + 3) : 7; const int rem = last - (u + 1);
          if (rem == 2) waitv<8>(); else if (rem == 1) waitv<4>(); else waitv<0>(); }
        C_READ(u + 1);
      }
      SCHED(); asm volatile("; B");
#pragma unroll
      for (int t = 0; t < 4; ++t)
#pragma unroll
        for (int qt = 0; qt < 3; ++qt) cacc[t][qt] = mfma(wf[u & 1][t], of[u & 1][qt], u == 0 ? f32x4{0.f, 0.f, 0.f, 0.f} : cacc[t][qt]);
      SCHED(); asm volatile("; B");
    }
    }
  }
  STAMP(19);
  const int s16 = kvh * 2 + hp;
  {
    auto rs = __builtin_amdgcn_make_buffer_rsrc((void*)(ws + WS_PART + ((size_t)(b * 16 + s16) * 8 + w) * 6144), 0, 6144, 0x00020000);
#pragma unroll
    for (int a = 0; a < 2; ++a)
#pragma unroll
      for (int qt = 0; qt < 3; ++qt)
        __builtin_amdgcn_raw_buffer_store_b128(__builtin_bit_cast(u32x4, cvt8(cacc[2 * a][qt], cacc[2 * a + 1][qt])), rs, l * 16 + (a * 3 + qt) * 1024, 0, 16);
  }
  asm volatile("s_waitcnt vmcnt(0)" ::: "memory");
  if (l == 0) __hip_atomic_store(flags + (w * 16 + s16) * 8, tag, __ATOMIC_RELAXED, __HIP_MEMORY_SCOPE_AGENT);
  if (w < 3) {
    for (unsigned spins = 0; spins < (1u << 22); ++spins) {
      const unsigned long long f = __hip_atomic_load(flags + ((s16 >> 1) * 16 + (l & 15)) * 8, __ATOMIC_RELAXED, __HIP_MEMORY_SCOPE_AGENT);
      if (__all(f == tag)) break;
      __builtin_amdgcn_s_sleep(1);
    }
    __builtin_amdgcn_fence(__ATOMIC_ACQUIRE, "wavefront");
    if (s16 == 0 && w == 0 && l == 0) __hip_atomic_store(epoch, tag, __ATOMIC_RELAXED, __HIP_MEMORY_SCOPE_AGENT);
    STAMP(21);
    const int rem = s16 * 192 + w * 64 + l;
    auto rs = __builtin_amdgcn_make_buffer_rsrc((void*)(ws + WS_PART + (size_t)b * 16 * 49152), 0, 16 * 49152, 0x00020000);
    u32x4 v[16];
#pragma unroll
    for (int sl = 0; sl < 16; ++sl) v[sl] = __builtin_amdgcn_raw_buffer_load_b128(rs, rem * 16, sl * 49152, 16);
    f32x4 a0 = {0.f, 0.f, 0.f, 0.f}, a1 = {0.f, 0.f, 0.f, 0.f};
#pragma unroll
    for (int sl = 0; sl < 16; ++sl) {
#pragma unroll
      for (int e = 0; e < 2; ++e) {
        a0[2 * e] += __uint_as_float(v[sl][e] << 16); a0[2 * e + 1] += __uint_as_float(v[sl][e] & 0xffff0000u);
        a1[2 * e] += __uint_as_float(v[sl][2 + e] << 16); a1[2 * e + 1] += __uint_as_float(v[sl][2 + e] & 0xffff0000u);
      }
    }
    const int tq = rem >> 6, qt = tq % 3, wa = tq / 3;
    const int q = 16 * qt + c, n = 32 * wa + 4 * g;
    __bf16* o = out + ((size_t)b * SQ + q) * DM + n;
    *(bf16x4*)o = cvt4(a0);
    *(bf16x4*)(o + 16) = cvt4(a1);
  }
}

extern "C" void kernel_launch(void* const* d_in, const int* in_sizes, int n_in, void* d_out, int out_size, void* d_ws, size_t ws_size,
                              hipStream_t stream) {
  const __bf16* x = (const __bf16*)d_in[0];
  const __bf16* kc = (const __bf16*)d_in[1];
  const __bf16* vc = (const __bf16*)d_in[2];
  const __bf16* wq = (const __bf16*)d_in[3];
  const __bf16* wo = (const __bf16*)d_in[4];
  (void)hipFuncSetAttribute((const void*)gqa_fused, hipFuncAttributeMaxDynamicSharedMemorySize, LDS_BYTES);
  hipLaunchKernelGGL(gqa_fused, dim3(256), dim3(512), LDS_BYTES, stream, x, kc, vc, wq, wo, (char*)d_ws, (__bf16*)d_out);
}
```

```cpp
#include <hip/hip_runtime.h>
#include <hip/hip_bf16.h>
#include <stdint.h>

typedef __bf16 bf16x8 __attribute__((ext_vector_type(8)));
typedef __bf16 bf16x4 __attribute__((ext_vector_type(4)));
typedef float f32x4 __attribute__((ext_vector_type(4)));
typedef float f32x2 __attribute__((ext_vector_type(2)));
typedef int i32x4 __attribute__((ext_vector_type(4)));
typedef unsigned u32x4 __attribute__((ext_vector_type(4)));
#define LDS_AS __attribute__((address_space(3)))
typedef LDS_AS char* ldsp;

constexpr int B_ = 16, SQ = 48, DM = 512, HQ = 32, HKV = 8, SK = 512, DH = 128;
constexpr float SCL2E = 0.08838834764831845f * 1.4426950408889634f;

constexpr int L_SLOT = 32768;
constexpr int L_RING = 32768;
constexpr int L_X = 114688;
constexpr int L_Q = 131072;
constexpr int L_ST1 = 155648;
constexpr int L_ST2 = 32768 + 24576;
constexpr int L_O2 = 2 * 32768;
constexpr int LDS_BYTES = 163840;
constexpr int WS_EPOCH = 0, WS_FLAGS = 4096, WS_PART = 4096 + 16 * 128 * 64;
constexpr int NRING = 5;
#define RESC_T 16.0f

#define PRIO 0
#define BAR() asm volatile("s_waitcnt lgkmcnt(0)\n\ts_barrier" ::: "memory")
#define SCHED() __builtin_amdgcn_sched_barrier(0)
#define STAMP(i)
template <int N> __device__ __forceinline__ void waitv() { asm volatile("s_waitcnt vmcnt(%0)" ::"i"(N) : "memory"); }

__device__ __forceinline__ void dma4(unsigned lds, unsigned v0, unsigned v1, unsigned v2, unsigned v3, i32x4 srd, unsigned soff) {
  asm volatile("s_mov_b32 m0, %0\n\ts_nop 0\n\t"
               "buffer_load_dwordx4 %1, %5, %6 offen lds\n\t"
               "buffer_load_dwordx4 %2, %5, %6 offen offset:1024 lds\n\t"
               "buffer_load_dwordx4 %3, %5, %6 offen offset:2048 lds\n\t"
               "buffer_load_dwordx4 %4, %5, %6 offen offset:3072 lds"
               ::"s"(lds), "v"(v0), "v"(v1), "v"(v2), "v"(v3), "s"(srd), "s"(soff) : "memory");
}
__device__ __forceinline__ void dma4nt(unsigned lds, unsigned v0, unsigned v1, unsigned v2, unsigned v3, i32x4 srd, unsigned soff) {
  asm volatile("s_mov_b32 m0, %0\n\ts_nop 0\n\t"
               "buffer_load_dwordx4 %1, %5, %6 offen nt lds\n\t"
               "buffer_load_dwordx4 %2, %5, %6 offen offset:1024 nt lds\n\t"
               "buffer_load_dwordx4 %3, %5, %6 offen offset:2048 nt lds\n\t"
               "buffer_load_dwordx4 %4, %5, %6 offen offset:3072 nt lds"
               ::"s"(lds), "v"(v0), "v"(v1), "v"(v2), "v"(v3), "s"(srd), "s"(soff) : "memory");
}
__device__ __forceinline__ i32x4 make_srd(const void* p, unsigned bytes) {
  unsigned long long a = (unsigned long long)p;
  i32x4 s;
  s[0] = (int)(unsigned)a; s[1] = (int)((unsigned)(a >> 32) & 0xffffu); s[2] = (int)bytes; s[3] = 0x00020000;
  return s;
}
__device__ __forceinline__ bf16x8 lds_rd128(ldsp p) { return *(LDS_AS bf16x8*)p; }
__device__ __forceinline__ bf16x8 lds_tr2(ldsp p0, ldsp p1) {
  bf16x4 a = __builtin_amdgcn_ds_read_tr16_b64_v4bf16((LDS_AS bf16x4*)p0);
  bf16x4 b = __builtin_amdgcn_ds_read_tr16_b64_v4bf16((LDS_AS bf16x4*)p1);
  return __builtin_shufflevector(a, b, 0, 1, 2, 3, 4, 5, 6, 7);
}
__device__ __forceinline__ f32x4 mfma(bf16x8 a, bf16x8 b, f32x4 c) { return __builtin_amdgcn_mfma_f32_16x16x32_bf16(a, b, c, 0, 0, 0); }
__device__ __forceinline__ bf16x4 cvt4(f32x4 v) {
  bf16x4 r; r[0] = (__bf16)v[0]; r[1] = (__bf16)v[1]; r[2] = (__bf16)v[2]; r[3] = (__bf16)v[3]; return r;
}
__device__ __forceinline__ bf16x8 cvt8(f32x4 a, f32x4 b) { return __builtin_shufflevector(cvt4(a), cvt4(b), 0, 1, 2, 3, 4, 5, 6, 7); }
__device__ __forceinline__ float max3(float a, float b, float c) { return __builtin_elementwise_maximum(__builtin_elementwise_maximum(a, b), c); }
__device__ __forceinline__ float max8(f32x4 a, f32x4 b) {
  return max3(max3(max3(a[0], a[1], a[2]), a[3], b[0]), max3(b[1], b[2], b[3]), b[3]);
}
__device__ __forceinline__ float xmax_g(float v) {
  auto r = __builtin_amdgcn_permlane16_swap(__float_as_uint(v), __float_as_uint(v), false, false);
  v = fmaxf(__uint_as_float(r[0]), __uint_as_float(r[1]));
  auto r2 = __builtin_amdgcn_permlane32_swap(__float_as_uint(v), __float_as_uint(v), false, false);
  return fmaxf(__uint_as_float(r2[0]), __uint_as_float(r2[1]));
}
__device__ __forceinline__ int xoff(int wd, int r, int qt) { return (r == 0 ? L_O2 : (r == 1 ? L_SLOT : 4 * L_SLOT)) + (wd * 3 + qt) * 1024; }

__global__ __launch_bounds__(512, 2) void gqa_fused(const __bf16* __restrict__ x, const __bf16* __restrict__ kc, const __bf16* __restrict__ vc,
                                                    const __bf16* __restrict__ wq, const __bf16* __restrict__ wo, char* ws, __bf16* __restrict__ out) {
  extern __shared__ __attribute__((aligned(16))) char smem_raw[];
  ldsp sm = (ldsp)smem_raw;
  const unsigned smbase = (unsigned)(uintptr_t)sm;
  const int w = __builtin_amdgcn_readfirstlane(threadIdx.x >> 6);
  const int bid = blockIdx.x, kvh = bid & 7, jj = bid >> 3, b = jj >> 1, hp = jj & 1;
  STAMP(0);
  const int n0 = kvh * 512 + hp * 256;
  const i32x4 srd_k = make_srd(kc + (size_t)(b * HKV + kvh) * SK * DH, SK * DH * 2);
  const i32x4 srd_v = make_srd(vc + (size_t)(b * HKV + kvh) * SK * DH, SK * DH * 2);
  const i32x4 srd_o = make_srd(wo + (size_t)n0 * DM, 256 * DM * 2);
  const int h = w >> 2, kq = w & 3;
  typedef __attribute__((address_space(1))) unsigned long long gu64;
  gu64* epoch = (gu64*)(ws + WS_EPOCH + b * 256);
  gu64* flags = (gu64*)(ws + WS_FLAGS + b * 128 * 64);
  const unsigned long long tag = __hip_atomic_load(epoch, __ATOMIC_RELAXED, __HIP_MEMORY_SCOPE_AGENT) + 1ull;
  f32x4 oacc[8][3];
  f32x4 lacc[3];
  float mref[3];
  {
    const int l = threadIdx.x & 63, c = l & 15, g = l >> 4, qq = c >> 2, pp = l & 3;
    const unsigned kv0 = (unsigned)(g * 256 + ((c ^ g) << 4));
    const unsigned vv0 = (unsigned)(g * 256 + ((c ^ (2 * g)) << 4));
    if (w < 4) {
      f32x4 qacc[4][3];
      const i32x4 srd_x = make_srd(x + (size_t)b * SQ * DM, SQ * DM * 2);
      const i32x4 srd_w = make_srd(wq, DM * HQ * DH * 2);
#pragma unroll
      for (int i = 0; i < 3; ++i) {
        const int r = w * 12 + 4 * i;
        dma4(smbase + L_X + r * 1024, (unsigned)((l ^ (r & 15)) * 16), (unsigned)((l ^ ((r + 1) & 15)) * 16), (unsigned)((l ^ ((r + 2) & 15)) * 16),
             (unsigned)((l ^ ((r + 3) & 15)) * 16), srd_x, (unsigned)(r * 1024));
      }
      const unsigned ring = smbase + L_RING + w * (NRING * 4096);
      const unsigned wv0 = (unsigned)((l >> 3) * 8192 + (((l & 7) ^ (((l >> 4) & 1) << 1)) * 16));
      const unsigned wv1 = (wv0 ^ 64u) + (65536u - 1024u), wv2 = wv0 + 2u * (65536u - 1024u), wv3 = (wv0 ^ 64u) + 3u * (65536u - 1024u);
      const unsigned scol = (unsigned)((n0 + 64 * w) * 2);
#define WQ_ISSUE(s) dma4(ring + ((s) % NRING) * 4096, wv0, wv1, wv2, wv3, srd_w, scol + (unsigned)((((s) + b) & 15) * (32 * 8192)))
      WQ_ISSUE(0); WQ_ISSUE(1); WQ_ISSUE(2); WQ_ISSUE(3);
      waitv<16>();
      STAMP(1); BAR(); STAMP(2);
      const int fA = ((qq >> 1) & 1) | ((g & 1) << 1);
      ldsp wqa = sm + L_RING + w * (NRING * 4096) + (8 * g + qq) * 128 + 8 * pp;
      ldsp xa = sm + L_X + c * 1024;
      bf16x8 fw[2][4], fx[2][3];
#define A_READ(s, buf)                                                                                          \
  do {                                                                                                          \
    _Pragma("unroll") for (int t_ = 0; t_ < 4; ++t_) {                                                          \
      ldsp p_ = wqa + ((s) % NRING) * 4096 + ((t_ ^ fA) << 5);                                                  \
      fw[buf][t_] = lds_tr2(p_, p_ + 512);                                                                      \
    }                                                                                                           \
    _Pragma("unroll") for (int q_ = 0; q_ < 3; ++q_)                                                            \
        fx[buf][q_] = lds_rd128(xa + q_ * 16384 + ((((4 * (((s) + b) & 15)) | g) ^ c) << 4));                   \
  } while (0)
      waitv<12>();
      A_READ(0, 0);
#pragma unroll
      for (int s = 0; s < 16; ++s) {
        if (s + 4 < 16) WQ_ISSUE(s + 4);
        if (s == 12) {
          dma4nt(smbase + (32 * w) * 256, kv0, kv0 ^ 64u, kv0 ^ 128u, kv0 ^ 192u, srd_k, (unsigned)(128 * w * 256));
          dma4nt(smbase + (32 * w + 16) * 256, kv0, kv0 ^ 64u, kv0 ^ 128u, kv0 ^ 192u, srd_k, (unsigned)((128 * w + 16) * 256));
        }
        if (s + 1 < 16) {
          const int rem = ((s + 4 < 16) ? (s + 4) : 15) - (s + 1);
          if (s < 12) waitv<12>(); else if (rem == 2) waitv<16>(); else if (rem == 1) waitv<12>(); else waitv<8>();
          A_READ(s + 1, (s + 1) & 1);
        }
        SCHED();
#pragma unroll
        for (int t = 0; t < 4; ++t)
#pragma unroll
          for (int qt = 0; qt < 3; ++qt) qacc[t][qt] = mfma(fw[s & 1][t], fx[s & 1][qt], s == 0 ? f32x4{0.f, 0.f, 0.f, 0.f} : qacc[t][qt]);
        SCHED();
      }
      STAMP(3); BAR(); STAMP(4);
#pragma unroll
      for (int t = 0; t < 4; ++t)
#pragma unroll
        for (int qt = 0; qt < 3; ++qt) {
          const int c16 = 8 * (w & 1) + 2 * t + (g >> 1);
          *(LDS_AS bf16x4*)(sm + L_Q + (48 * (w >> 1) + 16 * qt + c) * 256 + ((c16 ^ c) << 4) + 8 * (g & 1)) = cvt4(qacc[t][qt] * SCL2E);
        }
    } else {
      STAMP(1); BAR(); STAMP(2);
      STAMP(3); BAR(); STAMP(4);
    }
    const unsigned kvrow = (unsigned)(128 * kq * 256);
#define K_ISSUE(j, slot)                                                                                                                  \
  do { if (h == 0) {                                                                                                                      \
    dma4nt(smbase + (slot) * L_SLOT + kq * 8192, kv0, kv0 ^ 64u, kv0 ^ 128u, kv0 ^ 192u, srd_k, kvrow + (unsigned)((j) * 8192));          \
    dma4nt(smbase + (slot) * L_SLOT + kq * 8192 + 4096, kv0, kv0 ^ 64u, kv0 ^ 128u, kv0 ^ 192u, srd_k, kvrow + (unsigned)((j) * 8192 + 4096)); \
  } } while (0)
#define V_ISSUE(j, slot)                                                                                                                  \
  do { if (h == 0) {                                                                                                                      \
    dma4nt(smbase + (slot) * L_SLOT + kq * 8192, vv0, vv0 ^ 128u, vv0, vv0 ^ 128u, srd_v, kvrow + (unsigned)((j) * 8192));                \
    dma4nt(smbase + (slot) * L_SLOT + kq * 8192 + 4096, vv0, vv0 ^ 128u, vv0, vv0 ^ 128u, srd_v, kvrow + (unsigned)((j) * 8192 + 4096));  \
  } } while (0)
    const unsigned ov0 = (unsigned)((l >> 3) * 1024 + (((l & 7) ^ (g << 1)) << 4));
#define WO_ISSUE_W(u, slot, ww) dma4(smbase + (slot) * L_SLOT + (ww) * 4096, ov0, ov0 + 7168u, ov0 + 14336u, ov0 + 21504u, srd_o, (unsigned)((ww) * 128 + (((u) + b) & 7) * 32768))
#define WO_ISSUE(u, slot) WO_ISSUE_W(u, slot, w)
#define WO_ISSUE2(u, slot) do { if (h == 0) { WO_ISSUE_W(u, slot, w); WO_ISSUE_W(u, slot, w + 4); } } while (0)
    K_ISSUE(1, 1);
    waitv<8>();
    STAMP(5); BAR(); STAMP(6);

    bf16x8 qf[4][3];
#pragma unroll
    for (int s = 0; s < 4; ++s)
#pragma unroll
      for (int qt = 0; qt < 3; ++qt) qf[s][qt] = lds_rd128(sm + L_Q + (48 * h + 16 * qt + c) * 256 + ((((4 * s) | g) ^ c) << 4));
    V_ISSUE(0, 2);
    ldsp ka = sm + (32 * kq + c) * 256;
    const int fV = 4 * (g & 1) + qq;
    unsigned va[4];
#pragma unroll
    for (int r = 0; r < 4; ++r) va[r] = smbase + (32 * kq + 4 * g + qq) * 256 + 8 * pp + (((2 * ((r + kq) & 3)) ^ fV) << 5);
    bf16x8 ones;
#pragma unroll
    for (int e = 0; e < 8; ++e) ones[e] = (__bf16)1.0f;
    f32x4 negm[3];
    constexpr int kslot[4] = {0, 1, 3, 4}, vslot[4] = {2, 0, 1, 2};
#define SYNC(j)                                                               \
  do {                                                                        \
    if ((j) < 3) waitv<8>(); else waitv<16>();                                \
    if ((j) == 1 && w == 3) waitv<0>();                                       \
    BAR(); STAMP(7 + (j));                                                    \
    if ((j) == 0) { V_ISSUE(1, 0); }                                          \
    if ((j) == 1) { if (w == 3) K_ISSUE(3, 4); V_ISSUE(2, 1); }               \
    if ((j) == 2) { WO_ISSUE2(0, 3); }                                        \
  } while (0)
#pragma unroll
    for (int j = 0; j < 4; ++j) {
      f32x4 sacc[2][3];
      {
        unsigned kso = (unsigned)(kslot[j] * L_SLOT);
        asm volatile("" : "+s"(kso));
        bf16x8 kf[2][4];
#pragma unroll
        for (int s = 0; s < 4; ++s)
#pragma unroll
          for (int kt = 0; kt < 2; ++kt) kf[kt][s] = lds_rd128(ka + kso + kt * 4096 + ((((4 * s) | g) ^ c) << 4));
        SCHED();
#pragma unroll
        for (int s = 0; s < 4; ++s)
#pragma unroll
          for (int kt = 0; kt < 2; ++kt)
#pragma unroll
            for (int qt = 0; qt < 3; ++qt)
              sacc[kt][qt] = mfma(kf[kt][s], qf[s][qt], s == 0 ? (j == 0 ? f32x4{0.f, 0.f, 0.f, 0.f} : negm[qt]) : sacc[kt][qt]);
        SCHED();
        if (j == 0) K_ISSUE(2, 3);
      }
      bool resc = false;
      float al[3];
      if (j == 0) {
#pragma unroll
        for (int qt = 0; qt < 3; ++qt) {
          const float m = xmax_g(max8(sacc[0][qt], sacc[1][qt]));
          if (g == 0) *(LDS_AS float*)(sm + L_ST1 + ((w * 3 + qt) * 16 + c) * 4) = m;
        }
        SYNC(0);
#pragma unroll
        for (int qt = 0; qt < 3; ++qt) {
          float m = *(LDS_AS float*)(sm + L_ST1 + ((h * 12 + qt) * 16 + c) * 4);
#pragma unroll
          for (int k = 1; k < 4; ++k) m = fmaxf(m, *(LDS_AS float*)(sm + L_ST1 + ((h * 12 + 3 * k + qt) * 16 + c) * 4));
          mref[qt] = m;
          negm[qt] = f32x4{-m, -m, -m, -m};
          sacc[0][qt] += negm[qt];
          sacc[1][qt] += negm[qt];
        }
      } else {
        if (h == 0) SYNC(j);
        float t = max3(sacc[0][0][0], sacc[0][0][1], sacc[0][0][2]);
        t = max3(t, sacc[0][0][3], sacc[1][0][0]); t = max3(t, sacc[1][0][1], sacc[1][0][2]);
        t = max3(t, sacc[1][0][3], sacc[0][1][0]); t = max3(t, sacc[0][1][1], sacc[0][1][2]);
        t = max3(t, sacc[0][1][3], sacc[1][1][0]); t = max3(t, sacc[1][1][1], sacc[1][1][2]);
        t = max3(t, sacc[1][1][3], sacc[0][2][0]); t = max3(t, sacc[0][2][1], sacc[0][2][2]);
        t = max3(t, sacc[0][2][3], sacc[1][2][0]); t = max3(t, sacc[1][2][1], sacc[1][2][2]);
        t = max3(t, sacc[1][2][3], t);
        resc = __any(!(t <= RESC_T));
        if (__builtin_expect(resc, 0)) {
#pragma unroll
          for (int qt = 0; qt < 3; ++qt) {
            const float d = fmaxf(xmax_g(max8(sacc[0][qt], sacc[1][qt])), 0.f);
            al[qt] = __builtin_amdgcn_exp2f(-d);
            mref[qt] += d;
            negm[qt] -= d;
            sacc[0][qt] -= d;
            sacc[1][qt] -= d;
          }
        }
      }
      bf16x8 pf[3];
#pragma unroll
      for (int qt = 0; qt < 3; ++qt) {
#pragma unroll
        for (int kt = 0; kt < 2; ++kt)
#pragma unroll
          for (int r = 0; r < 4; ++r) sacc[kt][qt][r] = __builtin_amdgcn_exp2f(sacc[kt][qt][r]);
        pf[qt] = cvt8(sacc[0][qt], sacc[1][qt]);
        asm volatile("" : "+v"(pf[qt]));
      }
      SCHED();
      if (j > 0) {
        if (h == 1) SYNC(j);
        if (__builtin_expect(resc, 0)) {
#pragma unroll
          for (int qt = 0; qt < 3; ++qt) {
            lacc[qt] *= al[qt];
#pragma unroll
            for (int i = 0; i < 8; ++i) oacc[i][qt] *= al[qt];
          }
        }
      }
      {
        unsigned vso = (unsigned)(vslot[j] * L_SLOT);
        asm volatile("" : "+s"(vso));
        bf16x8 vf[8];
#pragma unroll
        for (int i = 0; i < 8; ++i) {
          ldsp p = (ldsp)((va[i >> 1] ^ (unsigned)((i & 1) << 5)) + vso);
          vf[i] = lds_tr2(p, p + 4096);
        }
        SCHED();
#pragma unroll
        for (int qt = 0; qt < 3; ++qt) lacc[qt] = mfma(ones, pf[qt], j == 0 ? f32x4{0.f, 0.f, 0.f, 0.f} : lacc[qt]);
#pragma unroll
        for (int i = 0; i < 8; ++i)
#pragma unroll
          for (int qt = 0; qt < 3; ++qt) oacc[i][qt] = mfma(vf[i], pf[qt], j == 0 ? f32x4{0.f, 0.f, 0.f, 0.f} : oacc[i][qt]);
        SCHED();
        if (j == 0 && w != 3) K_ISSUE(3, 4);
        if (j == 1) V_ISSUE(3, 2);
        if (j == 2) WO_ISSUE2(1, 0);
      }
    }
    if (g == 0) {
#pragma unroll
      for (int qt = 0; qt < 3; ++qt) *(LDS_AS f32x2*)(sm + L_ST2 + ((w * 3 + qt) * 16 + c) * 8) = f32x2{mref[qt], lacc[qt][0]};
    }
    BAR(); STAMP(11);
  }
  int tz = threadIdx.x;
  asm volatile("" : "+v"(tz));
  const int l = tz & 63, c = l & 15, g = l >> 4, qq = c >> 2, pp = l & 3;
  {
    float inv[3], sc[3];
    bool differ = false;
#pragma unroll
    for (int qt = 0; qt < 3; ++qt) {
      f32x2 ml[4];
#pragma unroll
      for (int k = 0; k < 4; ++k) ml[k] = *(LDS_AS f32x2*)(sm + L_ST2 + ((h * 12 + 3 * k + qt) * 16 + c) * 8);
      const float m = fmaxf(fmaxf(ml[0][0], ml[1][0]), fmaxf(ml[2][0], ml[3][0]));
      float lt = 0.f;
#pragma unroll
      for (int k = 0; k < 4; ++k) lt += ml[k][1] * __builtin_amdgcn_exp2f(ml[k][0] - m);
      inv[qt] = __builtin_amdgcn_rcpf(lt);
      sc[qt] = __builtin_amdgcn_exp2f(mref[qt] - m);
      differ = differ || (mref[qt] != m);
    }
    if (__builtin_expect(__any(differ), 0)) {
#pragma unroll
      for (int qt = 0; qt < 3; ++qt)
#pragma unroll
        for (int i = 0; i < 8; ++i) oacc[i][qt] *= sc[qt];
    }
#pragma unroll
    for (int r = 1; r < 4; ++r) {
      const int wd = h * 4 + ((kq + r) & 3);
#pragma unroll
      for (int qt = 0; qt < 3; ++qt) *(LDS_AS bf16x8*)(sm + xoff(wd, r - 1, qt) + l * 16) = cvt8(oacc[2 * r][qt], oacc[2 * r + 1][qt]);
    }
    if (tz < 4) *(LDS_AS unsigned*)(sm + L_O2 + 24576 + tz * 4) = 0u;
    bf16x8 sel0, sel1;
#pragma unroll
    for (int e = 0; e < 4; ++e) {
      const __bf16 v = (4 * g + e == c) ? (__bf16)1.0f : (__bf16)0.0f;
      sel0[e] = v; sel0[4 + e] = (__bf16)0.0f; sel1[e] = (__bf16)0.0f; sel1[4 + e] = v;
    }
    BAR(); STAMP(17);
#pragma unroll
    for (int qt = 0; qt < 3; ++qt) {
#pragma unroll
      for (int r = 0; r < 3; ++r) {
        bf16x8 t = lds_rd128(sm + xoff(w, r, qt) + l * 16);
        oacc[0][qt] = mfma(sel0, t, oacc[0][qt]);
        oacc[1][qt] = mfma(sel1, t, oacc[1][qt]);
      }
    }
#pragma unroll
    for (int qt = 0; qt < 3; ++qt) *(LDS_AS bf16x8*)(sm + L_O2 + (w * 3 + qt) * 1024 + l * 16) = cvt8(oacc[0][qt] * inv[qt], oacc[1][qt] * inv[qt]);
  }
  waitv<0>();
  BAR(); STAMP(18);
  const unsigned ov0 = (unsigned)((l >> 3) * 1024 + (((l & 7) ^ (g << 1)) << 4));

  f32x4 cacc[4][3];
  {
    bf16x8 of[2][3];
    const int fC = 2 * (g & 1) + (qq >> 1);
    ldsp wra = sm + w * 4096 + (4 * g + qq) * 128 + 8 * pp;
    bf16x8 wf[2][4];
#define CSLOT(u) (((u) & 3) == 0 ? 3 : (((u) & 3) == 1 ? 0 : (((u) & 3) == 2 ? 1 : 4)))
#define C_READ(u)                                                                                                   \
  do {                                                                                                              \
    _Pragma("unroll") for (int t_ = 0; t_ < 4; ++t_) {                                                              \
      ldsp p_ = wra + CSLOT(u) * L_SLOT + ((t_ ^ fC) << 5);                                                         \
      wf[(u) & 1][t_] = lds_tr2(p_, p_ + 2048);                                                                     \
    }                                                                                                               \
    _Pragma("unroll") for (int q_ = 0; q_ < 3; ++q_)                                                                \
        of[(u) & 1][q_] = lds_rd128(sm + L_O2 + ((((u) + b) & 7) * 3 + q_) * 1024 + l * 16);                        \
  } while (0)
    volatile LDS_AS unsigned* oflag = (volatile LDS_AS unsigned*)(sm + L_O2 + 24576 + kq * 4);
    if (h == 0) { WO_ISSUE(2, 1); WO_ISSUE(3, 4); }
    C_READ(0);
#pragma unroll
    for (int u = 0; u < 8; ++u) {
      if (u == 1 && h == 1) {
        while (*oflag == 0u) __builtin_amdgcn_s_sleep(1);
        WO_ISSUE(2, 1); WO_ISSUE(3, 4);
      }
      if (u >= 1 && u + 3 < 8) WO_ISSUE(u + 3, CSLOT(u + 3));
      if (u == 4 && h == 0) { if (l == 0) *oflag = 1u; }
      if (u + 1 < 8) {
        if (u + 1 >= 2) { const int last = (u + 3 < 8) ? (u + 3) : 7; const int rem = last - (u + 1);
          if (rem == 2) waitv<8>(); else if (rem == 1) waitv<4>(); else waitv<0>(); }
        C_READ(u + 1);
      }
      SCHED();
#pragma unroll
      for (int t = 0; t < 4; ++t)
#pragma unroll
        for (int qt = 0; qt < 3; ++qt) cacc[t][qt] = mfma(wf[u & 1][t], of[u & 1][qt], u == 0 ? f32x4{0.f, 0.f, 0.f, 0.f} : cacc[t][qt]);
      SCHED();
    }
  }
  STAMP(19);
  const int s16 = kvh * 2 + hp;
  {
    auto rs = __builtin_amdgcn_make_buffer_rsrc((void*)(ws + WS_PART + ((size_t)(b * 16 + s16) * 8 + w) * 6144), 0, 6144, 0x00020000);
#pragma unroll
    for (int a = 0; a < 2; ++a)
#pragma unroll
      for (int qt = 0; qt < 3; ++qt)
        __builtin_amdgcn_raw_buffer_store_b128(__builtin_bit_cast(u32x4, cvt8(cacc[2 * a][qt], cacc[2 * a + 1][qt])), rs, l * 16 + (a * 3 + qt) * 1024, 0, 16);
  }
  asm volatile("s_waitcnt vmcnt(0)" ::: "memory");
  if (l == 0) __hip_atomic_store(flags + (w * 16 + s16) * 8, tag, __ATOMIC_RELAXED, __HIP_MEMORY_SCOPE_AGENT);
  if (w < 3) {
    for (unsigned spins = 0; spins < (1u << 22); ++spins) {
      const unsigned long long f = __hip_atomic_load(flags + ((s16 >> 1) * 16 + (l & 15)) * 8, __ATOMIC_RELAXED, __HIP_MEMORY_SCOPE_AGENT);
      if (__all(f == tag)) break;
      __builtin_amdgcn_s_sleep(1);
    }
    __builtin_amdgcn_fence(__ATOMIC_ACQUIRE, "wavefront");
    if (s16 == 0 && w == 0 && l == 0) __hip_atomic_store(epoch, tag, __ATOMIC_RELAXED, __HIP_MEMORY_SCOPE_AGENT);
    STAMP(21);
    const int rem = s16 * 192 + w * 64 + l;
    auto rs = __builtin_amdgcn_make_buffer_rsrc((void*)(ws + WS_PART + (size_t)b * 16 * 49152), 0, 16 * 49152, 0x00020000);
    u32x4 v[16];
#pragma unroll
    for (int sl = 0; sl < 16; ++sl) v[sl] = __builtin_amdgcn_raw_buffer_load_b128(rs, rem * 16, sl * 49152, 16);
    f32x4 a0 = {0.f, 0.f, 0.f, 0.f}, a1 = {0.f, 0.f, 0.f, 0.f};
#pragma unroll
    for (int sl = 0; sl < 16; ++sl) {
#pragma unroll
      for (int e = 0; e < 2; ++e) {
        a0[2 * e] += __uint_as_float(v[sl][e] << 16); a0[2 * e + 1] += __uint_as_float(v[sl][e] & 0xffff0000u);
        a1[2 * e] += __uint_as_float(v[sl][2 + e] << 16); a1[2 * e + 1] += __uint_as_float(v[sl][2 + e] & 0xffff0000u);
      }
    }
    const int tq = rem >> 6, qt = tq % 3, wa = tq / 3;
    const int q = 16 * qt + c, n = 32 * wa + 4 * g;
    __bf16* o = out + ((size_t)b * SQ + q) * DM + n;
    *(bf16x4*)o = cvt4(a0);
    *(bf16x4*)(o + 16) = cvt4(a1);
  }
}

extern "C" void kernel_launch(void* const* d_in, const int* in_sizes, int n_in, void* d_out, int out_size, void* d_ws, size_t ws_size,
                              hipStream_t stream) {
  const __bf16* x = (const __bf16*)d_in[0];
  const __bf16* kc = (const __bf16*)d_in[1];
  const __bf16* vc = (const __bf16*)d_in[2];
  const __bf16* wq = (const __bf16*)d_in[3];
  const __bf16* wo = (const __bf16*)d_in[4];
  (void)hipFuncSetAttribute((const void*)gqa_fused, hipFuncAttributeMaxDynamicSharedMemorySize, LDS_BYTES);
  hipLaunchKernelGGL(gqa_fused, dim3(256), dim3(512), LDS_BYTES, stream, x, kc, vc, wq, wo, (char*)d_ws, (__bf16*)d_out);
}
```

```cpp
#include <hip/hip_runtime.h>
#include <hip/hip_bf16.h>
#include <stdint.h>

typedef __bf16 bf16x8 __attribute__((ext_vector_type(8)));
typedef __bf16 bf16x4 __attribute__((ext_vector_type(4)));
typedef float f32x4 __attribute__((ext_vector_type(4)));
typedef float f32x2 __attribute__((ext_vector_type(2)));
typedef int i32x4 __attribute__((ext_vector_type(4)));
typedef unsigned u32x4 __attribute__((ext_vector_type(4)));
#define LDS_AS __attribute__((address_space(3)))
typedef LDS_AS char* ldsp;

constexpr int B_ = 16, SQ = 48, DM = 512, HQ = 32, HKV = 8, SK = 512, DH = 128;
constexpr float SCL2E = 0.08838834764831845f * 1.4426950408889634f;

constexpr int L_SLOT = 32768;
constexpr int L_RING = 32768;
constexpr int L_X = 114688;
constexpr int L_Q = 131072;
constexpr int L_ST1 = 155648;
constexpr int L_ST2 = 32768 + 24576;
constexpr int L_O2 = 2 * 32768;
constexpr int LDS_BYTES = 163840;
constexpr int WS_EPOCH = 0, WS_FLAGS = 4096, WS_PART = 4096 + 16 * 128 * 64;
constexpr int NRING = 5;
#define RESC_T 16.0f

#define PRIO 1
#define BAR() asm volatile("s_waitcnt lgkmcnt(0)\n\ts_barrier" ::: "memory")
#define SCHED() __builtin_amdgcn_sched_barrier(0)
#define STAMP(i)
template <int N> __device__ __forceinline__ void waitv() { asm volatile("s_waitcnt vmcnt(%0)" ::"i"(N) : "memory"); }

__device__ __forceinline__ void dma4(unsigned lds, unsigned v0, unsigned v1, unsigned v2, unsigned v3, i32x4 srd, unsigned soff) {
  asm volatile("s_mov_b32 m0, %0\n\ts_nop 0\n\t"
               "buffer_load_dwordx4 %1, %5, %6 offen lds\n\t"
               "buffer_load_dwordx4 %2, %5, %6 offen offset:1024 lds\n\t"
               "buffer_load_dwordx4 %3, %5, %6 offen offset:2048 lds\n\t"
               "buffer_load_dwordx4 %4, %5, %6 offen offset:3072 lds"
               ::"s"(lds), "v"(v0), "v"(v1), "v"(v2), "v"(v3), "s"(srd), "s"(soff) : "memory");
}
__device__ __forceinline__ void dma4nt(unsigned lds, unsigned v0, unsigned v1, unsigned v2, unsigned v3, i32x4 srd, unsigned soff) {
  asm volatile("s_mov_b32 m0, %0\n\ts_nop 0\n\t"
               "buffer_load_dwordx4 %1, %5, %6 offen nt lds\n\t"
               "buffer_load_dwordx4 %2, %5, %6 offen offset:1024 nt lds\n\t"
               "buffer_load_dwordx4 %3, %5, %6 offen offset:2048 nt lds\n\t"
               "buffer_load_dwordx4 %4, %5, %6 offen offset:3072 nt lds"
               ::"s"(lds), "v"(v0), "v"(v1), "v"(v2), "v"(v3), "s"(srd), "s"(soff) : "memory");
}
__device__ __forceinline__ i32x4 make_srd(const void* p, unsigned bytes) {
  unsigned long long a = (unsigned long long)p;
  i32x4 s;
  s[0] = (int)(unsigned)a; s[1] = (int)((unsigned)(a >> 32) & 0xffffu); s[2] = (int)bytes; s[3] = 0x00020000;
  return s;
}
__device__ __forceinline__ bf16x8 lds_rd128(ldsp p) { return *(LDS_AS bf16x8*)p; }
__device__ __forceinline__ bf16x8 lds_tr2(ldsp p0, ldsp p1) {
  bf16x4 a = __builtin_amdgcn_ds_read_tr16_b64_v4bf16((LDS_AS bf16x4*)p0);
  bf16x4 b = __builtin_amdgcn_ds_read_tr16_b64_v4bf16((LDS_AS bf16x4*)p1);
  return __builtin_shufflevector(a, b, 0, 1, 2, 3, 4, 5, 6, 7);
}
__device__ __forceinline__ f32x4 mfma(bf16x8 a, bf16x8 b, f32x4 c) { return __builtin_amdgcn_mfma_f32_16x16x32_bf16(a, b, c, 0, 0, 0); }
__device__ __forceinline__ bf16x4 cvt4(f32x4 v) {
  bf16x4 r; r[0] = (__bf16)v[0]; r[1] = (__bf16)v[1]; r[2] = (__bf16)v[2]; r[3] = (__bf16)v[3]; return r;
}
__device__ __forceinline__ bf16x8 cvt8(f32x4 a, f32x4 b) { return __builtin_shufflevector(cvt4(a), cvt4(b), 0, 1, 2, 3, 4, 5, 6, 7); }
__device__ __forceinline__ float max3(float a, float b, float c) { return __builtin_elementwise_maximum(__builtin_elementwise_maximum(a, b), c); }
__device__ __forceinline__ float max8(f32x4 a, f32x4 b) {
  return max3(max3(max3(a[0], a[1], a[2]), a[3], b[0]), max3(b[1], b[2], b[3]), b[3]);
}
__device__ __forceinline__ float xmax_g(float v) {
  auto r = __builtin_amdgcn_permlane16_swap(__float_as_uint(v), __float_as_uint(v), false, false);
  v = fmaxf(__uint_as_float(r[0]), __uint_as_float(r[1]));
  auto r2 = __builtin_amdgcn_permlane32_swap(__float_as_uint(v), __float_as_uint(v), false, false);
  return fmaxf(__uint_as_float(r2[0]), __uint_as_float(r2[1]));
}
__device__ __forceinline__ int xoff(int wd, int r, int qt) { return (r == 0 ? L_O2 : (r == 1 ? L_SLOT : 4 * L_SLOT)) + (wd * 3 + qt) * 1024; }

__global__ __launch_bounds__(512, 2) void gqa_fused(const __bf16* __restrict__ x, const __bf16* __restrict__ kc, const __bf16* __restrict__ vc,
                                                    const __bf16* __restrict__ wq, const __bf16* __restrict__ wo, char* ws, __bf16* __restrict__ out) {
  extern __shared__ __attribute__((aligned(16))) char smem_raw[];
  ldsp sm = (ldsp)smem_raw;
  const unsigned smbase = (unsigned)(uintptr_t)sm;
  const int w = __builtin_amdgcn_readfirstlane(threadIdx.x >> 6);
  const int bid = blockIdx.x, kvh = bid & 7, jj = bid >> 3, b = jj >> 1, hp = jj & 1;
  STAMP(0);
  const int n0 = kvh * 512 + hp * 256;
  const i32x4 srd_k = make_srd(kc + (size_t)(b * HKV + kvh) * SK * DH, SK * DH * 2);
  const i32x4 srd_v = make_srd(vc + (size_t)(b * HKV + kvh) * SK * DH, SK * DH * 2);
  const i32x4 srd_o = make_srd(wo + (size_t)n0 * DM, 256 * DM * 2);
  const int h = w >> 2, kq = w & 3;
  if (w >= 4) __builtin_amdgcn_s_setprio(1);
  typedef __attribute__((address_space(1))) unsigned long long gu64;
  gu64* epoch = (gu64*)(ws + WS_EPOCH + b * 256);
  gu64* flags = (gu64*)(ws + WS_FLAGS + b * 128 * 64);
  const unsigned long long tag = __hip_atomic_load(epoch, __ATOMIC_RELAXED, __HIP_MEMORY_SCOPE_AGENT) + 1ull;
  f32x4 oacc[8][3];
  f32x4 lacc[3];
  float mref[3];
  {
    const int l = threadIdx.x & 63, c = l & 15, g = l >> 4, qq = c >> 2, pp = l & 3;
    const unsigned kv0 = (unsigned)(g * 256 + ((c ^ g) << 4));
    const unsigned vv0 = (unsigned)(g * 256 + ((c ^ (2 * g)) << 4));
    if (w < 4) {
      f32x4 qacc[4][3];
      const i32x4 srd_x = make_srd(x + (size_t)b * SQ * DM, SQ * DM * 2);
      const i32x4 srd_w = make_srd(wq, DM * HQ * DH * 2);
#pragma unroll
      for (int i = 0; i < 3; ++i) {
        const int r = w * 12 + 4 * i;
        dma4(smbase + L_X + r * 1024, (unsigned)((l ^ (r & 15)) * 16), (unsigned)((l ^ ((r + 1) & 15)) * 16), (unsigned)((l ^ ((r + 2) & 15)) * 16),
             (unsigned)((l ^ ((r + 3) & 15)) * 16), srd_x, (unsigned)(r * 1024));
      }
      const unsigned ring = smbase + L_RING + w * (NRING * 4096);
      const unsigned wv0 = (unsigned)((l >> 3) * 8192 + (((l & 7) ^ (((l >> 4) & 1) << 1)) * 16));
      const unsigned wv1 = (wv0 ^ 64u) + (65536u - 1024u), wv2 = wv0 + 2u * (65536u - 1024u), wv3 = (wv0 ^ 64u) + 3u * (65536u - 1024u);
      const unsigned scol = (unsigned)((n0 + 64 * w) * 2);
#define WQ_ISSUE(s) dma4(ring + ((s) % NRING) * 4096, wv0, wv1, wv2, wv3, srd_w, scol + (unsigned)((((s) + b) & 15) * (32 * 8192)))
      WQ_ISSUE(0); WQ_ISSUE(1); WQ_ISSUE(2); WQ_ISSUE(3);
      waitv<16>();
      STAMP(1); BAR(); STAMP(2);
      const int fA = ((qq >> 1) & 1) | ((g & 1) << 1);
      ldsp wqa = sm + L_RING + w * (NRING * 4096) + (8 * g + qq) * 128 + 8 * pp;
      ldsp xa = sm + L_X + c * 1024;
      bf16x8 fw[2][4], fx[2][3];
#define A_READ(s, buf)                                                                                          \
  do {                                                                                                          \
    _Pragma("unroll") for (int t_ = 0; t_ < 4; ++t_) {                                                          \
      ldsp p_ = wqa + ((s) % NRING) * 4096 + ((t_ ^ fA) << 5);                                                  \
      fw[buf][t_] = lds_tr2(p_, p_ + 512);                                                                      \
    }                                                                                                           \
    _Pragma("unroll") for (int q_ = 0; q_ < 3; ++q_)                                                            \
        fx[buf][q_] = lds_rd128(xa + q_ * 16384 + ((((4 * (((s) + b) & 15)) | g) ^ c) << 4));                   \
  } while (0)
      waitv<12>();
      A_READ(0, 0);
#pragma unroll
      for (int s = 0; s < 16; ++s) {
        if (s + 4 < 16) WQ_ISSUE(s + 4);
        if (s == 12) {
          dma4nt(smbase + (32 * w) * 256, kv0, kv0 ^ 64u, kv0 ^ 128u, kv0 ^ 192u, srd_k, (unsigned)(128 * w * 256));
          dma4nt(smbase + (32 * w + 16) * 256, kv0, kv0 ^ 64u, kv0 ^ 128u, kv0 ^ 192u, srd_k, (unsigned)((128 * w + 16) * 256));
        }
        if (s + 1 < 16) {
          const int rem = ((s + 4 < 16) ? (s + 4) : 15) - (s + 1);
          if (s < 12) waitv<12>(); else if (rem == 2) waitv<16>(); else if (rem == 1) waitv<12>(); else waitv<8>();
          A_READ(s + 1, (s + 1) & 1);
        }
        SCHED();
#pragma unroll
        for (int t = 0; t < 4; ++t)
#pragma unroll
          for (int qt = 0; qt < 3; ++qt) qacc[t][qt] = mfma(fw[s & 1][t], fx[s & 1][qt], s == 0 ? f32x4{0.f, 0.f, 0.f, 0.f} : qacc[t][qt]);
        SCHED();
      }
      STAMP(3); BAR(); STAMP(4);
#pragma unroll
      for (int t = 0; t < 4; ++t)
#pragma unroll
        for (int qt = 0; qt < 3; ++qt) {
          const int c16 = 8 * (w & 1) + 2 * t + (g >> 1);
          *(LDS_AS bf16x4*)(sm + L_Q + (48 * (w >> 1) + 16 * qt + c) * 256 + ((c16 ^ c) << 4) + 8 * (g & 1)) = cvt4(qacc[t][qt] * SCL2E);
        }
    } else {
      STAMP(1); BAR(); STAMP(2);
      STAMP(3); BAR(); STAMP(4);
    }
    const unsigned kvrow = (unsigned)(128 * kq * 256);
#define K_ISSUE(j, slot)                                                                                                                  \
  do { if (h == 0) {                                                                                                                      \
    dma4nt(smbase + (slot) * L_SLOT + kq * 8192, kv0, kv0 ^ 64u, kv0 ^ 128u, kv0 ^ 192u, srd_k, kvrow + (unsigned)((j) * 8192));          \
    dma4nt(smbase + (slot) * L_SLOT + kq * 8192 + 4096, kv0, kv0 ^ 64u, kv0 ^ 128u, kv0 ^ 192u, srd_k, kvrow + (unsigned)((j) * 8192 + 4096)); \
  } } while (0)
#define V_ISSUE(j, slot)                                                                                                                  \
  do { if (h == 0) {                                                                                                                      \
    dma4nt(smbase + (slot) * L_SLOT + kq * 8192, vv0, vv0 ^ 128u, vv0, vv0 ^ 128u, srd_v, kvrow + (unsigned)((j) * 8192));                \
    dma4nt(smbase + (slot) * L_SLOT + kq * 8192 + 4096, vv0, vv0 ^ 128u, vv0, vv0 ^ 128u, srd_v, kvrow + (unsigned)((j) * 8192 + 4096));  \
  } } while (0)
    const unsigned ov0 = (unsigned)((l >> 3) * 1024 + (((l & 7) ^ (g << 1)) << 4));
#define WO_ISSUE_W(u, slot, ww) dma4(smbase + (slot) * L_SLOT + (ww) * 4096, ov0, ov0 + 7168u, ov0 + 14336u, ov0 + 21504u, srd_o, (unsigned)((ww) * 128 + (((u) + b) & 7) * 32768))
#define WO_ISSUE(u, slot) WO_ISSUE_W(u, slot, w)
#define WO_ISSUE2(u, slot) do { if (h == 0) { WO_ISSUE_W(u, slot, w); WO_ISSUE_W(u, slot, w + 4); } } while (0)
    K_ISSUE(1, 1);
    waitv<8>();
    STAMP(5); BAR(); STAMP(6);

    bf16x8 qf[4][3];
#pragma unroll
    for (int s = 0; s < 4; ++s)
#pragma unroll
      for (int qt = 0; qt < 3; ++qt) qf[s][qt] = lds_rd128(sm + L_Q + (48 * h + 16 * qt + c) * 256 + ((((4 * s) | g) ^ c) << 4));
    V_ISSUE(0, 2);
    ldsp ka = sm + (32 * kq + c) * 256;
    const int fV = 4 * (g & 1) + qq;
    unsigned va[4];
#pragma unroll
    for (int r = 0; r < 4; ++r) va[r] = smbase + (32 * kq + 4 * g + qq) * 256 + 8 * pp + (((2 * ((r + kq) & 3)) ^ fV) << 5);
    bf16x8 ones;
#pragma unroll
    for (int e = 0; e < 8; ++e) ones[e] = (__bf16)1.0f;
    f32x4 negm[3];
    constexpr int kslot[4] = {0, 1, 3, 4}, vslot[4] = {2, 0, 1, 2};
#define SYNC(j)                                                               \
  do {                                                                        \
    if ((j) < 3) waitv<8>(); else waitv<16>();                                \
    if ((j) == 1 && w == 3) waitv<0>();                                       \
    BAR(); STAMP(7 + (j));                                                    \
    if ((j) == 0) { V_ISSUE(1, 0); }                                          \
    if ((j) == 1) { if (w == 3) K_ISSUE(3, 4); V_ISSUE(2, 1); }               \
    if ((j) == 2) { WO_ISSUE2(0, 3); }                                        \
  } while (0)
#pragma unroll
    for (int j = 0; j < 4; ++j) {
      f32x4 sacc[2][3];
      {
        unsigned kso = (unsigned)(kslot[j] * L_SLOT);
        asm volatile("" : "+s"(kso));
        bf16x8 kf[2][4];
#pragma unroll
        for (int s = 0; s < 4; ++s)
#pragma unroll
          for (int kt = 0; kt < 2; ++kt) kf[kt][s] = lds_rd128(ka + kso + kt * 4096 + ((((4 * s) | g) ^ c) << 4));
        SCHED();
#pragma unroll
        for (int s = 0; s < 4; ++s)
#pragma unroll
          for (int kt = 0; kt < 2; ++kt)
#pragma unroll
            for (int qt = 0; qt < 3; ++qt)
              sacc[kt][qt] = mfma(kf[kt][s], qf[s][qt], s == 0 ? (j == 0 ? f32x4{0.f, 0.f, 0.f, 0.f} : negm[qt]) : sacc[kt][qt]);
        SCHED();
        if (j == 0) K_ISSUE(2, 3);
      }
      bool resc = false;
      float al[3];
      if (j == 0) {
#pragma unroll
        for (int qt = 0; qt < 3; ++qt) {
          const float m = xmax_g(max8(sacc[0][qt], sacc[1][qt]));
          if (g == 0) *(LDS_AS float*)(sm + L_ST1 + ((w * 3 + qt) * 16 + c) * 4) = m;
        }
        SYNC(0);
#pragma unroll
        for (int qt = 0; qt < 3; ++qt) {
          float m = *(LDS_AS float*)(sm + L_ST1 + ((h * 12 + qt) * 16 + c) * 4);
#pragma unroll
          for (int k = 1; k < 4; ++k) m = fmaxf(m, *(LDS_AS float*)(sm + L_ST1 + ((h * 12 + 3 * k + qt) * 16 + c) * 4));
          mref[qt] = m;
          negm[qt] = f32x4{-m, -m, -m, -m};
          sacc[0][qt] += negm[qt];
          sacc[1][qt] += negm[qt];
        }
      } else {
        if (h == 0) SYNC(j);
        float t = max3(sacc[0][0][0], sacc[0][0][1], sacc[0][0][2]);
        t = max3(t, sacc[0][0][3], sacc[1][0][0]); t = max3(t, sacc[1][0][1], sacc[1][0][2]);
        t = max3(t, sacc[1][0][3], sacc[0][1][0]); t = max3(t, sacc[0][1][1], sacc[0][1][2]);
        t = max3(t, sacc[0][1][3], sacc[1][1][0]); t = max3(t, sacc[1][1][1], sacc[1][1][2]);
        t = max3(t, sacc[1][1][3], sacc[0][2][0]); t = max3(t, sacc[0][2][1], sacc[0][2][2]);
        t = max3(t, sacc[0][2][3], sacc[1][2][0]); t = max3(t, sacc[1][2][1], sacc[1][2][2]);
        t = max3(t, sacc[1][2][3], t);
        resc = __any(!(t <= RESC_T));
        if (__builtin_expect(resc, 0)) {
#pragma unroll
          for (int qt = 0; qt < 3; ++qt) {
            const float d = fmaxf(xmax_g(max8(sacc[0][qt], sacc[1][qt])), 0.f);
            al[qt] = __builtin_amdgcn_exp2f(-d);
            mref[qt] += d;
            negm[qt] -= d;
            sacc[0][qt] -= d;
            sacc[1][qt] -= d;
          }
        }
      }
      bf16x8 pf[3];
#pragma unroll
      for (int qt = 0; qt < 3; ++qt) {
#pragma unroll
        for (int kt = 0; kt < 2; ++kt)
#pragma unroll
          for (int r = 0; r < 4; ++r) sacc[kt][qt][r] = __builtin_amdgcn_exp2f(sacc[kt][qt][r]);
        pf[qt] = cvt8(sacc[0][qt], sacc[1][qt]);
        asm volatile("" : "+v"(pf[qt]));
      }
      SCHED();
      if (j > 0) {
        if (h == 1) SYNC(j);
        if (__builtin_expect(resc, 0)) {
#pragma unroll
          for (int qt = 0; qt < 3; ++qt) {
            lacc[qt] *= al[qt];
#pragma unroll
            for (int i = 0; i < 8; ++i) oacc[i][qt] *= al[qt];
          }
        }
      }
      {
        unsigned vso = (unsigned)(vslot[j] * L_SLOT);
        asm volatile("" : "+s"(vso));
        bf16x8 vf[8];
#pragma unroll
        for (int i = 0; i < 8; ++i) {
          ldsp p = (ldsp)((va[i >> 1] ^ (unsigned)((i & 1) << 5)) + vso);
          vf[i] = lds_tr2(p, p + 4096);
        }
        SCHED();
#pragma unroll
        for (int qt = 0; qt < 3; ++qt) lacc[qt] = mfma(ones, pf[qt], j == 0 ? f32x4{0.f, 0.f, 0.f, 0.f} : lacc[qt]);
#pragma unroll
        for (int i = 0; i < 8; ++i)
#pragma unroll
          for (int qt = 0; qt < 3; ++qt) oacc[i][qt] = mfma(vf[i], pf[qt], j == 0 ? f32x4{0.f, 0.f, 0.f, 0.f} : oacc[i][qt]);
        SCHED();
        if (j == 0 && w != 3) K_ISSUE(3, 4);
        if (j == 1) V_ISSUE(3, 2);
        if (j == 2) WO_ISSUE2(1, 0);
      }
    }
    if (g == 0) {
#pragma unroll
      for (int qt = 0; qt < 3; ++qt) *(LDS_AS f32x2*)(sm + L_ST2 + ((w * 3 + qt) * 16 + c) * 8) = f32x2{mref[qt], lacc[qt][0]};
    }
    BAR(); STAMP(11);
  }
  int tz = threadIdx.x;
  asm volatile("" : "+v"(tz));
  const int l = tz & 63, c = l & 15, g = l >> 4, qq = c >> 2, pp = l & 3;
  {
    float inv[3], sc[3];
    bool differ = false;
#pragma unroll
    for (int qt = 0; qt < 3; ++qt) {
      f32x2 ml[4];
#pragma unroll
      for (int k = 0; k < 4; ++k) ml[k] = *(LDS_AS f32x2*)(sm + L_ST2 + ((h * 12 + 3 * k + qt) * 16 + c) * 8);
      const float m = fmaxf(fmaxf(ml[0][0], ml[1][0]), fmaxf(ml[2][0], ml[3][0]));
      float lt = 0.f;
#pragma unroll
      for (int k = 0; k < 4; ++k) lt += ml[k][1] * __builtin_amdgcn_exp2f(ml[k][0] - m);
      inv[qt] = __builtin_amdgcn_rcpf(lt);
      sc[qt] = __builtin_amdgcn_exp2f(mref[qt] - m);
      differ = differ || (mref[qt] != m);
    }
    if (__builtin_expect(__any(differ), 0)) {
#pragma unroll
      for (int qt = 0; qt < 3; ++qt)
#pragma unroll
        for (int i = 0; i < 8; ++i) oacc[i][qt] *= sc[qt];
    }
#pragma unroll
    for (int r = 1; r < 4; ++r) {
      const int wd = h * 4 + ((kq + r) & 3);
#pragma unroll
      for (int qt = 0; qt < 3; ++qt) *(LDS_AS bf16x8*)(sm + xoff(wd, r - 1, qt) + l * 16) = cvt8(oacc[2 * r][qt], oacc[2 * r + 1][qt]);
    }
    bf16x8 sel0, sel1;
#pragma unroll
    for (int e = 0; e < 4; ++e) {
      const __bf16 v = (4 * g + e == c) ? (__bf16)1.0f : (__bf16)0.0f;
      sel0[e] = v; sel0[4 + e] = (__bf16)0.0f; sel1[e] = (__bf16)0.0f; sel1[4 + e] = v;
    }
    BAR(); STAMP(17);
#pragma unroll
    for (int qt = 0; qt < 3; ++qt) {
#pragma unroll
      for (int r = 0; r < 3; ++r) {
        bf16x8 t = lds_rd128(sm + xoff(w, r, qt) + l * 16);
        oacc[0][qt] = mfma(sel0, t, oacc[0][qt]);
        oacc[1][qt] = mfma(sel1, t, oacc[1][qt]);
      }
    }
#pragma unroll
    for (int qt = 0; qt < 3; ++qt) *(LDS_AS bf16x8*)(sm + L_O2 + (w * 3 + qt) * 1024 + l * 16) = cvt8(oacc[0][qt] * inv[qt], oacc[1][qt] * inv[qt]);
  }
  waitv<0>();
  BAR(); STAMP(18);
  const unsigned ov0 = (unsigned)((l >> 3) * 1024 + (((l & 7) ^ (g << 1)) << 4));

  f32x4 cacc[4][3];
  {
    bf16x8 of[2][3];
    const int fC = 2 * (g & 1) + (qq >> 1);
    ldsp wra = sm + w * 4096 + (4 * g + qq) * 128 + 8 * pp;
    bf16x8 wf[2][4];
#define CSLOT(u) (((u) & 3) == 0 ? 3 : (((u) & 3) == 1 ? 0 : (((u) & 3) == 2 ? 1 : 4)))
#define C_READ(u)                                                                                                   \
  do {                                                                                                              \
    _Pragma("unroll") for (int t_ = 0; t_ < 4; ++t_) {                                                              \
      ldsp p_ = wra + CSLOT(u) * L_SLOT + ((t_ ^ fC) << 5);                                                         \
      wf[(u) & 1][t_] = lds_tr2(p_, p_ + 2048);                                                                     \
    }                                                                                                               \
    _Pragma("unroll") for (int q_ = 0; q_ < 3; ++q_)                                                                \
        of[(u) & 1][q_] = lds_rd128(sm + L_O2 + ((((u) + b) & 7) * 3 + q_) * 1024 + l * 16);                        \
  } while (0)
    WO_ISSUE(2, 1);
    C_READ(0);
#pragma unroll
    for (int u = 0; u < 8; ++u) {
      if (u + 3 < 8) WO_ISSUE(u + 3, CSLOT(u + 3));
      if (u + 1 < 8) {
        if (u + 1 >= 2) { const int last = (u + 3 < 8) ? (u + 3) : 7; const int rem = last - (u + 1);
          if (rem == 2) waitv<8>(); else if (rem == 1) waitv<4>(); else waitv<0>(); }
        C_READ(u + 1);
      }
      SCHED();
#pragma unroll
      for (int t = 0; t < 4; ++t)
#pragma unroll
        for (int qt = 0; qt < 3; ++qt) cacc[t][qt] = mfma(wf[u & 1][t], of[u & 1][qt], u == 0 ? f32x4{0.f, 0.f, 0.f, 0.f} : cacc[t][qt]);
      SCHED();
    }
  }
  STAMP(19);
  const int s16 = kvh * 2 + hp;
  {
    auto rs = __builtin_amdgcn_make_buffer_rsrc((void*)(ws + WS_PART + ((size_t)(b * 16 + s16) * 8 + w) * 6144), 0, 6144, 0x00020000);
#pragma unroll
    for (int a = 0; a < 2; ++a)
#pragma unroll
      for (int qt = 0; qt < 3; ++qt)
        __builtin_amdgcn_raw_buffer_store_b128(__builtin_bit_cast(u32x4, cvt8(cacc[2 * a][qt], cacc[2 * a + 1][qt])), rs, l * 16 + (a * 3 + qt) * 1024, 0, 16);
  }
  asm volatile("s_waitcnt vmcnt(0)" ::: "memory");
  if (l == 0) __hip_atomic_store(flags + (w * 16 + s16) * 8, tag, __ATOMIC_RELAXED, __HIP_MEMORY_SCOPE_AGENT);
  if (w < 3) {
    for (unsigned spins = 0; spins < (1u << 22); ++spins) {
      const unsigned long long f = __hip_atomic_load(flags + ((s16 >> 1) * 16 + (l & 15)) * 8, __ATOMIC_RELAXED, __HIP_MEMORY_SCOPE_AGENT);
      if (__all(f == tag)) break;
      __builtin_amdgcn_s_sleep(1);
    }
    __builtin_amdgcn_fence(__ATOMIC_ACQUIRE, "wavefront");
    if (s16 == 0 && w == 0 && l == 0) __hip_atomic_store(epoch, tag, __ATOMIC_RELAXED, __HIP_MEMORY_SCOPE_AGENT);
    STAMP(21);
    const int rem = s16 * 192 + w * 64 + l;
    auto rs = __builtin_amdgcn_make_buffer_rsrc((void*)(ws + WS_PART + (size_t)b * 16 * 49152), 0, 16 * 49152, 0x00020000);
    u32x4 v[16];
#pragma unroll
    for (int sl = 0; sl < 16; ++sl) v[sl] = __builtin_amdgcn_raw_buffer_load_b128(rs, rem * 16, sl * 49152, 16);
    f32x4 a0 = {0.f, 0.f, 0.f, 0.f}, a1 = {0.f, 0.f, 0.f, 0.f};
#pragma unroll
    for (int sl = 0; sl < 16; ++sl) {
#pragma unroll
      for (int e = 0; e < 2; ++e) {
        a0[2 * e] += __uint_as_float(v[sl][e] << 16); a0[2 * e + 1] += __uint_as_float(v[sl][e] & 0xffff0000u);
        a1[2 * e] += __uint_as_float(v[sl][2 + e] << 16); a1[2 * e + 1] += __uint_as_float(v[sl][2 + e] & 0xffff0000u);
      }
    }
    const int tq = rem >> 6, qt = tq % 3, wa = tq / 3;
    const int q = 16 * qt + c, n = 32 * wa + 4 * g;
    __bf16* o = out + ((size_t)b * SQ + q) * DM + n;
    *(bf16x4*)o = cvt4(a0);
    *(bf16x4*)(o + 16) = cvt4(a1);
  }
}

extern "C" void kernel_launch(void* const* d_in, const int* in_sizes, int n_in, void* d_out, int out_size, void* d_ws, size_t ws_size,
                              hipStream_t stream) {
  const __bf16* x = (const __bf16*)d_in[0];
  const __bf16* kc = (const __bf16*)d_in[1];
  const __bf16* vc = (const __bf16*)d_in[2];
  const __bf16* wq = (const __bf16*)d_in[3];
  const __bf16* wo = (const __bf16*)d_in[4];
  (void)hipFuncSetAttribute((const void*)gqa_fused, hipFuncAttributeMaxDynamicSharedMemorySize, LDS_BYTES);
  hipLaunchKernelGGL(gqa_fused, dim3(256), dim3(512), LDS_BYTES, stream, x, kc, vc, wq, wo, (char*)d_ws, (__bf16*)d_out);
}
```
